```python
import torch

def _softmax(x, axis=-1):
    return torch.softmax(x, dim=axis)

def _gelu(x):
    return torch.nn.functional.gelu(x, approximate='tanh')

def _silu(x):
    return torch.nn.functional.silu(x)

def _sigmoid(x):
    return torch.sigmoid(x)

def _layernorm(x, axis=-1, eps=1e-05):
    m = x.mean(dim=axis, keepdim=True)
    v = x.var(dim=axis, keepdim=True, unbiased=False)
    return (x - m) / torch.sqrt(v + eps)

def _scatter_add(tgt, idx, src, axis=0):
    return tgt.clone().index_add_(axis, idx, src)

def _segment_sum(src, seg, n):
    out = torch.zeros((n,) + src.shape[1:], dtype=src.dtype)
    return out.index_add_(0, seg, src)

def _pad(x, pw):
    flat = tuple((v for pair in reversed(pw) for v in pair))
    return torch.nn.functional.pad(x, flat)
kernel_name = 'interleave_layers__linear_attn_block__memory_aug_recurrent_mem_tr__prompt'

def setup_inputs():

    _w = lambda a: (lambda t: t.to(torch.bfloat16) if t.is_floating_point() else t)(torch.as_tensor(a))

    def _A_setup():
        B, S, D = (1, 2048, 512)
        return {'x': _w((lambda *s: torch.randn(*s, dtype=torch.bfloat16) * 0.05)(B, S, D)), 'wqkv': _w((lambda *s: torch.randn(*s, dtype=torch.bfloat16) * 0.05)(D, 3 * D)), 'wo': _w((lambda *s: torch.randn(*s, dtype=torch.bfloat16) * 0.05)(D, D))}
    _A_in = _A_setup()

    def _B_setup():
        B, S, Dm = (1, 2048, 512)
        H, Dh = (32, 128)
        M, seg = (8, 32)
        seg = min(seg, S)
        S = S // seg * seg
        return {'x': _w((lambda *s: torch.randn(*s, dtype=torch.bfloat16) * 0.05)(B, S, Dm)), 'mem0': _w((lambda *s: torch.randn(*s, dtype=torch.bfloat16) * 0.4)(B, M, Dm)), 'Wq': _w((lambda *s: torch.randn(*s, dtype=torch.bfloat16) * 3.2)(Dm, H, Dh) * Dm ** (-0.5)), 'Wk': _w((lambda *s: torch.randn(*s, dtype=torch.bfloat16) * 3.2)(Dm, H, Dh) * Dm ** (-0.5)), 'Wv': _w((lambda *s: torch.randn(*s, dtype=torch.bfloat16) * 0.4)(Dm, H, Dh) * Dm ** (-0.5)), 'Wo': _w((lambda *s: torch.randn(*s, dtype=torch.bfloat16) * 0.4)(H, Dh, Dm) * (H * Dh) ** (-0.5)), 'seg_len': seg}
    _B_in = _B_setup()
    return {'A_x': _A_in['x'], 'A_wqkv': _A_in['wqkv'], 'A_wo': _A_in['wo'], 'B_x': _B_in['x'], 'B_mem0': _B_in['mem0'], 'B_Wq': _B_in['Wq'], 'B_Wk': _B_in['Wk'], 'B_Wv': _B_in['Wv'], 'B_Wo': _B_in['Wo'], 'B_seg_len': _B_in['seg_len']}

def reference(A_x, A_wqkv, A_wo, B_x, B_mem0, B_Wq, B_Wk, B_Wv, B_Wo, B_seg_len):

    def _A_ref(x, wqkv, wo):
        B, S, D = x.shape
        H = 32
        Dh = D // H
        qkv = (lambda x, *d: x.permute(*d))((x @ wqkv).reshape(B, S, 3, H, Dh), 2, 0, 3, 1, 4)
        q, k, v = (qkv[0], qkv[1], qkv[2])
        phi = lambda t: torch.where(t > 0, t + 1, torch.exp(t)) + 1e-06
        pq, pk = (phi(q), phi(k))
        kv = torch.einsum('bhsd,bhse->bhde', pk, v)
        z = torch.einsum('bhsd->bhd', pk)
        num = torch.einsum('bhqd,bhde->bhqe', pq, kv)
        den = torch.einsum('bhqd,bhd->bhq', pq, z)[..., None] + 1e-06
        o = num / den
        return (lambda x, *d: x.permute(*d))(o, 0, 2, 1, 3).reshape(B, S, D) @ wo

    def _B_ref(x, mem0, Wq, Wk, Wv, Wo, seg_len):
        B, S, Dm = x.shape
        H, Dh = (Wq.shape[1], Wq.shape[2])
        M = mem0.shape[1]
        scale = Dh ** (-0.5)
        n_seg = S // seg_len
        L = M + seg_len
        idx = torch.arange(L)
        mask = (idx[:, None] < M) | (idx[:, None] >= idx[None, :])
        mem = mem0
        outs = []
        for s in range(n_seg):
            xs = x[:, s * seg_len:(s + 1) * seg_len, :].clone()
            h = torch.concatenate([mem, xs], axis=1)
            q = torch.einsum('btd,dhe->bhte', h, Wq)
            k = torch.einsum('btd,dhe->bhte', h, Wk)
            v = torch.einsum('btd,dhe->bhte', h, Wv)
            scores = torch.einsum('bhqe,bhke->bhqk', q, k) * scale
            scores = torch.where(mask[None, None], scores, -1000000000.0)
            p = _softmax(scores, axis=-1)
            o = torch.einsum('bhqk,bhke->bhqe', p, v)
            o = torch.einsum('bhte,hed->btd', o, Wo)
            mem = o[:, :M, :].clone()
            outs.append(o[:, M:, :])
        return torch.concatenate(outs, axis=1)
    x = A_x
    x = x + _A_ref(_layernorm(x, axis=-1), A_wqkv, A_wo)
    x = x + _B_ref(_layernorm(x, axis=-1), B_mem0, B_Wq, B_Wk, B_Wv, B_Wo, B_seg_len)
    x = x + _A_ref(_layernorm(x, axis=-1), A_wqkv, A_wo)
    x = x + _B_ref(_layernorm(x, axis=-1), B_mem0, B_Wq, B_Wk, B_Wv, B_Wo, B_seg_len)
    return x
```

```cpp
#include <hip/hip_runtime.h>
#include <hip/hip_bf16.h>

typedef __attribute__((ext_vector_type(8))) __bf16 bf8;
typedef __attribute__((ext_vector_type(4))) __bf16 bf4;
typedef __attribute__((ext_vector_type(2))) __bf16 bf2;
typedef __attribute__((ext_vector_type(4))) float f4;
typedef __attribute__((ext_vector_type(2))) float f2;
typedef __attribute__((ext_vector_type(4))) unsigned u4;
typedef __attribute__((ext_vector_type(2))) unsigned u2;
typedef __attribute__((ext_vector_type(4))) short s4v;
typedef unsigned long long u64;
#define LAS __attribute__((address_space(3)))
#define GAS __attribute__((address_space(1)))
#define DEV __device__ __forceinline__
#define RLX_AGENT __ATOMIC_RELAXED, __HIP_MEMORY_SCOPE_AGENT

#define CHAIN_ASM \
  "v_mbcnt_lo_u32_b32 v193, -1, 0\n" \
  "v_mbcnt_hi_u32_b32 v193, -1, v193\n" \
  "v_lshlrev_b32 v192, 2, v193\n" \
  "v_add_u32 v192, 162816, v192\n" \
  "ds_read_b32 v192, v192\n" \
  "s_waitcnt lgkmcnt(0)\n" \
  "v_readlane_b32 s4, v192, 0\n" \
  "v_readlane_b32 s5, v192, 1\n" \
  "v_readlane_b32 s6, v192, 2\n" \
  "v_readlane_b32 s7, v192, 3\n" \
  "v_readlane_b32 s8, v192, 4\n" \
  "v_readlane_b32 s9, v192, 5\n" \
  "v_readlane_b32 s10, v192, 6\n" \
  "v_readlane_b32 s11, v192, 7\n" \
  "v_readlane_b32 s12, v192, 8\n" \
  "v_readlane_b32 s13, v192, 9\n" \
  "v_readlane_b32 s14, v192, 10\n" \
  "v_readlane_b32 s15, v192, 11\n" \
  "v_readlane_b32 s16, v192, 12\n" \
  "v_readlane_b32 s17, v192, 13\n" \
  "v_readlane_b32 s18, v192, 14\n" \
  "v_readlane_b32 s19, v192, 15\n" \
  "v_readlane_b32 s20, v192, 16\n" \
  "v_readlane_b32 s21, v192, 17\n" \
  "v_readlane_b32 s22, v192, 18\n" \
  "v_readlane_b32 s23, v192, 19\n" \
  "v_readlane_b32 s24, v192, 20\n" \
  "v_readlane_b32 s25, v192, 21\n" \
  "v_readlane_b32 s34, v192, 22\n" \
  "v_readlane_b32 s35, v192, 23\n" \
  "v_readlane_b32 s36, v192, 24\n" \
  "v_readlane_b32 s37, v192, 25\n" \
  "v_readlane_b32 s30, v192, 26\n" \
  "v_readlane_b32 s31, v192, 27\n" \
  "v_readlane_b32 s29, v192, 28\n" \
  "v_readlane_b32 s64, v192, 29\n" \
  "v_readlane_b32 s82, v192, 30\n" \
  "v_readlane_b32 s83, v192, 31\n" \
  "s_nop 7\n" \
  "s_nop 1\n" \
  "s_mov_b64 s[40:41], s[10:11]\n" \
  "s_mov_b64 s[44:45], s[12:13]\n" \
  "s_mov_b32 s57, s25\n" \
  "s_bfe_u32 s96, s29, 65537\n" \
  "s_mov_b32 s98, 0\n" \
  "s_mov_b32 s59, s23\n" \
  "s_mov_b32 s60, s24\n" \
  "s_mov_b32 s80, s60\n" \
  "s_mov_b32 s60, 172\n" \
  "s_mul_i32 s67, s60, 12\n" \
  "s_mov_b32 s88, 2260\n" \
  "s_mov_b32 s56, %[w]\n" \
  "v_mov_b32 v239, v193\n" \
  "v_lshlrev_b32 v241, 4, v239\n" \
  "s_mov_b32 s61, 100000\n" \
  "s_mov_b32 s62, 0x3e0293ee\n" \
  "s_xor_b32 s28, s62, 0x80000000\n" \
  "s_mov_b32 s68, 0x00ff00ff\n" \
  "s_mov_b32 s69, 0x00ff00ff\n" \
  "s_barrier\n" \
  "v_lshrrev_b32 v187, 2, v239\n" \
  "v_bfe_u32 v186, v187, 3, 1\n" \
  "v_lshlrev_b32 v176, 4, v186\n" \
  "v_bfe_u32 v186, v187, 2, 1\n" \
  "v_lshl_add_u32 v176, v186, 2, v176\n" \
  "v_bfe_u32 v186, v187, 1, 1\n" \
  "v_lshl_add_u32 v176, v186, 3, v176\n" \
  "v_and_b32 v186, 1, v187\n" \
  "v_add_u32 v176, v176, v186\n" \
  "v_lshlrev_b32 v176, 13, v176\n" \
  "v_and_b32 v186, 3, v239\n" \
  "v_lshl_add_u32 v176, v186, 4, v176\n" \
  "v_lshlrev_b32 v179, 10, v187\n" \
  "v_lshl_add_u32 v179, v186, 4, v179\n" \
  "v_lshrrev_b32 v187, 4, v239\n" \
  "v_bfe_u32 v186, v187, 3, 1\n" \
  "v_lshlrev_b32 v177, 4, v186\n" \
  "v_bfe_u32 v186, v187, 2, 1\n" \
  "v_lshl_add_u32 v177, v186, 2, v177\n" \
  "v_bfe_u32 v186, v187, 1, 1\n" \
  "v_lshl_add_u32 v177, v186, 3, v177\n" \
  "v_and_b32 v186, 1, v187\n" \
  "v_add_u32 v177, v177, v186\n" \
  "v_lshlrev_b32 v177, 13, v177\n" \
  "v_and_b32 v186, 15, v239\n" \
  "v_xor_b32 v186, v186, v187\n" \
  "v_lshl_add_u32 v177, v186, 4, v177\n" \
  "s_mul_i32 s84, s56, 12288\n" \
  "v_lshlrev_b32 v186, 2, v187\n" \
  "v_bfe_u32 v178, v239, 2, 2\n" \
  "v_add_u32 v186, v186, v178\n" \
  "v_lshlrev_b32 v186, 6, v186\n" \
  "v_and_b32 v180, 3, v239\n" \
  "v_lshl_add_u32 v186, v180, 4, v186\n" \
  "v_lshl_add_u32 v180, v187, 8, v186\n" \
  "v_add_u32 v178, s84, v186\n" \
  "s_add_u32 s72, s84, 98304\n" \
  "v_add_u32 v180, s72, v180\n" \
  "v_and_b32 v186, 15, v239\n" \
  "v_xor_b32 v181, v187, v186\n" \
  "v_lshlrev_b32 v181, 4, v181\n" \
  "v_lshl_add_u32 v181, v186, 8, v181\n" \
  "v_add_u32 v181, 8192, v181\n" \
  "v_xor_b32 v182, 64, v181\n" \
  "v_xor_b32 v183, 128, v181\n" \
  "v_xor_b32 v184, 192, v181\n" \
  "s_lshl_b32 s72, s56, 6\n" \
  "s_add_u32 s4, s4, s72\n" \
  "s_addc_u32 s5, s5, 0\n" \
  "s_add_u32 s82, s82, s72\n" \
  "s_addc_u32 s83, s83, 0\n" \
  "s_add_u32 s8, s8, s72\n" \
  "s_addc_u32 s9, s9, 0\n" \
  "s_lshr_b32 s72, s56, 1\n" \
  "s_lshl_b32 s72, s72, 5\n" \
  "s_and_b32 s73, s56, 1\n" \
  "s_lshl_b32 s73, s73, 1\n" \
  "s_add_u32 s72, s72, s73\n" \
  "s_lshl_b32 s72, s72, 13\n" \
  "s_add_u32 s6, s6, s72\n" \
  "s_addc_u32 s7, s7, 0\n" \
  "s_add_u32 m0, s84, 98304\n" \
  "s_nop 0\n" \
  "global_load_lds_dwordx4 v179, s[8:9]\n" \
  "s_add_u32 s8, s8, 16384\n" \
  "s_addc_u32 s9, s9, 0\n" \
  "s_add_u32 m0, s84, 99328\n" \
  "s_nop 0\n" \
  "global_load_lds_dwordx4 v179, s[8:9]\n" \
  "s_add_u32 s8, s8, 16384\n" \
  "s_addc_u32 s9, s9, 0\n" \
  "s_add_u32 m0, s84, 100352\n" \
  "s_nop 0\n" \
  "global_load_lds_dwordx4 v179, s[8:9]\n" \
  "s_add_u32 s8, s8, 16384\n" \
  "s_addc_u32 s9, s9, 0\n" \
  "s_add_u32 m0, s84, 101376\n" \
  "s_nop 0\n" \
  "global_load_lds_dwordx4 v179, s[8:9]\n" \
  "s_add_u32 s8, s8, 16384\n" \
  "s_addc_u32 s9, s9, 0\n" \
  "s_add_u32 m0, s84, 102400\n" \
  "s_nop 0\n" \
  "global_load_lds_dwordx4 v179, s[8:9]\n" \
  "s_add_u32 s8, s8, 16384\n" \
  "s_addc_u32 s9, s9, 0\n" \
  "s_add_u32 m0, s84, 103424\n" \
  "s_nop 0\n" \
  "global_load_lds_dwordx4 v179, s[8:9]\n" \
  "s_add_u32 s8, s8, 16384\n" \
  "s_addc_u32 s9, s9, 0\n" \
  "s_add_u32 m0, s84, 104448\n" \
  "s_nop 0\n" \
  "global_load_lds_dwordx4 v179, s[8:9]\n" \
  "s_add_u32 s8, s8, 16384\n" \
  "s_addc_u32 s9, s9, 0\n" \
  "s_add_u32 m0, s84, 105472\n" \
  "s_nop 0\n" \
  "global_load_lds_dwordx4 v179, s[8:9]\n" \
  "s_add_u32 s8, s8, 16384\n" \
  "s_addc_u32 s9, s9, 0\n" \
  "s_add_u32 m0, s84, 0\n" \
  "s_nop 0\n" \
  "global_load_lds_dwordx4 v176, s[4:5]\n" \
  "s_add_u32 s4, s4, 16384\n" \
  "s_addc_u32 s5, s5, 0\n" \
  "s_add_u32 m0, s84, 1024\n" \
  "s_nop 0\n" \
  "global_load_lds_dwordx4 v176, s[4:5]\n" \
  "s_add_u32 s4, s4, 245760\n" \
  "s_addc_u32 s5, s5, 0\n" \
  "s_add_u32 m0, s84, 2048\n" \
  "s_nop 0\n" \
  "global_load_lds_dwordx4 v176, s[4:5]\n" \
  "s_add_u32 s4, s4, 16384\n" \
  "s_addc_u32 s5, s5, 0\n" \
  "s_add_u32 m0, s84, 3072\n" \
  "s_nop 0\n" \
  "global_load_lds_dwordx4 v176, s[4:5]\n" \
  "s_add_u32 s4, s4, 245760\n" \
  "s_addc_u32 s5, s5, 0\n" \
  "s_add_u32 m0, s84, 4096\n" \
  "s_nop 0\n" \
  "global_load_lds_dwordx4 v176, s[82:83]\n" \
  "s_add_u32 s82, s82, 16384\n" \
  "s_addc_u32 s83, s83, 0\n" \
  "s_add_u32 m0, s84, 5120\n" \
  "s_nop 0\n" \
  "global_load_lds_dwordx4 v176, s[82:83]\n" \
  "s_add_u32 s82, s82, 245760\n" \
  "s_addc_u32 s83, s83, 0\n" \
  "s_add_u32 m0, s84, 6144\n" \
  "s_nop 0\n" \
  "global_load_lds_dwordx4 v176, s[82:83]\n" \
  "s_add_u32 s82, s82, 16384\n" \
  "s_addc_u32 s83, s83, 0\n" \
  "s_add_u32 m0, s84, 7168\n" \
  "s_nop 0\n" \
  "global_load_lds_dwordx4 v176, s[82:83]\n" \
  "s_add_u32 s82, s82, 245760\n" \
  "s_addc_u32 s83, s83, 0\n" \
  "s_add_u32 m0, s84, 8192\n" \
  "s_nop 0\n" \
  "global_load_lds_dwordx4 v177, s[6:7]\n" \
  "s_add_u32 s6, s6, 32768\n" \
  "s_addc_u32 s7, s7, 0\n" \
  "v_xor_b32 v186, 64, v177\n" \
  "s_add_u32 m0, s84, 9216\n" \
  "s_nop 0\n" \
  "global_load_lds_dwordx4 v186, s[6:7]\n" \
  "s_add_u32 s6, s6, 98304\n" \
  "s_addc_u32 s7, s7, 0\n" \
  "v_xor_b32 v186, 128, v177\n" \
  "s_add_u32 m0, s84, 10240\n" \
  "s_nop 0\n" \
  "global_load_lds_dwordx4 v186, s[6:7]\n" \
  "s_add_u32 s6, s6, 32768\n" \
  "s_addc_u32 s7, s7, 0\n" \
  "v_xor_b32 v186, 192, v177\n" \
  "s_add_u32 m0, s84, 11264\n" \
  "s_nop 0\n" \
  "global_load_lds_dwordx4 v186, s[6:7]\n" \
  "s_add_u32 s6, s6, 360448\n" \
  "s_addc_u32 s7, s7, 0\n" \
  "s_add_u32 m0, s84, 49152\n" \
  "s_nop 0\n" \
  "global_load_lds_dwordx4 v176, s[4:5]\n" \
  "s_add_u32 s4, s4, 16384\n" \
  "s_addc_u32 s5, s5, 0\n" \
  "s_add_u32 m0, s84, 50176\n" \
  "s_nop 0\n" \
  "global_load_lds_dwordx4 v176, s[4:5]\n" \
  "s_add_u32 s4, s4, 245760\n" \
  "s_addc_u32 s5, s5, 0\n" \
  "s_add_u32 m0, s84, 51200\n" \
  "s_nop 0\n" \
  "global_load_lds_dwordx4 v176, s[4:5]\n" \
  "s_add_u32 s4, s4, 16384\n" \
  "s_addc_u32 s5, s5, 0\n" \
  "s_add_u32 m0, s84, 52224\n" \
  "s_nop 0\n" \
  "global_load_lds_dwordx4 v176, s[4:5]\n" \
  "s_add_u32 s4, s4, 245760\n" \
  "s_addc_u32 s5, s5, 0\n" \
  "s_add_u32 m0, s84, 53248\n" \
  "s_nop 0\n" \
  "global_load_lds_dwordx4 v176, s[82:83]\n" \
  "s_add_u32 s82, s82, 16384\n" \
  "s_addc_u32 s83, s83, 0\n" \
  "s_add_u32 m0, s84, 54272\n" \
  "s_nop 0\n" \
  "global_load_lds_dwordx4 v176, s[82:83]\n" \
  "s_add_u32 s82, s82, 245760\n" \
  "s_addc_u32 s83, s83, 0\n" \
  "s_add_u32 m0, s84, 55296\n" \
  "s_nop 0\n" \
  "global_load_lds_dwordx4 v176, s[82:83]\n" \
  "s_add_u32 s82, s82, 16384\n" \
  "s_addc_u32 s83, s83, 0\n" \
  "s_add_u32 m0, s84, 56320\n" \
  "s_nop 0\n" \
  "global_load_lds_dwordx4 v176, s[82:83]\n" \
  "s_add_u32 s82, s82, 245760\n" \
  "s_addc_u32 s83, s83, 0\n" \
  "s_add_u32 m0, s84, 57344\n" \
  "s_nop 0\n" \
  "global_load_lds_dwordx4 v177, s[6:7]\n" \
  "s_add_u32 s6, s6, 32768\n" \
  "s_addc_u32 s7, s7, 0\n" \
  "v_xor_b32 v186, 64, v177\n" \
  "s_add_u32 m0, s84, 58368\n" \
  "s_nop 0\n" \
  "global_load_lds_dwordx4 v186, s[6:7]\n" \
  "s_add_u32 s6, s6, 98304\n" \
  "s_addc_u32 s7, s7, 0\n" \
  "v_xor_b32 v186, 128, v177\n" \
  "s_add_u32 m0, s84, 59392\n" \
  "s_nop 0\n" \
  "global_load_lds_dwordx4 v186, s[6:7]\n" \
  "s_add_u32 s6, s6, 32768\n" \
  "s_addc_u32 s7, s7, 0\n" \
  "v_xor_b32 v186, 192, v177\n" \
  "s_add_u32 m0, s84, 60416\n" \
  "s_nop 0\n" \
  "global_load_lds_dwordx4 v186, s[6:7]\n" \
  "s_add_u32 s6, s6, 360448\n" \
  "s_addc_u32 s7, s7, 0\n" \
  "s_waitcnt vmcnt(24)\n" \
  "ds_read_b64_tr_b16 v[192:193], v180\n" \
  "ds_read_b64_tr_b16 v[194:195], v180 offset:256\n" \
  "ds_read_b64_tr_b16 v[208:209], v180 offset:8\n" \
  "ds_read_b64_tr_b16 v[210:211], v180 offset:264\n" \
  "ds_read_b64_tr_b16 v[196:197], v180 offset:2048\n" \
  "ds_read_b64_tr_b16 v[198:199], v180 offset:2304\n" \
  "ds_read_b64_tr_b16 v[212:213], v180 offset:2056\n" \
  "ds_read_b64_tr_b16 v[214:215], v180 offset:2312\n" \
  "s_waitcnt lgkmcnt(0)\n" \
  "ds_read_b64_tr_b16 v[200:201], v180 offset:4096\n" \
  "ds_read_b64_tr_b16 v[202:203], v180 offset:4352\n" \
  "ds_read_b64_tr_b16 v[216:217], v180 offset:4104\n" \
  "ds_read_b64_tr_b16 v[218:219], v180 offset:4360\n" \
  "ds_read_b64_tr_b16 v[204:205], v180 offset:6144\n" \
  "ds_read_b64_tr_b16 v[206:207], v180 offset:6400\n" \
  "ds_read_b64_tr_b16 v[220:221], v180 offset:6152\n" \
  "ds_read_b64_tr_b16 v[222:223], v180 offset:6408\n" \
  "s_waitcnt lgkmcnt(0)\n" \
  "s_waitcnt vmcnt(12)\n" \
  "s_barrier\n" \
  "s_add_u32 m0, s84, 98304\n" \
  "s_nop 0\n" \
  "global_load_lds_dwordx4 v176, s[4:5]\n" \
  "s_add_u32 s4, s4, 16384\n" \
  "s_addc_u32 s5, s5, 0\n" \
  "s_add_u32 m0, s84, 99328\n" \
  "s_nop 0\n" \
  "global_load_lds_dwordx4 v176, s[4:5]\n" \
  "s_add_u32 s4, s4, 245760\n" \
  "s_addc_u32 s5, s5, 0\n" \
  "s_add_u32 m0, s84, 100352\n" \
  "s_nop 0\n" \
  "global_load_lds_dwordx4 v176, s[4:5]\n" \
  "s_add_u32 s4, s4, 16384\n" \
  "s_addc_u32 s5, s5, 0\n" \
  "s_add_u32 m0, s84, 101376\n" \
  "s_nop 0\n" \
  "global_load_lds_dwordx4 v176, s[4:5]\n" \
  "s_add_u32 s4, s4, 245760\n" \
  "s_addc_u32 s5, s5, 0\n" \
  "s_add_u32 m0, s84, 102400\n" \
  "s_nop 0\n" \
  "global_load_lds_dwordx4 v176, s[82:83]\n" \
  "s_add_u32 s82, s82, 16384\n" \
  "s_addc_u32 s83, s83, 0\n" \
  "s_add_u32 m0, s84, 103424\n" \
  "s_nop 0\n" \
  "global_load_lds_dwordx4 v176, s[82:83]\n" \
  "s_add_u32 s82, s82, 245760\n" \
  "s_addc_u32 s83, s83, 0\n" \
  "s_add_u32 m0, s84, 104448\n" \
  "s_nop 0\n" \
  "global_load_lds_dwordx4 v176, s[82:83]\n" \
  "s_add_u32 s82, s82, 16384\n" \
  "s_addc_u32 s83, s83, 0\n" \
  "s_add_u32 m0, s84, 105472\n" \
  "s_nop 0\n" \
  "global_load_lds_dwordx4 v176, s[82:83]\n" \
  "s_add_u32 s82, s82, 245760\n" \
  "s_addc_u32 s83, s83, 0\n" \
  "s_add_u32 m0, s84, 106496\n" \
  "s_nop 0\n" \
  "global_load_lds_dwordx4 v177, s[6:7]\n" \
  "s_add_u32 s6, s6, 32768\n" \
  "s_addc_u32 s7, s7, 0\n" \
  "v_xor_b32 v186, 64, v177\n" \
  "s_add_u32 m0, s84, 107520\n" \
  "s_nop 0\n" \
  "global_load_lds_dwordx4 v186, s[6:7]\n" \
  "s_add_u32 s6, s6, 98304\n" \
  "s_addc_u32 s7, s7, 0\n" \
  "v_xor_b32 v186, 128, v177\n" \
  "s_add_u32 m0, s84, 108544\n" \
  "s_nop 0\n" \
  "global_load_lds_dwordx4 v186, s[6:7]\n" \
  "s_add_u32 s6, s6, 32768\n" \
  "s_addc_u32 s7, s7, 0\n" \
  "v_xor_b32 v186, 192, v177\n" \
  "s_add_u32 m0, s84, 109568\n" \
  "s_nop 0\n" \
  "global_load_lds_dwordx4 v186, s[6:7]\n" \
  "s_add_u32 s6, s6, 360448\n" \
  "s_addc_u32 s7, s7, 0\n" \
  "ds_read_b128 v[128:131], v181\n" \
  "ds_read_b128 v[132:135], v182\n" \
  "ds_read_b128 v[136:139], v183\n" \
  "ds_read_b128 v[140:143], v184\n" \
  "ds_read_b128 v[144:147], v181 offset:12288\n" \
  "ds_read_b128 v[148:151], v182 offset:12288\n" \
  "ds_read_b128 v[152:155], v183 offset:12288\n" \
  "ds_read_b128 v[156:159], v184 offset:12288\n" \
  "ds_read_b64_tr_b16 a[0:1], v178\n" \
  "ds_read_b64_tr_b16 a[2:3], v178 offset:1024\n" \
  "ds_read_b64_tr_b16 a[4:5], v178 offset:2048\n" \
  "ds_read_b64_tr_b16 a[6:7], v178 offset:3072\n" \
  "ds_read_b64_tr_b16 a[64:65], v178 offset:8\n" \
  "ds_read_b64_tr_b16 a[66:67], v178 offset:1032\n" \
  "s_waitcnt lgkmcnt(6)\n" \
  "ds_read_b64_tr_b16 a[68:69], v178 offset:2056\n" \
  "ds_read_b64_tr_b16 a[70:71], v178 offset:3080\n" \
  "s_waitcnt lgkmcnt(8)\n" \
  "v_mfma_f32_16x16x32_bf16 v[160:163], v[128:131], v[192:195], 0\n" \
  "v_mfma_f32_16x16x32_bf16 v[164:167], v[128:131], v[208:211], 0\n" \
  "v_mfma_f32_16x16x32_bf16 v[160:163], v[132:135], v[196:199], v[160:163]\n" \
  "v_mfma_f32_16x16x32_bf16 v[164:167], v[132:135], v[212:215], v[164:167]\n" \
  "v_mfma_f32_16x16x32_bf16 v[160:163], v[136:139], v[200:203], v[160:163]\n" \
  "v_mfma_f32_16x16x32_bf16 v[164:167], v[136:139], v[216:219], v[164:167]\n" \
  "v_mfma_f32_16x16x32_bf16 v[160:163], v[140:143], v[204:207], v[160:163]\n" \
  "v_mfma_f32_16x16x32_bf16 v[164:167], v[140:143], v[220:223], v[164:167]\n" \
  "v_mfma_f32_16x16x32_bf16 v[168:171], v[144:147], v[192:195], 0\n" \
  "v_mfma_f32_16x16x32_bf16 v[172:175], v[144:147], v[208:211], 0\n" \
  "v_mfma_f32_16x16x32_bf16 v[168:171], v[148:151], v[196:199], v[168:171]\n" \
  "v_mfma_f32_16x16x32_bf16 v[172:175], v[148:151], v[212:215], v[172:175]\n" \
  "v_mfma_f32_16x16x32_bf16 v[168:171], v[152:155], v[200:203], v[168:171]\n" \
  "v_mfma_f32_16x16x32_bf16 v[172:175], v[152:155], v[216:219], v[172:175]\n" \
  "v_mfma_f32_16x16x32_bf16 v[168:171], v[156:159], v[204:207], v[168:171]\n" \
  "v_mfma_f32_16x16x32_bf16 v[172:175], v[156:159], v[220:223], v[172:175]\n" \
  "v_cvt_pk_bf16_f32 v0, v160, v161\n" \
  "v_cvt_pk_bf16_f32 v1, v162, v163\n" \
  "v_cvt_pk_bf16_f32 v64, v164, v165\n" \
  "v_cvt_pk_bf16_f32 v65, v166, v167\n" \
  "v_cvt_pk_bf16_f32 v2, v168, v169\n" \
  "v_cvt_pk_bf16_f32 v3, v170, v171\n" \
  "s_nop 1\n" \
  "v_cvt_pk_bf16_f32 v66, v172, v173\n" \
  "v_cvt_pk_bf16_f32 v67, v174, v175\n" \
  "s_waitcnt lgkmcnt(0)\n" \
  "ds_read_b128 v[128:131], v181 offset:24576\n" \
  "ds_read_b128 v[132:135], v182 offset:24576\n" \
  "ds_read_b128 v[136:139], v183 offset:24576\n" \
  "ds_read_b128 v[140:143], v184 offset:24576\n" \
  "ds_read_b128 v[144:147], v181 offset:36864\n" \
  "ds_read_b128 v[148:151], v182 offset:36864\n" \
  "ds_read_b128 v[152:155], v183 offset:36864\n" \
  "ds_read_b128 v[156:159], v184 offset:36864\n" \
  "ds_read_b64_tr_b16 a[128:129], v178 offset:4096\n" \
  "ds_read_b64_tr_b16 a[130:131], v178 offset:5120\n" \
  "ds_read_b64_tr_b16 a[132:133], v178 offset:6144\n" \
  "ds_read_b64_tr_b16 a[134:135], v178 offset:7168\n" \
  "ds_read_b64_tr_b16 a[192:193], v178 offset:4104\n" \
  "ds_read_b64_tr_b16 a[194:195], v178 offset:5128\n" \
  "s_waitcnt lgkmcnt(6)\n" \
  "ds_read_b64_tr_b16 a[196:197], v178 offset:6152\n" \
  "ds_read_b64_tr_b16 a[198:199], v178 offset:7176\n" \
  "s_waitcnt lgkmcnt(8)\n" \
  "v_mfma_f32_16x16x32_bf16 v[160:163], v[128:131], v[192:195], 0\n" \
  "v_mfma_f32_16x16x32_bf16 v[164:167], v[128:131], v[208:211], 0\n" \
  "v_mfma_f32_16x16x32_bf16 v[160:163], v[132:135], v[196:199], v[160:163]\n" \
  "v_mfma_f32_16x16x32_bf16 v[164:167], v[132:135], v[212:215], v[164:167]\n" \
  "v_mfma_f32_16x16x32_bf16 v[160:163], v[136:139], v[200:203], v[160:163]\n" \
  "v_mfma_f32_16x16x32_bf16 v[164:167], v[136:139], v[216:219], v[164:167]\n" \
  "v_mfma_f32_16x16x32_bf16 v[160:163], v[140:143], v[204:207], v[160:163]\n" \
  "v_mfma_f32_16x16x32_bf16 v[164:167], v[140:143], v[220:223], v[164:167]\n" \
  "v_mfma_f32_16x16x32_bf16 v[168:171], v[144:147], v[192:195], 0\n" \
  "v_mfma_f32_16x16x32_bf16 v[172:175], v[144:147], v[208:211], 0\n" \
  "v_mfma_f32_16x16x32_bf16 v[168:171], v[148:151], v[196:199], v[168:171]\n" \
  "v_mfma_f32_16x16x32_bf16 v[172:175], v[148:151], v[212:215], v[172:175]\n" \
  "v_mfma_f32_16x16x32_bf16 v[168:171], v[152:155], v[200:203], v[168:171]\n" \
  "v_mfma_f32_16x16x32_bf16 v[172:175], v[152:155], v[216:219], v[172:175]\n" \
  "v_mfma_f32_16x16x32_bf16 v[168:171], v[156:159], v[204:207], v[168:171]\n" \
  "v_mfma_f32_16x16x32_bf16 v[172:175], v[156:159], v[220:223], v[172:175]\n" \
  "v_cvt_pk_bf16_f32 v4, v160, v161\n" \
  "v_cvt_pk_bf16_f32 v5, v162, v163\n" \
  "v_cvt_pk_bf16_f32 v68, v164, v165\n" \
  "v_cvt_pk_bf16_f32 v69, v166, v167\n" \
  "v_cvt_pk_bf16_f32 v6, v168, v169\n" \
  "v_cvt_pk_bf16_f32 v7, v170, v171\n" \
  "s_nop 1\n" \
  "v_cvt_pk_bf16_f32 v70, v172, v173\n" \
  "v_cvt_pk_bf16_f32 v71, v174, v175\n" \
  "s_waitcnt lgkmcnt(0)\n" \
  "s_waitcnt vmcnt(12)\n" \
  "s_barrier\n" \
  "s_add_u32 m0, s84, 0\n" \
  "s_nop 0\n" \
  "global_load_lds_dwordx4 v176, s[4:5]\n" \
  "s_add_u32 s4, s4, 16384\n" \
  "s_addc_u32 s5, s5, 0\n" \
  "s_add_u32 m0, s84, 1024\n" \
  "s_nop 0\n" \
  "global_load_lds_dwordx4 v176, s[4:5]\n" \
  "s_add_u32 s4, s4, 245760\n" \
  "s_addc_u32 s5, s5, 0\n" \
  "s_add_u32 m0, s84, 2048\n" \
  "s_nop 0\n" \
  "global_load_lds_dwordx4 v176, s[4:5]\n" \
  "s_add_u32 s4, s4, 16384\n" \
  "s_addc_u32 s5, s5, 0\n" \
  "s_add_u32 m0, s84, 3072\n" \
  "s_nop 0\n" \
  "global_load_lds_dwordx4 v176, s[4:5]\n" \
  "s_add_u32 s4, s4, 245760\n" \
  "s_addc_u32 s5, s5, 0\n" \
  "s_add_u32 m0, s84, 4096\n" \
  "s_nop 0\n" \
  "global_load_lds_dwordx4 v176, s[82:83]\n" \
  "s_add_u32 s82, s82, 16384\n" \
  "s_addc_u32 s83, s83, 0\n" \
  "s_add_u32 m0, s84, 5120\n" \
  "s_nop 0\n" \
  "global_load_lds_dwordx4 v176, s[82:83]\n" \
  "s_add_u32 s82, s82, 245760\n" \
  "s_addc_u32 s83, s83, 0\n" \
  "s_add_u32 m0, s84, 6144\n" \
  "s_nop 0\n" \
  "global_load_lds_dwordx4 v176, s[82:83]\n" \
  "s_add_u32 s82, s82, 16384\n" \
  "s_addc_u32 s83, s83, 0\n" \
  "s_add_u32 m0, s84, 7168\n" \
  "s_nop 0\n" \
  "global_load_lds_dwordx4 v176, s[82:83]\n" \
  "s_add_u32 s82, s82, 245760\n" \
  "s_addc_u32 s83, s83, 0\n" \
  "s_add_u32 m0, s84, 8192\n" \
  "s_nop 0\n" \
  "global_load_lds_dwordx4 v177, s[6:7]\n" \
  "s_add_u32 s6, s6, 32768\n" \
  "s_addc_u32 s7, s7, 0\n" \
  "v_xor_b32 v186, 64, v177\n" \
  "s_add_u32 m0, s84, 9216\n" \
  "s_nop 0\n" \
  "global_load_lds_dwordx4 v186, s[6:7]\n" \
  "s_add_u32 s6, s6, 98304\n" \
  "s_addc_u32 s7, s7, 0\n" \
  "v_xor_b32 v186, 128, v177\n" \
  "s_add_u32 m0, s84, 10240\n" \
  "s_nop 0\n" \
  "global_load_lds_dwordx4 v186, s[6:7]\n" \
  "s_add_u32 s6, s6, 32768\n" \
  "s_addc_u32 s7, s7, 0\n" \
  "v_xor_b32 v186, 192, v177\n" \
  "s_add_u32 m0, s84, 11264\n" \
  "s_nop 0\n" \
  "global_load_lds_dwordx4 v186, s[6:7]\n" \
  "s_add_u32 s6, s6, 360448\n" \
  "s_addc_u32 s7, s7, 0\n" \
  "v_add_u32 v188, 49152, v181\n" \
  "v_add_u32 v189, 49152, v182\n" \
  "v_add_u32 v190, 49152, v183\n" \
  "v_add_u32 v191, 49152, v184\n" \
  "v_add_u32 v187, 49152, v178\n" \
  "ds_read_b128 v[128:131], v188\n" \
  "ds_read_b128 v[132:135], v189\n" \
  "ds_read_b128 v[136:139], v190\n" \
  "ds_read_b128 v[140:143], v191\n" \
  "ds_read_b128 v[144:147], v188 offset:12288\n" \
  "ds_read_b128 v[148:151], v189 offset:12288\n" \
  "ds_read_b128 v[152:155], v190 offset:12288\n" \
  "ds_read_b128 v[156:159], v191 offset:12288\n" \
  "ds_read_b64_tr_b16 a[8:9], v187\n" \
  "ds_read_b64_tr_b16 a[10:11], v187 offset:1024\n" \
  "ds_read_b64_tr_b16 a[12:13], v187 offset:2048\n" \
  "ds_read_b64_tr_b16 a[14:15], v187 offset:3072\n" \
  "ds_read_b64_tr_b16 a[72:73], v187 offset:8\n" \
  "ds_read_b64_tr_b16 a[74:75], v187 offset:1032\n" \
  "s_waitcnt lgkmcnt(6)\n" \
  "ds_read_b64_tr_b16 a[76:77], v187 offset:2056\n" \
  "ds_read_b64_tr_b16 a[78:79], v187 offset:3080\n" \
  "s_waitcnt lgkmcnt(8)\n" \
  "v_mfma_f32_16x16x32_bf16 v[160:163], v[128:131], v[192:195], 0\n" \
  "v_mfma_f32_16x16x32_bf16 v[164:167], v[128:131], v[208:211], 0\n" \
  "v_mfma_f32_16x16x32_bf16 v[160:163], v[132:135], v[196:199], v[160:163]\n" \
  "v_mfma_f32_16x16x32_bf16 v[164:167], v[132:135], v[212:215], v[164:167]\n" \
  "v_mfma_f32_16x16x32_bf16 v[160:163], v[136:139], v[200:203], v[160:163]\n" \
  "v_mfma_f32_16x16x32_bf16 v[164:167], v[136:139], v[216:219], v[164:167]\n" \
  "v_mfma_f32_16x16x32_bf16 v[160:163], v[140:143], v[204:207], v[160:163]\n" \
  "v_mfma_f32_16x16x32_bf16 v[164:167], v[140:143], v[220:223], v[164:167]\n" \
  "v_mfma_f32_16x16x32_bf16 v[168:171], v[144:147], v[192:195], 0\n" \
  "v_mfma_f32_16x16x32_bf16 v[172:175], v[144:147], v[208:211], 0\n" \
  "v_mfma_f32_16x16x32_bf16 v[168:171], v[148:151], v[196:199], v[168:171]\n" \
  "v_mfma_f32_16x16x32_bf16 v[172:175], v[148:151], v[212:215], v[172:175]\n" \
  "v_mfma_f32_16x16x32_bf16 v[168:171], v[152:155], v[200:203], v[168:171]\n" \
  "v_mfma_f32_16x16x32_bf16 v[172:175], v[152:155], v[216:219], v[172:175]\n" \
  "v_mfma_f32_16x16x32_bf16 v[168:171], v[156:159], v[204:207], v[168:171]\n" \
  "v_mfma_f32_16x16x32_bf16 v[172:175], v[156:159], v[220:223], v[172:175]\n" \
  "v_cvt_pk_bf16_f32 v8, v160, v161\n" \
  "v_cvt_pk_bf16_f32 v9, v162, v163\n" \
  "v_cvt_pk_bf16_f32 v72, v164, v165\n" \
  "v_cvt_pk_bf16_f32 v73, v166, v167\n" \
  "v_cvt_pk_bf16_f32 v10, v168, v169\n" \
  "v_cvt_pk_bf16_f32 v11, v170, v171\n" \
  "s_nop 1\n" \
  "v_cvt_pk_bf16_f32 v74, v172, v173\n" \
  "v_cvt_pk_bf16_f32 v75, v174, v175\n" \
  "s_waitcnt lgkmcnt(0)\n" \
  "ds_read_b128 v[128:131], v188 offset:24576\n" \
  "ds_read_b128 v[132:135], v189 offset:24576\n" \
  "ds_read_b128 v[136:139], v190 offset:24576\n" \
  "ds_read_b128 v[140:143], v191 offset:24576\n" \
  "ds_read_b128 v[144:147], v188 offset:36864\n" \
  "ds_read_b128 v[148:151], v189 offset:36864\n" \
  "ds_read_b128 v[152:155], v190 offset:36864\n" \
  "ds_read_b128 v[156:159], v191 offset:36864\n" \
  "ds_read_b64_tr_b16 a[136:137], v187 offset:4096\n" \
  "ds_read_b64_tr_b16 a[138:139], v187 offset:5120\n" \
  "ds_read_b64_tr_b16 a[140:141], v187 offset:6144\n" \
  "ds_read_b64_tr_b16 a[142:143], v187 offset:7168\n" \
  "ds_read_b64_tr_b16 a[200:201], v187 offset:4104\n" \
  "ds_read_b64_tr_b16 a[202:203], v187 offset:5128\n" \
  "s_waitcnt lgkmcnt(6)\n" \
  "ds_read_b64_tr_b16 a[204:205], v187 offset:6152\n" \
  "ds_read_b64_tr_b16 a[206:207], v187 offset:7176\n" \
  "s_waitcnt lgkmcnt(8)\n" \
  "v_mfma_f32_16x16x32_bf16 v[160:163], v[128:131], v[192:195], 0\n" \
  "v_mfma_f32_16x16x32_bf16 v[164:167], v[128:131], v[208:211], 0\n" \
  "v_mfma_f32_16x16x32_bf16 v[160:163], v[132:135], v[196:199], v[160:163]\n" \
  "v_mfma_f32_16x16x32_bf16 v[164:167], v[132:135], v[212:215], v[164:167]\n" \
  "v_mfma_f32_16x16x32_bf16 v[160:163], v[136:139], v[200:203], v[160:163]\n" \
  "v_mfma_f32_16x16x32_bf16 v[164:167], v[136:139], v[216:219], v[164:167]\n" \
  "v_mfma_f32_16x16x32_bf16 v[160:163], v[140:143], v[204:207], v[160:163]\n" \
  "v_mfma_f32_16x16x32_bf16 v[164:167], v[140:143], v[220:223], v[164:167]\n" \
  "v_mfma_f32_16x16x32_bf16 v[168:171], v[144:147], v[192:195], 0\n" \
  "v_mfma_f32_16x16x32_bf16 v[172:175], v[144:147], v[208:211], 0\n" \
  "v_mfma_f32_16x16x32_bf16 v[168:171], v[148:151], v[196:199], v[168:171]\n" \
  "v_mfma_f32_16x16x32_bf16 v[172:175], v[148:151], v[212:215], v[172:175]\n" \
  "v_mfma_f32_16x16x32_bf16 v[168:171], v[152:155], v[200:203], v[168:171]\n" \
  "v_mfma_f32_16x16x32_bf16 v[172:175], v[152:155], v[216:219], v[172:175]\n" \
  "v_mfma_f32_16x16x32_bf16 v[168:171], v[156:159], v[204:207], v[168:171]\n" \
  "v_mfma_f32_16x16x32_bf16 v[172:175], v[156:159], v[220:223], v[172:175]\n" \
  "v_cvt_pk_bf16_f32 v12, v160, v161\n" \
  "v_cvt_pk_bf16_f32 v13, v162, v163\n" \
  "v_cvt_pk_bf16_f32 v76, v164, v165\n" \
  "v_cvt_pk_bf16_f32 v77, v166, v167\n" \
  "v_cvt_pk_bf16_f32 v14, v168, v169\n" \
  "v_cvt_pk_bf16_f32 v15, v170, v171\n" \
  "s_nop 1\n" \
  "v_cvt_pk_bf16_f32 v78, v172, v173\n" \
  "v_cvt_pk_bf16_f32 v79, v174, v175\n" \
  "s_waitcnt lgkmcnt(0)\n" \
  "s_waitcnt vmcnt(12)\n" \
  "s_barrier\n" \
  "s_add_u32 m0, s84, 49152\n" \
  "s_nop 0\n" \
  "global_load_lds_dwordx4 v176, s[4:5]\n" \
  "s_add_u32 s4, s4, 16384\n" \
  "s_addc_u32 s5, s5, 0\n" \
  "s_add_u32 m0, s84, 50176\n" \
  "s_nop 0\n" \
  "global_load_lds_dwordx4 v176, s[4:5]\n" \
  "s_add_u32 s4, s4, 245760\n" \
  "s_addc_u32 s5, s5, 0\n" \
  "s_add_u32 m0, s84, 51200\n" \
  "s_nop 0\n" \
  "global_load_lds_dwordx4 v176, s[4:5]\n" \
  "s_add_u32 s4, s4, 16384\n" \
  "s_addc_u32 s5, s5, 0\n" \
  "s_add_u32 m0, s84, 52224\n" \
  "s_nop 0\n" \
  "global_load_lds_dwordx4 v176, s[4:5]\n" \
  "s_add_u32 s4, s4, 245760\n" \
  "s_addc_u32 s5, s5, 0\n" \
  "s_add_u32 m0, s84, 53248\n" \
  "s_nop 0\n" \
  "global_load_lds_dwordx4 v176, s[82:83]\n" \
  "s_add_u32 s82, s82, 16384\n" \
  "s_addc_u32 s83, s83, 0\n" \
  "s_add_u32 m0, s84, 54272\n" \
  "s_nop 0\n" \
  "global_load_lds_dwordx4 v176, s[82:83]\n" \
  "s_add_u32 s82, s82, 245760\n" \
  "s_addc_u32 s83, s83, 0\n" \
  "s_add_u32 m0, s84, 55296\n" \
  "s_nop 0\n" \
  "global_load_lds_dwordx4 v176, s[82:83]\n" \
  "s_add_u32 s82, s82, 16384\n" \
  "s_addc_u32 s83, s83, 0\n" \
  "s_add_u32 m0, s84, 56320\n" \
  "s_nop 0\n" \
  "global_load_lds_dwordx4 v176, s[82:83]\n" \
  "s_add_u32 s82, s82, 245760\n" \
  "s_addc_u32 s83, s83, 0\n" \
  "s_add_u32 m0, s84, 57344\n" \
  "s_nop 0\n" \
  "global_load_lds_dwordx4 v177, s[6:7]\n" \
  "s_add_u32 s6, s6, 32768\n" \
  "s_addc_u32 s7, s7, 0\n" \
  "v_xor_b32 v186, 64, v177\n" \
  "s_add_u32 m0, s84, 58368\n" \
  "s_nop 0\n" \
  "global_load_lds_dwordx4 v186, s[6:7]\n" \
  "s_add_u32 s6, s6, 98304\n" \
  "s_addc_u32 s7, s7, 0\n" \
  "v_xor_b32 v186, 128, v177\n" \
  "s_add_u32 m0, s84, 59392\n" \
  "s_nop 0\n" \
  "global_load_lds_dwordx4 v186, s[6:7]\n" \
  "s_add_u32 s6, s6, 32768\n" \
  "s_addc_u32 s7, s7, 0\n" \
  "v_xor_b32 v186, 192, v177\n" \
  "s_add_u32 m0, s84, 60416\n" \
  "s_nop 0\n" \
  "global_load_lds_dwordx4 v186, s[6:7]\n" \
  "s_add_u32 s6, s6, 360448\n" \
  "s_addc_u32 s7, s7, 0\n" \
  "v_add_u32 v188, 98304, v181\n" \
  "v_add_u32 v189, 98304, v182\n" \
  "v_add_u32 v190, 98304, v183\n" \
  "v_add_u32 v191, 98304, v184\n" \
  "v_add_u32 v187, 98304, v178\n" \
  "ds_read_b128 v[128:131], v188\n" \
  "ds_read_b128 v[132:135], v189\n" \
  "ds_read_b128 v[136:139], v190\n" \
  "ds_read_b128 v[140:143], v191\n" \
  "ds_read_b128 v[144:147], v188 offset:12288\n" \
  "ds_read_b128 v[148:151], v189 offset:12288\n" \
  "ds_read_b128 v[152:155], v190 offset:12288\n" \
  "ds_read_b128 v[156:159], v191 offset:12288\n" \
  "ds_read_b64_tr_b16 a[16:17], v187\n" \
  "ds_read_b64_tr_b16 a[18:19], v187 offset:1024\n" \
  "ds_read_b64_tr_b16 a[20:21], v187 offset:2048\n" \
  "ds_read_b64_tr_b16 a[22:23], v187 offset:3072\n" \
  "ds_read_b64_tr_b16 a[80:81], v187 offset:8\n" \
  "ds_read_b64_tr_b16 a[82:83], v187 offset:1032\n" \
  "s_waitcnt lgkmcnt(6)\n" \
  "ds_read_b64_tr_b16 a[84:85], v187 offset:2056\n" \
  "ds_read_b64_tr_b16 a[86:87], v187 offset:3080\n" \
  "s_waitcnt lgkmcnt(8)\n" \
  "v_mfma_f32_16x16x32_bf16 v[160:163], v[128:131], v[192:195], 0\n" \
  "v_mfma_f32_16x16x32_bf16 v[164:167], v[128:131], v[208:211], 0\n" \
  "v_mfma_f32_16x16x32_bf16 v[160:163], v[132:135], v[196:199], v[160:163]\n" \
  "v_mfma_f32_16x16x32_bf16 v[164:167], v[132:135], v[212:215], v[164:167]\n" \
  "v_mfma_f32_16x16x32_bf16 v[160:163], v[136:139], v[200:203], v[160:163]\n" \
  "v_mfma_f32_16x16x32_bf16 v[164:167], v[136:139], v[216:219], v[164:167]\n" \
  "v_mfma_f32_16x16x32_bf16 v[160:163], v[140:143], v[204:207], v[160:163]\n" \
  "v_mfma_f32_16x16x32_bf16 v[164:167], v[140:143], v[220:223], v[164:167]\n" \
  "v_mfma_f32_16x16x32_bf16 v[168:171], v[144:147], v[192:195], 0\n" \
  "v_mfma_f32_16x16x32_bf16 v[172:175], v[144:147], v[208:211], 0\n" \
  "v_mfma_f32_16x16x32_bf16 v[168:171], v[148:151], v[196:199], v[168:171]\n" \
  "v_mfma_f32_16x16x32_bf16 v[172:175], v[148:151], v[212:215], v[172:175]\n" \
  "v_mfma_f32_16x16x32_bf16 v[168:171], v[152:155], v[200:203], v[168:171]\n" \
  "v_mfma_f32_16x16x32_bf16 v[172:175], v[152:155], v[216:219], v[172:175]\n" \
  "v_mfma_f32_16x16x32_bf16 v[168:171], v[156:159], v[204:207], v[168:171]\n" \
  "v_mfma_f32_16x16x32_bf16 v[172:175], v[156:159], v[220:223], v[172:175]\n" \
  "v_cvt_pk_bf16_f32 v16, v160, v161\n" \
  "v_cvt_pk_bf16_f32 v17, v162, v163\n" \
  "v_cvt_pk_bf16_f32 v80, v164, v165\n" \
  "v_cvt_pk_bf16_f32 v81, v166, v167\n" \
  "v_cvt_pk_bf16_f32 v18, v168, v169\n" \
  "v_cvt_pk_bf16_f32 v19, v170, v171\n" \
  "s_nop 1\n" \
  "v_cvt_pk_bf16_f32 v82, v172, v173\n" \
  "v_cvt_pk_bf16_f32 v83, v174, v175\n" \
  "s_waitcnt lgkmcnt(0)\n" \
  "ds_read_b128 v[128:131], v188 offset:24576\n" \
  "ds_read_b128 v[132:135], v189 offset:24576\n" \
  "ds_read_b128 v[136:139], v190 offset:24576\n" \
  "ds_read_b128 v[140:143], v191 offset:24576\n" \
  "ds_read_b128 v[144:147], v188 offset:36864\n" \
  "ds_read_b128 v[148:151], v189 offset:36864\n" \
  "ds_read_b128 v[152:155], v190 offset:36864\n" \
  "ds_read_b128 v[156:159], v191 offset:36864\n" \
  "ds_read_b64_tr_b16 a[144:145], v187 offset:4096\n" \
  "ds_read_b64_tr_b16 a[146:147], v187 offset:5120\n" \
  "ds_read_b64_tr_b16 a[148:149], v187 offset:6144\n" \
  "ds_read_b64_tr_b16 a[150:151], v187 offset:7168\n" \
  "ds_read_b64_tr_b16 a[208:209], v187 offset:4104\n" \
  "ds_read_b64_tr_b16 a[210:211], v187 offset:5128\n" \
  "s_waitcnt lgkmcnt(6)\n" \
  "ds_read_b64_tr_b16 a[212:213], v187 offset:6152\n" \
  "ds_read_b64_tr_b16 a[214:215], v187 offset:7176\n" \
  "s_waitcnt lgkmcnt(8)\n" \
  "v_mfma_f32_16x16x32_bf16 v[160:163], v[128:131], v[192:195], 0\n" \
  "v_mfma_f32_16x16x32_bf16 v[164:167], v[128:131], v[208:211], 0\n" \
  "v_mfma_f32_16x16x32_bf16 v[160:163], v[132:135], v[196:199], v[160:163]\n" \
  "v_mfma_f32_16x16x32_bf16 v[164:167], v[132:135], v[212:215], v[164:167]\n" \
  "v_mfma_f32_16x16x32_bf16 v[160:163], v[136:139], v[200:203], v[160:163]\n" \
  "v_mfma_f32_16x16x32_bf16 v[164:167], v[136:139], v[216:219], v[164:167]\n" \
  "v_mfma_f32_16x16x32_bf16 v[160:163], v[140:143], v[204:207], v[160:163]\n" \
  "v_mfma_f32_16x16x32_bf16 v[164:167], v[140:143], v[220:223], v[164:167]\n" \
  "v_mfma_f32_16x16x32_bf16 v[168:171], v[144:147], v[192:195], 0\n" \
  "v_mfma_f32_16x16x32_bf16 v[172:175], v[144:147], v[208:211], 0\n" \
  "v_mfma_f32_16x16x32_bf16 v[168:171], v[148:151], v[196:199], v[168:171]\n" \
  "v_mfma_f32_16x16x32_bf16 v[172:175], v[148:151], v[212:215], v[172:175]\n" \
  "v_mfma_f32_16x16x32_bf16 v[168:171], v[152:155], v[200:203], v[168:171]\n" \
  "v_mfma_f32_16x16x32_bf16 v[172:175], v[152:155], v[216:219], v[172:175]\n" \
  "v_mfma_f32_16x16x32_bf16 v[168:171], v[156:159], v[204:207], v[168:171]\n" \
  "v_mfma_f32_16x16x32_bf16 v[172:175], v[156:159], v[220:223], v[172:175]\n" \
  "v_cvt_pk_bf16_f32 v20, v160, v161\n" \
  "v_cvt_pk_bf16_f32 v21, v162, v163\n" \
  "v_cvt_pk_bf16_f32 v84, v164, v165\n" \
  "v_cvt_pk_bf16_f32 v85, v166, v167\n" \
  "v_cvt_pk_bf16_f32 v22, v168, v169\n" \
  "v_cvt_pk_bf16_f32 v23, v170, v171\n" \
  "s_nop 1\n" \
  "v_cvt_pk_bf16_f32 v86, v172, v173\n" \
  "v_cvt_pk_bf16_f32 v87, v174, v175\n" \
  "s_waitcnt lgkmcnt(0)\n" \
  "s_waitcnt vmcnt(12)\n" \
  "s_barrier\n" \
  "s_add_u32 m0, s84, 98304\n" \
  "s_nop 0\n" \
  "global_load_lds_dwordx4 v176, s[4:5]\n" \
  "s_add_u32 s4, s4, 16384\n" \
  "s_addc_u32 s5, s5, 0\n" \
  "s_add_u32 m0, s84, 99328\n" \
  "s_nop 0\n" \
  "global_load_lds_dwordx4 v176, s[4:5]\n" \
  "s_add_u32 s4, s4, 245760\n" \
  "s_addc_u32 s5, s5, 0\n" \
  "s_add_u32 m0, s84, 100352\n" \
  "s_nop 0\n" \
  "global_load_lds_dwordx4 v176, s[4:5]\n" \
  "s_add_u32 s4, s4, 16384\n" \
  "s_addc_u32 s5, s5, 0\n" \
  "s_add_u32 m0, s84, 101376\n" \
  "s_nop 0\n" \
  "global_load_lds_dwordx4 v176, s[4:5]\n" \
  "s_add_u32 s4, s4, 245760\n" \
  "s_addc_u32 s5, s5, 0\n" \
  "s_add_u32 m0, s84, 102400\n" \
  "s_nop 0\n" \
  "global_load_lds_dwordx4 v176, s[82:83]\n" \
  "s_add_u32 s82, s82, 16384\n" \
  "s_addc_u32 s83, s83, 0\n" \
  "s_add_u32 m0, s84, 103424\n" \
  "s_nop 0\n" \
  "global_load_lds_dwordx4 v176, s[82:83]\n" \
  "s_add_u32 s82, s82, 245760\n" \
  "s_addc_u32 s83, s83, 0\n" \
  "s_add_u32 m0, s84, 104448\n" \
  "s_nop 0\n" \
  "global_load_lds_dwordx4 v176, s[82:83]\n" \
  "s_add_u32 s82, s82, 16384\n" \
  "s_addc_u32 s83, s83, 0\n" \
  "s_add_u32 m0, s84, 105472\n" \
  "s_nop 0\n" \
  "global_load_lds_dwordx4 v176, s[82:83]\n" \
  "s_add_u32 s82, s82, 245760\n" \
  "s_addc_u32 s83, s83, 0\n" \
  "s_add_u32 m0, s84, 106496\n" \
  "s_nop 0\n" \
  "global_load_lds_dwordx4 v177, s[6:7]\n" \
  "s_add_u32 s6, s6, 32768\n" \
  "s_addc_u32 s7, s7, 0\n" \
  "v_xor_b32 v186, 64, v177\n" \
  "s_add_u32 m0, s84, 107520\n" \
  "s_nop 0\n" \
  "global_load_lds_dwordx4 v186, s[6:7]\n" \
  "s_add_u32 s6, s6, 98304\n" \
  "s_addc_u32 s7, s7, 0\n" \
  "v_xor_b32 v186, 128, v177\n" \
  "s_add_u32 m0, s84, 108544\n" \
  "s_nop 0\n" \
  "global_load_lds_dwordx4 v186, s[6:7]\n" \
  "s_add_u32 s6, s6, 32768\n" \
  "s_addc_u32 s7, s7, 0\n" \
  "v_xor_b32 v186, 192, v177\n" \
  "s_add_u32 m0, s84, 109568\n" \
  "s_nop 0\n" \
  "global_load_lds_dwordx4 v186, s[6:7]\n" \
  "s_add_u32 s6, s6, 360448\n" \
  "s_addc_u32 s7, s7, 0\n" \
  "ds_read_b128 v[128:131], v181\n" \
  "ds_read_b128 v[132:135], v182\n" \
  "ds_read_b128 v[136:139], v183\n" \
  "ds_read_b128 v[140:143], v184\n" \
  "ds_read_b128 v[144:147], v181 offset:12288\n" \
  "ds_read_b128 v[148:151], v182 offset:12288\n" \
  "ds_read_b128 v[152:155], v183 offset:12288\n" \
  "ds_read_b128 v[156:159], v184 offset:12288\n" \
  "ds_read_b64_tr_b16 a[24:25], v178\n" \
  "ds_read_b64_tr_b16 a[26:27], v178 offset:1024\n" \
  "ds_read_b64_tr_b16 a[28:29], v178 offset:2048\n" \
  "ds_read_b64_tr_b16 a[30:31], v178 offset:3072\n" \
  "ds_read_b64_tr_b16 a[88:89], v178 offset:8\n" \
  "ds_read_b64_tr_b16 a[90:91], v178 offset:1032\n" \
  "s_waitcnt lgkmcnt(6)\n" \
  "ds_read_b64_tr_b16 a[92:93], v178 offset:2056\n" \
  "ds_read_b64_tr_b16 a[94:95], v178 offset:3080\n" \
  "s_waitcnt lgkmcnt(8)\n" \
  "v_mfma_f32_16x16x32_bf16 v[160:163], v[128:131], v[192:195], 0\n" \
  "v_mfma_f32_16x16x32_bf16 v[164:167], v[128:131], v[208:211], 0\n" \
  "v_mfma_f32_16x16x32_bf16 v[160:163], v[132:135], v[196:199], v[160:163]\n" \
  "v_mfma_f32_16x16x32_bf16 v[164:167], v[132:135], v[212:215], v[164:167]\n" \
  "v_mfma_f32_16x16x32_bf16 v[160:163], v[136:139], v[200:203], v[160:163]\n" \
  "v_mfma_f32_16x16x32_bf16 v[164:167], v[136:139], v[216:219], v[164:167]\n" \
  "v_mfma_f32_16x16x32_bf16 v[160:163], v[140:143], v[204:207], v[160:163]\n" \
  "v_mfma_f32_16x16x32_bf16 v[164:167], v[140:143], v[220:223], v[164:167]\n" \
  "v_mfma_f32_16x16x32_bf16 v[168:171], v[144:147], v[192:195], 0\n" \
  "v_mfma_f32_16x16x32_bf16 v[172:175], v[144:147], v[208:211], 0\n" \
  "v_mfma_f32_16x16x32_bf16 v[168:171], v[148:151], v[196:199], v[168:171]\n" \
  "v_mfma_f32_16x16x32_bf16 v[172:175], v[148:151], v[212:215], v[172:175]\n" \
  "v_mfma_f32_16x16x32_bf16 v[168:171], v[152:155], v[200:203], v[168:171]\n" \
  "v_mfma_f32_16x16x32_bf16 v[172:175], v[152:155], v[216:219], v[172:175]\n" \
  "v_mfma_f32_16x16x32_bf16 v[168:171], v[156:159], v[204:207], v[168:171]\n" \
  "v_mfma_f32_16x16x32_bf16 v[172:175], v[156:159], v[220:223], v[172:175]\n" \
  "v_cvt_pk_bf16_f32 v24, v160, v161\n" \
  "v_cvt_pk_bf16_f32 v25, v162, v163\n" \
  "v_cvt_pk_bf16_f32 v88, v164, v165\n" \
  "v_cvt_pk_bf16_f32 v89, v166, v167\n" \
  "v_cvt_pk_bf16_f32 v26, v168, v169\n" \
  "v_cvt_pk_bf16_f32 v27, v170, v171\n" \
  "s_nop 1\n" \
  "v_cvt_pk_bf16_f32 v90, v172, v173\n" \
  "v_cvt_pk_bf16_f32 v91, v174, v175\n" \
  "s_waitcnt lgkmcnt(0)\n" \
  "ds_read_b128 v[128:131], v181 offset:24576\n" \
  "ds_read_b128 v[132:135], v182 offset:24576\n" \
  "ds_read_b128 v[136:139], v183 offset:24576\n" \
  "ds_read_b128 v[140:143], v184 offset:24576\n" \
  "ds_read_b128 v[144:147], v181 offset:36864\n" \
  "ds_read_b128 v[148:151], v182 offset:36864\n" \
  "ds_read_b128 v[152:155], v183 offset:36864\n" \
  "ds_read_b128 v[156:159], v184 offset:36864\n" \
  "ds_read_b64_tr_b16 a[152:153], v178 offset:4096\n" \
  "ds_read_b64_tr_b16 a[154:155], v178 offset:5120\n" \
  "ds_read_b64_tr_b16 a[156:157], v178 offset:6144\n" \
  "ds_read_b64_tr_b16 a[158:159], v178 offset:7168\n" \
  "ds_read_b64_tr_b16 a[216:217], v178 offset:4104\n" \
  "ds_read_b64_tr_b16 a[218:219], v178 offset:5128\n" \
  "s_waitcnt lgkmcnt(6)\n" \
  "ds_read_b64_tr_b16 a[220:221], v178 offset:6152\n" \
  "ds_read_b64_tr_b16 a[222:223], v178 offset:7176\n" \
  "s_waitcnt lgkmcnt(8)\n" \
  "v_mfma_f32_16x16x32_bf16 v[160:163], v[128:131], v[192:195], 0\n" \
  "v_mfma_f32_16x16x32_bf16 v[164:167], v[128:131], v[208:211], 0\n" \
  "v_mfma_f32_16x16x32_bf16 v[160:163], v[132:135], v[196:199], v[160:163]\n" \
  "v_mfma_f32_16x16x32_bf16 v[164:167], v[132:135], v[212:215], v[164:167]\n" \
  "v_mfma_f32_16x16x32_bf16 v[160:163], v[136:139], v[200:203], v[160:163]\n" \
  "v_mfma_f32_16x16x32_bf16 v[164:167], v[136:139], v[216:219], v[164:167]\n" \
  "v_mfma_f32_16x16x32_bf16 v[160:163], v[140:143], v[204:207], v[160:163]\n" \
  "v_mfma_f32_16x16x32_bf16 v[164:167], v[140:143], v[220:223], v[164:167]\n" \
  "v_mfma_f32_16x16x32_bf16 v[168:171], v[144:147], v[192:195], 0\n" \
  "v_mfma_f32_16x16x32_bf16 v[172:175], v[144:147], v[208:211], 0\n" \
  "v_mfma_f32_16x16x32_bf16 v[168:171], v[148:151], v[196:199], v[168:171]\n" \
  "v_mfma_f32_16x16x32_bf16 v[172:175], v[148:151], v[212:215], v[172:175]\n" \
  "v_mfma_f32_16x16x32_bf16 v[168:171], v[152:155], v[200:203], v[168:171]\n" \
  "v_mfma_f32_16x16x32_bf16 v[172:175], v[152:155], v[216:219], v[172:175]\n" \
  "v_mfma_f32_16x16x32_bf16 v[168:171], v[156:159], v[204:207], v[168:171]\n" \
  "v_mfma_f32_16x16x32_bf16 v[172:175], v[156:159], v[220:223], v[172:175]\n" \
  "v_cvt_pk_bf16_f32 v28, v160, v161\n" \
  "v_cvt_pk_bf16_f32 v29, v162, v163\n" \
  "v_cvt_pk_bf16_f32 v92, v164, v165\n" \
  "v_cvt_pk_bf16_f32 v93, v166, v167\n" \
  "v_cvt_pk_bf16_f32 v30, v168, v169\n" \
  "v_cvt_pk_bf16_f32 v31, v170, v171\n" \
  "s_nop 1\n" \
  "v_cvt_pk_bf16_f32 v94, v172, v173\n" \
  "v_cvt_pk_bf16_f32 v95, v174, v175\n" \
  "s_waitcnt lgkmcnt(0)\n" \
  "s_waitcnt vmcnt(12)\n" \
  "s_barrier\n" \
  "s_add_u32 m0, s84, 0\n" \
  "s_nop 0\n" \
  "global_load_lds_dwordx4 v176, s[4:5]\n" \
  "s_add_u32 s4, s4, 16384\n" \
  "s_addc_u32 s5, s5, 0\n" \
  "s_add_u32 m0, s84, 1024\n" \
  "s_nop 0\n" \
  "global_load_lds_dwordx4 v176, s[4:5]\n" \
  "s_add_u32 s4, s4, 245760\n" \
  "s_addc_u32 s5, s5, 0\n" \
  "s_add_u32 m0, s84, 2048\n" \
  "s_nop 0\n" \
  "global_load_lds_dwordx4 v176, s[4:5]\n" \
  "s_add_u32 s4, s4, 16384\n" \
  "s_addc_u32 s5, s5, 0\n" \
  "s_add_u32 m0, s84, 3072\n" \
  "s_nop 0\n" \
  "global_load_lds_dwordx4 v176, s[4:5]\n" \
  "s_add_u32 s4, s4, 245760\n" \
  "s_addc_u32 s5, s5, 0\n" \
  "s_add_u32 m0, s84, 4096\n" \
  "s_nop 0\n" \
  "global_load_lds_dwordx4 v176, s[82:83]\n" \
  "s_add_u32 s82, s82, 16384\n" \
  "s_addc_u32 s83, s83, 0\n" \
  "s_add_u32 m0, s84, 5120\n" \
  "s_nop 0\n" \
  "global_load_lds_dwordx4 v176, s[82:83]\n" \
  "s_add_u32 s82, s82, 245760\n" \
  "s_addc_u32 s83, s83, 0\n" \
  "s_add_u32 m0, s84, 6144\n" \
  "s_nop 0\n" \
  "global_load_lds_dwordx4 v176, s[82:83]\n" \
  "s_add_u32 s82, s82, 16384\n" \
  "s_addc_u32 s83, s83, 0\n" \
  "s_add_u32 m0, s84, 7168\n" \
  "s_nop 0\n" \
  "global_load_lds_dwordx4 v176, s[82:83]\n" \
  "s_add_u32 s82, s82, 245760\n" \
  "s_addc_u32 s83, s83, 0\n" \
  "s_add_u32 m0, s84, 8192\n" \
  "s_nop 0\n" \
  "global_load_lds_dwordx4 v177, s[6:7]\n" \
  "s_add_u32 s6, s6, 32768\n" \
  "s_addc_u32 s7, s7, 0\n" \
  "v_xor_b32 v186, 64, v177\n" \
  "s_add_u32 m0, s84, 9216\n" \
  "s_nop 0\n" \
  "global_load_lds_dwordx4 v186, s[6:7]\n" \
  "s_add_u32 s6, s6, 98304\n" \
  "s_addc_u32 s7, s7, 0\n" \
  "v_xor_b32 v186, 128, v177\n" \
  "s_add_u32 m0, s84, 10240\n" \
  "s_nop 0\n" \
  "global_load_lds_dwordx4 v186, s[6:7]\n" \
  "s_add_u32 s6, s6, 32768\n" \
  "s_addc_u32 s7, s7, 0\n" \
  "v_xor_b32 v186, 192, v177\n" \
  "s_add_u32 m0, s84, 11264\n" \
  "s_nop 0\n" \
  "global_load_lds_dwordx4 v186, s[6:7]\n" \
  "s_add_u32 s6, s6, 360448\n" \
  "s_addc_u32 s7, s7, 0\n" \
  "v_add_u32 v188, 49152, v181\n" \
  "v_add_u32 v189, 49152, v182\n" \
  "v_add_u32 v190, 49152, v183\n" \
  "v_add_u32 v191, 49152, v184\n" \
  "v_add_u32 v187, 49152, v178\n" \
  "ds_read_b128 v[128:131], v188\n" \
  "ds_read_b128 v[132:135], v189\n" \
  "ds_read_b128 v[136:139], v190\n" \
  "ds_read_b128 v[140:143], v191\n" \
  "ds_read_b128 v[144:147], v188 offset:12288\n" \
  "ds_read_b128 v[148:151], v189 offset:12288\n" \
  "ds_read_b128 v[152:155], v190 offset:12288\n" \
  "ds_read_b128 v[156:159], v191 offset:12288\n" \
  "ds_read_b64_tr_b16 a[32:33], v187\n" \
  "ds_read_b64_tr_b16 a[34:35], v187 offset:1024\n" \
  "ds_read_b64_tr_b16 a[36:37], v187 offset:2048\n" \
  "ds_read_b64_tr_b16 a[38:39], v187 offset:3072\n" \
  "ds_read_b64_tr_b16 a[96:97], v187 offset:8\n" \
  "ds_read_b64_tr_b16 a[98:99], v187 offset:1032\n" \
  "s_waitcnt lgkmcnt(6)\n" \
  "ds_read_b64_tr_b16 a[100:101], v187 offset:2056\n" \
  "ds_read_b64_tr_b16 a[102:103], v187 offset:3080\n" \
  "s_waitcnt lgkmcnt(8)\n" \
  "v_mfma_f32_16x16x32_bf16 v[160:163], v[128:131], v[192:195], 0\n" \
  "v_mfma_f32_16x16x32_bf16 v[164:167], v[128:131], v[208:211], 0\n" \
  "v_mfma_f32_16x16x32_bf16 v[160:163], v[132:135], v[196:199], v[160:163]\n" \
  "v_mfma_f32_16x16x32_bf16 v[164:167], v[132:135], v[212:215], v[164:167]\n" \
  "v_mfma_f32_16x16x32_bf16 v[160:163], v[136:139], v[200:203], v[160:163]\n" \
  "v_mfma_f32_16x16x32_bf16 v[164:167], v[136:139], v[216:219], v[164:167]\n" \
  "v_mfma_f32_16x16x32_bf16 v[160:163], v[140:143], v[204:207], v[160:163]\n" \
  "v_mfma_f32_16x16x32_bf16 v[164:167], v[140:143], v[220:223], v[164:167]\n" \
  "v_mfma_f32_16x16x32_bf16 v[168:171], v[144:147], v[192:195], 0\n" \
  "v_mfma_f32_16x16x32_bf16 v[172:175], v[144:147], v[208:211], 0\n" \
  "v_mfma_f32_16x16x32_bf16 v[168:171], v[148:151], v[196:199], v[168:171]\n" \
  "v_mfma_f32_16x16x32_bf16 v[172:175], v[148:151], v[212:215], v[172:175]\n" \
  "v_mfma_f32_16x16x32_bf16 v[168:171], v[152:155], v[200:203], v[168:171]\n" \
  "v_mfma_f32_16x16x32_bf16 v[172:175], v[152:155], v[216:219], v[172:175]\n" \
  "v_mfma_f32_16x16x32_bf16 v[168:171], v[156:159], v[204:207], v[168:171]\n" \
  "v_mfma_f32_16x16x32_bf16 v[172:175], v[156:159], v[220:223], v[172:175]\n" \
  "v_cvt_pk_bf16_f32 v32, v160, v161\n" \
  "v_cvt_pk_bf16_f32 v33, v162, v163\n" \
  "v_cvt_pk_bf16_f32 v96, v164, v165\n" \
  "v_cvt_pk_bf16_f32 v97, v166, v167\n" \
  "v_cvt_pk_bf16_f32 v34, v168, v169\n" \
  "v_cvt_pk_bf16_f32 v35, v170, v171\n" \
  "s_nop 1\n" \
  "v_cvt_pk_bf16_f32 v98, v172, v173\n" \
  "v_cvt_pk_bf16_f32 v99, v174, v175\n" \
  "s_waitcnt lgkmcnt(0)\n" \
  "ds_read_b128 v[128:131], v188 offset:24576\n" \
  "ds_read_b128 v[132:135], v189 offset:24576\n" \
  "ds_read_b128 v[136:139], v190 offset:24576\n" \
  "ds_read_b128 v[140:143], v191 offset:24576\n" \
  "ds_read_b128 v[144:147], v188 offset:36864\n" \
  "ds_read_b128 v[148:151], v189 offset:36864\n" \
  "ds_read_b128 v[152:155], v190 offset:36864\n" \
  "ds_read_b128 v[156:159], v191 offset:36864\n" \
  "ds_read_b64_tr_b16 a[160:161], v187 offset:4096\n" \
  "ds_read_b64_tr_b16 a[162:163], v187 offset:5120\n" \
  "ds_read_b64_tr_b16 a[164:165], v187 offset:6144\n" \
  "ds_read_b64_tr_b16 a[166:167], v187 offset:7168\n" \
  "ds_read_b64_tr_b16 a[224:225], v187 offset:4104\n" \
  "ds_read_b64_tr_b16 a[226:227], v187 offset:5128\n" \
  "s_waitcnt lgkmcnt(6)\n" \
  "ds_read_b64_tr_b16 a[228:229], v187 offset:6152\n" \
  "ds_read_b64_tr_b16 a[230:231], v187 offset:7176\n" \
  "s_waitcnt lgkmcnt(8)\n" \
  "v_mfma_f32_16x16x32_bf16 v[160:163], v[128:131], v[192:195], 0\n" \
  "v_mfma_f32_16x16x32_bf16 v[164:167], v[128:131], v[208:211], 0\n" \
  "v_mfma_f32_16x16x32_bf16 v[160:163], v[132:135], v[196:199], v[160:163]\n" \
  "v_mfma_f32_16x16x32_bf16 v[164:167], v[132:135], v[212:215], v[164:167]\n" \
  "v_mfma_f32_16x16x32_bf16 v[160:163], v[136:139], v[200:203], v[160:163]\n" \
  "v_mfma_f32_16x16x32_bf16 v[164:167], v[136:139], v[216:219], v[164:167]\n" \
  "v_mfma_f32_16x16x32_bf16 v[160:163], v[140:143], v[204:207], v[160:163]\n" \
  "v_mfma_f32_16x16x32_bf16 v[164:167], v[140:143], v[220:223], v[164:167]\n" \
  "v_mfma_f32_16x16x32_bf16 v[168:171], v[144:147], v[192:195], 0\n" \
  "v_mfma_f32_16x16x32_bf16 v[172:175], v[144:147], v[208:211], 0\n" \
  "v_mfma_f32_16x16x32_bf16 v[168:171], v[148:151], v[196:199], v[168:171]\n" \
  "v_mfma_f32_16x16x32_bf16 v[172:175], v[148:151], v[212:215], v[172:175]\n" \
  "v_mfma_f32_16x16x32_bf16 v[168:171], v[152:155], v[200:203], v[168:171]\n" \
  "v_mfma_f32_16x16x32_bf16 v[172:175], v[152:155], v[216:219], v[172:175]\n" \
  "v_mfma_f32_16x16x32_bf16 v[168:171], v[156:159], v[204:207], v[168:171]\n" \
  "v_mfma_f32_16x16x32_bf16 v[172:175], v[156:159], v[220:223], v[172:175]\n" \
  "v_cvt_pk_bf16_f32 v36, v160, v161\n" \
  "v_cvt_pk_bf16_f32 v37, v162, v163\n" \
  "v_cvt_pk_bf16_f32 v100, v164, v165\n" \
  "v_cvt_pk_bf16_f32 v101, v166, v167\n" \
  "v_cvt_pk_bf16_f32 v38, v168, v169\n" \
  "v_cvt_pk_bf16_f32 v39, v170, v171\n" \
  "s_nop 1\n" \
  "v_cvt_pk_bf16_f32 v102, v172, v173\n" \
  "v_cvt_pk_bf16_f32 v103, v174, v175\n" \
  "s_waitcnt lgkmcnt(0)\n" \
  "s_waitcnt vmcnt(12)\n" \
  "s_barrier\n" \
  "s_add_u32 m0, s84, 49152\n" \
  "s_nop 0\n" \
  "global_load_lds_dwordx4 v176, s[4:5]\n" \
  "s_add_u32 s4, s4, 16384\n" \
  "s_addc_u32 s5, s5, 0\n" \
  "s_add_u32 m0, s84, 50176\n" \
  "s_nop 0\n" \
  "global_load_lds_dwordx4 v176, s[4:5]\n" \
  "s_add_u32 s4, s4, 245760\n" \
  "s_addc_u32 s5, s5, 0\n" \
  "s_add_u32 m0, s84, 51200\n" \
  "s_nop 0\n" \
  "global_load_lds_dwordx4 v176, s[4:5]\n" \
  "s_add_u32 s4, s4, 16384\n" \
  "s_addc_u32 s5, s5, 0\n" \
  "s_add_u32 m0, s84, 52224\n" \
  "s_nop 0\n" \
  "global_load_lds_dwordx4 v176, s[4:5]\n" \
  "s_add_u32 s4, s4, 245760\n" \
  "s_addc_u32 s5, s5, 0\n" \
  "s_add_u32 m0, s84, 53248\n" \
  "s_nop 0\n" \
  "global_load_lds_dwordx4 v176, s[82:83]\n" \
  "s_add_u32 s82, s82, 16384\n" \
  "s_addc_u32 s83, s83, 0\n" \
  "s_add_u32 m0, s84, 54272\n" \
  "s_nop 0\n" \
  "global_load_lds_dwordx4 v176, s[82:83]\n" \
  "s_add_u32 s82, s82, 245760\n" \
  "s_addc_u32 s83, s83, 0\n" \
  "s_add_u32 m0, s84, 55296\n" \
  "s_nop 0\n" \
  "global_load_lds_dwordx4 v176, s[82:83]\n" \
  "s_add_u32 s82, s82, 16384\n" \
  "s_addc_u32 s83, s83, 0\n" \
  "s_add_u32 m0, s84, 56320\n" \
  "s_nop 0\n" \
  "global_load_lds_dwordx4 v176, s[82:83]\n" \
  "s_add_u32 s82, s82, 245760\n" \
  "s_addc_u32 s83, s83, 0\n" \
  "s_add_u32 m0, s84, 57344\n" \
  "s_nop 0\n" \
  "global_load_lds_dwordx4 v177, s[6:7]\n" \
  "s_add_u32 s6, s6, 32768\n" \
  "s_addc_u32 s7, s7, 0\n" \
  "v_xor_b32 v186, 64, v177\n" \
  "s_add_u32 m0, s84, 58368\n" \
  "s_nop 0\n" \
  "global_load_lds_dwordx4 v186, s[6:7]\n" \
  "s_add_u32 s6, s6, 98304\n" \
  "s_addc_u32 s7, s7, 0\n" \
  "v_xor_b32 v186, 128, v177\n" \
  "s_add_u32 m0, s84, 59392\n" \
  "s_nop 0\n" \
  "global_load_lds_dwordx4 v186, s[6:7]\n" \
  "s_add_u32 s6, s6, 32768\n" \
  "s_addc_u32 s7, s7, 0\n" \
  "v_xor_b32 v186, 192, v177\n" \
  "s_add_u32 m0, s84, 60416\n" \
  "s_nop 0\n" \
  "global_load_lds_dwordx4 v186, s[6:7]\n" \
  "s_add_u32 s6, s6, 360448\n" \
  "s_addc_u32 s7, s7, 0\n" \
  "v_add_u32 v188, 98304, v181\n" \
  "v_add_u32 v189, 98304, v182\n" \
  "v_add_u32 v190, 98304, v183\n" \
  "v_add_u32 v191, 98304, v184\n" \
  "v_add_u32 v187, 98304, v178\n" \
  "ds_read_b128 v[128:131], v188\n" \
  "ds_read_b128 v[132:135], v189\n" \
  "ds_read_b128 v[136:139], v190\n" \
  "ds_read_b128 v[140:143], v191\n" \
  "ds_read_b128 v[144:147], v188 offset:12288\n" \
  "ds_read_b128 v[148:151], v189 offset:12288\n" \
  "ds_read_b128 v[152:155], v190 offset:12288\n" \
  "ds_read_b128 v[156:159], v191 offset:12288\n" \
  "ds_read_b64_tr_b16 a[40:41], v187\n" \
  "ds_read_b64_tr_b16 a[42:43], v187 offset:1024\n" \
  "ds_read_b64_tr_b16 a[44:45], v187 offset:2048\n" \
  "ds_read_b64_tr_b16 a[46:47], v187 offset:3072\n" \
  "ds_read_b64_tr_b16 a[104:105], v187 offset:8\n" \
  "ds_read_b64_tr_b16 a[106:107], v187 offset:1032\n" \
  "s_waitcnt lgkmcnt(6)\n" \
  "ds_read_b64_tr_b16 a[108:109], v187 offset:2056\n" \
  "ds_read_b64_tr_b16 a[110:111], v187 offset:3080\n" \
  "s_waitcnt lgkmcnt(8)\n" \
  "v_mfma_f32_16x16x32_bf16 v[160:163], v[128:131], v[192:195], 0\n" \
  "v_mfma_f32_16x16x32_bf16 v[164:167], v[128:131], v[208:211], 0\n" \
  "v_mfma_f32_16x16x32_bf16 v[160:163], v[132:135], v[196:199], v[160:163]\n" \
  "v_mfma_f32_16x16x32_bf16 v[164:167], v[132:135], v[212:215], v[164:167]\n" \
  "v_mfma_f32_16x16x32_bf16 v[160:163], v[136:139], v[200:203], v[160:163]\n" \
  "v_mfma_f32_16x16x32_bf16 v[164:167], v[136:139], v[216:219], v[164:167]\n" \
  "v_mfma_f32_16x16x32_bf16 v[160:163], v[140:143], v[204:207], v[160:163]\n" \
  "v_mfma_f32_16x16x32_bf16 v[164:167], v[140:143], v[220:223], v[164:167]\n" \
  "v_mfma_f32_16x16x32_bf16 v[168:171], v[144:147], v[192:195], 0\n" \
  "v_mfma_f32_16x16x32_bf16 v[172:175], v[144:147], v[208:211], 0\n" \
  "v_mfma_f32_16x16x32_bf16 v[168:171], v[148:151], v[196:199], v[168:171]\n" \
  "v_mfma_f32_16x16x32_bf16 v[172:175], v[148:151], v[212:215], v[172:175]\n" \
  "v_mfma_f32_16x16x32_bf16 v[168:171], v[152:155], v[200:203], v[168:171]\n" \
  "v_mfma_f32_16x16x32_bf16 v[172:175], v[152:155], v[216:219], v[172:175]\n" \
  "v_mfma_f32_16x16x32_bf16 v[168:171], v[156:159], v[204:207], v[168:171]\n" \
  "v_mfma_f32_16x16x32_bf16 v[172:175], v[156:159], v[220:223], v[172:175]\n" \
  "v_cvt_pk_bf16_f32 v40, v160, v161\n" \
  "v_cvt_pk_bf16_f32 v41, v162, v163\n" \
  "v_cvt_pk_bf16_f32 v104, v164, v165\n" \
  "v_cvt_pk_bf16_f32 v105, v166, v167\n" \
  "v_cvt_pk_bf16_f32 v42, v168, v169\n" \
  "v_cvt_pk_bf16_f32 v43, v170, v171\n" \
  "s_nop 1\n" \
  "v_cvt_pk_bf16_f32 v106, v172, v173\n" \
  "v_cvt_pk_bf16_f32 v107, v174, v175\n" \
  "s_waitcnt lgkmcnt(0)\n" \
  "ds_read_b128 v[128:131], v188 offset:24576\n" \
  "ds_read_b128 v[132:135], v189 offset:24576\n" \
  "ds_read_b128 v[136:139], v190 offset:24576\n" \
  "ds_read_b128 v[140:143], v191 offset:24576\n" \
  "ds_read_b128 v[144:147], v188 offset:36864\n" \
  "ds_read_b128 v[148:151], v189 offset:36864\n" \
  "ds_read_b128 v[152:155], v190 offset:36864\n" \
  "ds_read_b128 v[156:159], v191 offset:36864\n" \
  "ds_read_b64_tr_b16 a[168:169], v187 offset:4096\n" \
  "ds_read_b64_tr_b16 a[170:171], v187 offset:5120\n" \
  "ds_read_b64_tr_b16 a[172:173], v187 offset:6144\n" \
  "ds_read_b64_tr_b16 a[174:175], v187 offset:7168\n" \
  "ds_read_b64_tr_b16 a[232:233], v187 offset:4104\n" \
  "ds_read_b64_tr_b16 a[234:235], v187 offset:5128\n" \
  "s_waitcnt lgkmcnt(6)\n" \
  "ds_read_b64_tr_b16 a[236:237], v187 offset:6152\n" \
  "ds_read_b64_tr_b16 a[238:239], v187 offset:7176\n" \
  "s_waitcnt lgkmcnt(8)\n" \
  "v_mfma_f32_16x16x32_bf16 v[160:163], v[128:131], v[192:195], 0\n" \
  "v_mfma_f32_16x16x32_bf16 v[164:167], v[128:131], v[208:211], 0\n" \
  "v_mfma_f32_16x16x32_bf16 v[160:163], v[132:135], v[196:199], v[160:163]\n" \
  "v_mfma_f32_16x16x32_bf16 v[164:167], v[132:135], v[212:215], v[164:167]\n" \
  "v_mfma_f32_16x16x32_bf16 v[160:163], v[136:139], v[200:203], v[160:163]\n" \
  "v_mfma_f32_16x16x32_bf16 v[164:167], v[136:139], v[216:219], v[164:167]\n" \
  "v_mfma_f32_16x16x32_bf16 v[160:163], v[140:143], v[204:207], v[160:163]\n" \
  "v_mfma_f32_16x16x32_bf16 v[164:167], v[140:143], v[220:223], v[164:167]\n" \
  "v_mfma_f32_16x16x32_bf16 v[168:171], v[144:147], v[192:195], 0\n" \
  "v_mfma_f32_16x16x32_bf16 v[172:175], v[144:147], v[208:211], 0\n" \
  "v_mfma_f32_16x16x32_bf16 v[168:171], v[148:151], v[196:199], v[168:171]\n" \
  "v_mfma_f32_16x16x32_bf16 v[172:175], v[148:151], v[212:215], v[172:175]\n" \
  "v_mfma_f32_16x16x32_bf16 v[168:171], v[152:155], v[200:203], v[168:171]\n" \
  "v_mfma_f32_16x16x32_bf16 v[172:175], v[152:155], v[216:219], v[172:175]\n" \
  "v_mfma_f32_16x16x32_bf16 v[168:171], v[156:159], v[204:207], v[168:171]\n" \
  "v_mfma_f32_16x16x32_bf16 v[172:175], v[156:159], v[220:223], v[172:175]\n" \
  "v_cvt_pk_bf16_f32 v44, v160, v161\n" \
  "v_cvt_pk_bf16_f32 v45, v162, v163\n" \
  "v_cvt_pk_bf16_f32 v108, v164, v165\n" \
  "v_cvt_pk_bf16_f32 v109, v166, v167\n" \
  "v_cvt_pk_bf16_f32 v46, v168, v169\n" \
  "v_cvt_pk_bf16_f32 v47, v170, v171\n" \
  "s_nop 1\n" \
  "v_cvt_pk_bf16_f32 v110, v172, v173\n" \
  "v_cvt_pk_bf16_f32 v111, v174, v175\n" \
  "s_waitcnt lgkmcnt(0)\n" \
  "s_waitcnt vmcnt(12)\n" \
  "s_barrier\n" \
  "ds_read_b128 v[128:131], v181\n" \
  "ds_read_b128 v[132:135], v182\n" \
  "ds_read_b128 v[136:139], v183\n" \
  "ds_read_b128 v[140:143], v184\n" \
  "ds_read_b128 v[144:147], v181 offset:12288\n" \
  "ds_read_b128 v[148:151], v182 offset:12288\n" \
  "ds_read_b128 v[152:155], v183 offset:12288\n" \
  "ds_read_b128 v[156:159], v184 offset:12288\n" \
  "ds_read_b64_tr_b16 a[48:49], v178\n" \
  "ds_read_b64_tr_b16 a[50:51], v178 offset:1024\n" \
  "ds_read_b64_tr_b16 a[52:53], v178 offset:2048\n" \
  "ds_read_b64_tr_b16 a[54:55], v178 offset:3072\n" \
  "ds_read_b64_tr_b16 a[112:113], v178 offset:8\n" \
  "ds_read_b64_tr_b16 a[114:115], v178 offset:1032\n" \
  "s_waitcnt lgkmcnt(6)\n" \
  "ds_read_b64_tr_b16 a[116:117], v178 offset:2056\n" \
  "ds_read_b64_tr_b16 a[118:119], v178 offset:3080\n" \
  "s_waitcnt lgkmcnt(8)\n" \
  "v_mfma_f32_16x16x32_bf16 v[160:163], v[128:131], v[192:195], 0\n" \
  "v_mfma_f32_16x16x32_bf16 v[164:167], v[128:131], v[208:211], 0\n" \
  "v_mfma_f32_16x16x32_bf16 v[160:163], v[132:135], v[196:199], v[160:163]\n" \
  "v_mfma_f32_16x16x32_bf16 v[164:167], v[132:135], v[212:215], v[164:167]\n" \
  "v_mfma_f32_16x16x32_bf16 v[160:163], v[136:139], v[200:203], v[160:163]\n" \
  "v_mfma_f32_16x16x32_bf16 v[164:167], v[136:139], v[216:219], v[164:167]\n" \
  "v_mfma_f32_16x16x32_bf16 v[160:163], v[140:143], v[204:207], v[160:163]\n" \
  "v_mfma_f32_16x16x32_bf16 v[164:167], v[140:143], v[220:223], v[164:167]\n" \
  "v_mfma_f32_16x16x32_bf16 v[168:171], v[144:147], v[192:195], 0\n" \
  "v_mfma_f32_16x16x32_bf16 v[172:175], v[144:147], v[208:211], 0\n" \
  "v_mfma_f32_16x16x32_bf16 v[168:171], v[148:151], v[196:199], v[168:171]\n" \
  "v_mfma_f32_16x16x32_bf16 v[172:175], v[148:151], v[212:215], v[172:175]\n" \
  "v_mfma_f32_16x16x32_bf16 v[168:171], v[152:155], v[200:203], v[168:171]\n" \
  "v_mfma_f32_16x16x32_bf16 v[172:175], v[152:155], v[216:219], v[172:175]\n" \
  "v_mfma_f32_16x16x32_bf16 v[168:171], v[156:159], v[204:207], v[168:171]\n" \
  "v_mfma_f32_16x16x32_bf16 v[172:175], v[156:159], v[220:223], v[172:175]\n" \
  "v_cvt_pk_bf16_f32 v48, v160, v161\n" \
  "v_cvt_pk_bf16_f32 v49, v162, v163\n" \
  "v_cvt_pk_bf16_f32 v112, v164, v165\n" \
  "v_cvt_pk_bf16_f32 v113, v166, v167\n" \
  "v_cvt_pk_bf16_f32 v50, v168, v169\n" \
  "v_cvt_pk_bf16_f32 v51, v170, v171\n" \
  "s_nop 1\n" \
  "v_cvt_pk_bf16_f32 v114, v172, v173\n" \
  "v_cvt_pk_bf16_f32 v115, v174, v175\n" \
  "s_waitcnt lgkmcnt(0)\n" \
  "ds_read_b128 v[128:131], v181 offset:24576\n" \
  "ds_read_b128 v[132:135], v182 offset:24576\n" \
  "ds_read_b128 v[136:139], v183 offset:24576\n" \
  "ds_read_b128 v[140:143], v184 offset:24576\n" \
  "ds_read_b128 v[144:147], v181 offset:36864\n" \
  "ds_read_b128 v[148:151], v182 offset:36864\n" \
  "ds_read_b128 v[152:155], v183 offset:36864\n" \
  "ds_read_b128 v[156:159], v184 offset:36864\n" \
  "ds_read_b64_tr_b16 a[176:177], v178 offset:4096\n" \
  "ds_read_b64_tr_b16 a[178:179], v178 offset:5120\n" \
  "ds_read_b64_tr_b16 a[180:181], v178 offset:6144\n" \
  "ds_read_b64_tr_b16 a[182:183], v178 offset:7168\n" \
  "ds_read_b64_tr_b16 a[240:241], v178 offset:4104\n" \
  "ds_read_b64_tr_b16 a[242:243], v178 offset:5128\n" \
  "s_waitcnt lgkmcnt(6)\n" \
  "ds_read_b64_tr_b16 a[244:245], v178 offset:6152\n" \
  "ds_read_b64_tr_b16 a[246:247], v178 offset:7176\n" \
  "s_waitcnt lgkmcnt(8)\n" \
  "v_mfma_f32_16x16x32_bf16 v[160:163], v[128:131], v[192:195], 0\n" \
  "v_mfma_f32_16x16x32_bf16 v[164:167], v[128:131], v[208:211], 0\n" \
  "v_mfma_f32_16x16x32_bf16 v[160:163], v[132:135], v[196:199], v[160:163]\n" \
  "v_mfma_f32_16x16x32_bf16 v[164:167], v[132:135], v[212:215], v[164:167]\n" \
  "v_mfma_f32_16x16x32_bf16 v[160:163], v[136:139], v[200:203], v[160:163]\n" \
  "v_mfma_f32_16x16x32_bf16 v[164:167], v[136:139], v[216:219], v[164:167]\n" \
  "v_mfma_f32_16x16x32_bf16 v[160:163], v[140:143], v[204:207], v[160:163]\n" \
  "v_mfma_f32_16x16x32_bf16 v[164:167], v[140:143], v[220:223], v[164:167]\n" \
  "v_mfma_f32_16x16x32_bf16 v[168:171], v[144:147], v[192:195], 0\n" \
  "v_mfma_f32_16x16x32_bf16 v[172:175], v[144:147], v[208:211], 0\n" \
  "v_mfma_f32_16x16x32_bf16 v[168:171], v[148:151], v[196:199], v[168:171]\n" \
  "v_mfma_f32_16x16x32_bf16 v[172:175], v[148:151], v[212:215], v[172:175]\n" \
  "v_mfma_f32_16x16x32_bf16 v[168:171], v[152:155], v[200:203], v[168:171]\n" \
  "v_mfma_f32_16x16x32_bf16 v[172:175], v[152:155], v[216:219], v[172:175]\n" \
  "v_mfma_f32_16x16x32_bf16 v[168:171], v[156:159], v[204:207], v[168:171]\n" \
  "v_mfma_f32_16x16x32_bf16 v[172:175], v[156:159], v[220:223], v[172:175]\n" \
  "v_cvt_pk_bf16_f32 v52, v160, v161\n" \
  "v_cvt_pk_bf16_f32 v53, v162, v163\n" \
  "v_cvt_pk_bf16_f32 v116, v164, v165\n" \
  "v_cvt_pk_bf16_f32 v117, v166, v167\n" \
  "v_cvt_pk_bf16_f32 v54, v168, v169\n" \
  "v_cvt_pk_bf16_f32 v55, v170, v171\n" \
  "s_nop 1\n" \
  "v_cvt_pk_bf16_f32 v118, v172, v173\n" \
  "v_cvt_pk_bf16_f32 v119, v174, v175\n" \
  "s_waitcnt lgkmcnt(0)\n" \
  "s_waitcnt vmcnt(0)\n" \
  "s_barrier\n" \
  "v_add_u32 v188, 49152, v181\n" \
  "v_add_u32 v189, 49152, v182\n" \
  "v_add_u32 v190, 49152, v183\n" \
  "v_add_u32 v191, 49152, v184\n" \
  "v_add_u32 v187, 49152, v178\n" \
  "ds_read_b128 v[128:131], v188\n" \
  "ds_read_b128 v[132:135], v189\n" \
  "ds_read_b128 v[136:139], v190\n" \
  "ds_read_b128 v[140:143], v191\n" \
  "ds_read_b128 v[144:147], v188 offset:12288\n" \
  "ds_read_b128 v[148:151], v189 offset:12288\n" \
  "ds_read_b128 v[152:155], v190 offset:12288\n" \
  "ds_read_b128 v[156:159], v191 offset:12288\n" \
  "ds_read_b64_tr_b16 a[56:57], v187\n" \
  "ds_read_b64_tr_b16 a[58:59], v187 offset:1024\n" \
  "ds_read_b64_tr_b16 a[60:61], v187 offset:2048\n" \
  "ds_read_b64_tr_b16 a[62:63], v187 offset:3072\n" \
  "ds_read_b64_tr_b16 a[120:121], v187 offset:8\n" \
  "ds_read_b64_tr_b16 a[122:123], v187 offset:1032\n" \
  "s_waitcnt lgkmcnt(6)\n" \
  "ds_read_b64_tr_b16 a[124:125], v187 offset:2056\n" \
  "ds_read_b64_tr_b16 a[126:127], v187 offset:3080\n" \
  "s_waitcnt lgkmcnt(8)\n" \
  "v_mfma_f32_16x16x32_bf16 v[160:163], v[128:131], v[192:195], 0\n" \
  "v_mfma_f32_16x16x32_bf16 v[164:167], v[128:131], v[208:211], 0\n" \
  "v_mfma_f32_16x16x32_bf16 v[160:163], v[132:135], v[196:199], v[160:163]\n" \
  "v_mfma_f32_16x16x32_bf16 v[164:167], v[132:135], v[212:215], v[164:167]\n" \
  "v_mfma_f32_16x16x32_bf16 v[160:163], v[136:139], v[200:203], v[160:163]\n" \
  "v_mfma_f32_16x16x32_bf16 v[164:167], v[136:139], v[216:219], v[164:167]\n" \
  "v_mfma_f32_16x16x32_bf16 v[160:163], v[140:143], v[204:207], v[160:163]\n" \
  "v_mfma_f32_16x16x32_bf16 v[164:167], v[140:143], v[220:223], v[164:167]\n" \
  "v_mfma_f32_16x16x32_bf16 v[168:171], v[144:147], v[192:195], 0\n" \
  "v_mfma_f32_16x16x32_bf16 v[172:175], v[144:147], v[208:211], 0\n" \
  "v_mfma_f32_16x16x32_bf16 v[168:171], v[148:151], v[196:199], v[168:171]\n" \
  "v_mfma_f32_16x16x32_bf16 v[172:175], v[148:151], v[212:215], v[172:175]\n" \
  "v_mfma_f32_16x16x32_bf16 v[168:171], v[152:155], v[200:203], v[168:171]\n" \
  "v_mfma_f32_16x16x32_bf16 v[172:175], v[152:155], v[216:219], v[172:175]\n" \
  "v_mfma_f32_16x16x32_bf16 v[168:171], v[156:159], v[204:207], v[168:171]\n" \
  "v_mfma_f32_16x16x32_bf16 v[172:175], v[156:159], v[220:223], v[172:175]\n" \
  "v_cvt_pk_bf16_f32 v56, v160, v161\n" \
  "v_cvt_pk_bf16_f32 v57, v162, v163\n" \
  "v_cvt_pk_bf16_f32 v120, v164, v165\n" \
  "v_cvt_pk_bf16_f32 v121, v166, v167\n" \
  "v_cvt_pk_bf16_f32 v58, v168, v169\n" \
  "v_cvt_pk_bf16_f32 v59, v170, v171\n" \
  "s_nop 1\n" \
  "v_cvt_pk_bf16_f32 v122, v172, v173\n" \
  "v_cvt_pk_bf16_f32 v123, v174, v175\n" \
  "s_waitcnt lgkmcnt(0)\n" \
  "ds_read_b128 v[128:131], v188 offset:24576\n" \
  "ds_read_b128 v[132:135], v189 offset:24576\n" \
  "ds_read_b128 v[136:139], v190 offset:24576\n" \
  "ds_read_b128 v[140:143], v191 offset:24576\n" \
  "ds_read_b128 v[144:147], v188 offset:36864\n" \
  "ds_read_b128 v[148:151], v189 offset:36864\n" \
  "ds_read_b128 v[152:155], v190 offset:36864\n" \
  "ds_read_b128 v[156:159], v191 offset:36864\n" \
  "ds_read_b64_tr_b16 a[184:185], v187 offset:4096\n" \
  "ds_read_b64_tr_b16 a[186:187], v187 offset:5120\n" \
  "ds_read_b64_tr_b16 a[188:189], v187 offset:6144\n" \
  "ds_read_b64_tr_b16 a[190:191], v187 offset:7168\n" \
  "ds_read_b64_tr_b16 a[248:249], v187 offset:4104\n" \
  "ds_read_b64_tr_b16 a[250:251], v187 offset:5128\n" \
  "s_waitcnt lgkmcnt(6)\n" \
  "ds_read_b64_tr_b16 a[252:253], v187 offset:6152\n" \
  "ds_read_b64_tr_b16 a[254:255], v187 offset:7176\n" \
  "s_waitcnt lgkmcnt(8)\n" \
  "v_mfma_f32_16x16x32_bf16 v[160:163], v[128:131], v[192:195], 0\n" \
  "v_mfma_f32_16x16x32_bf16 v[164:167], v[128:131], v[208:211], 0\n" \
  "v_mfma_f32_16x16x32_bf16 v[160:163], v[132:135], v[196:199], v[160:163]\n" \
  "v_mfma_f32_16x16x32_bf16 v[164:167], v[132:135], v[212:215], v[164:167]\n" \
  "v_mfma_f32_16x16x32_bf16 v[160:163], v[136:139], v[200:203], v[160:163]\n" \
  "v_mfma_f32_16x16x32_bf16 v[164:167], v[136:139], v[216:219], v[164:167]\n" \
  "v_mfma_f32_16x16x32_bf16 v[160:163], v[140:143], v[204:207], v[160:163]\n" \
  "v_mfma_f32_16x16x32_bf16 v[164:167], v[140:143], v[220:223], v[164:167]\n" \
  "v_mfma_f32_16x16x32_bf16 v[168:171], v[144:147], v[192:195], 0\n" \
  "v_mfma_f32_16x16x32_bf16 v[172:175], v[144:147], v[208:211], 0\n" \
  "v_mfma_f32_16x16x32_bf16 v[168:171], v[148:151], v[196:199], v[168:171]\n" \
  "v_mfma_f32_16x16x32_bf16 v[172:175], v[148:151], v[212:215], v[172:175]\n" \
  "v_mfma_f32_16x16x32_bf16 v[168:171], v[152:155], v[200:203], v[168:171]\n" \
  "v_mfma_f32_16x16x32_bf16 v[172:175], v[152:155], v[216:219], v[172:175]\n" \
  "v_mfma_f32_16x16x32_bf16 v[168:171], v[156:159], v[204:207], v[168:171]\n" \
  "v_mfma_f32_16x16x32_bf16 v[172:175], v[156:159], v[220:223], v[172:175]\n" \
  "v_cvt_pk_bf16_f32 v60, v160, v161\n" \
  "v_cvt_pk_bf16_f32 v61, v162, v163\n" \
  "v_cvt_pk_bf16_f32 v124, v164, v165\n" \
  "v_cvt_pk_bf16_f32 v125, v166, v167\n" \
  "v_cvt_pk_bf16_f32 v62, v168, v169\n" \
  "v_cvt_pk_bf16_f32 v63, v170, v171\n" \
  "s_nop 1\n" \
  "v_cvt_pk_bf16_f32 v126, v172, v173\n" \
  "v_cvt_pk_bf16_f32 v127, v174, v175\n" \
  "s_waitcnt lgkmcnt(0)\n" \
  "s_barrier\n" \
  "s_mov_b32 s82, 0\n" \
  "s_mov_b32 s83, 0\n" \
  "s_mov_b32 s84, 0\n" \
  "s_mov_b32 s85, 0\n" \
  "s_mov_b32 s86, 0\n" \
  "s_mov_b32 s87, 0\n" \
  "s_mul_i32 s73, s56, 11264\n" \
  "s_add_u32 s73, s73, 65536\n" \
  "v_add_u32 v242, s73, v241\n" \
  "ds_write_b128 v242, v[192:195] offset:3072\n" \
  "ds_write_b128 v242, v[196:199] offset:4096\n" \
  "ds_write_b128 v242, v[200:203] offset:5120\n" \
  "ds_write_b128 v242, v[204:207] offset:6144\n" \
  "ds_write_b128 v242, v[208:211] offset:7168\n" \
  "ds_write_b128 v242, v[212:215] offset:8192\n" \
  "ds_write_b128 v242, v[216:219] offset:9216\n" \
  "ds_write_b128 v242, v[220:223] offset:10240\n" \
  "s_waitcnt lgkmcnt(0)\n" \
  "Llayer_%=:\n" \
  "v_mbcnt_lo_u32_b32 v239, -1, 0\n" \
  "v_mbcnt_hi_u32_b32 v239, -1, v239\n" \
  "v_and_b32 v195, 7, v239\n" \
  "v_bfe_u32 v196, v239, 3, 1\n" \
  "v_lshrrev_b32 v197, 4, v239\n" \
  "v_and_b32 v198, 15, v239\n" \
  "v_lshlrev_b32 v199, 10, v196\n" \
  "v_add_u32 v243, v241, v199\n" \
  "v_add_u32 v243, 65536, v243\n" \
  "v_lshlrev_b32 v199, 3, v196\n" \
  "v_add_u32 v244, v241, v199\n" \
  "v_add_u32 v244, 67584, v244\n" \
  "v_lshlrev_b32 v245, 4, v198\n" \
  "v_lshl_add_u32 v245, v197, 9, v245\n" \
  "s_lshl_b32 s64, s56, 11\n" \
  "s_add_u32 s72, s64, 16384\n" \
  "v_add_u32 v245, s72, v245\n" \
  "s_lshl_b32 s72, s56, 2\n" \
  "v_or_b32 v200, s72, v197\n" \
  "s_and_b32 s72, s21, 1\n" \
  "s_lshl_b32 s74, s72, 4\n" \
  "s_xor_b32 s74, s74, s20\n" \
  "v_xor_b32 v201, s74, v200\n" \
  "s_lshr_b32 s74, s21, 1\n" \
  "s_lshl_b32 s74, s74, 4\n" \
  "v_or_b32 v202, s74, v200\n" \
  "v_lshlrev_b32 v246, 15, v202\n" \
  "v_bfe_u32 v199, v202, 3, 1\n" \
  "v_lshl_or_b32 v246, v199, 11, v246\n" \
  "v_bfe_u32 v199, v202, 4, 1\n" \
  "v_lshl_or_b32 v246, v199, 14, v246\n" \
  "s_lshl_b32 s72, s72, 14\n" \
  "s_and_b32 s74, s12, 26624\n" \
  "s_xor_b32 s72, s72, s74\n" \
  "v_xor_b32 v246, s72, v246\n" \
  "v_lshrrev_b32 v199, 4, v201\n" \
  "v_lshl_or_b32 v246, v199, 12, v246\n" \
  "v_and_b32 v199, 15, v201\n" \
  "v_lshl_or_b32 v246, v199, 7, v246\n" \
  "v_lshl_or_b32 v246, v195, 4, v246\n" \
  "v_mov_b32 v240, 0x4444\n" \
  "v_mul_u32_u24 v199, 0xaaaa, v196\n" \
  "v_add_u32 v240, v240, v199\n" \
  "v_mov_b32 v248, 0\n" \
  "v_mov_b32 v249, 0\n" \
  "v_mov_b32 v252, 0\n" \
  "v_mov_b32 v253, 0\n" \
  "v_and_b32 v199, 1, v197\n" \
  "v_lshlrev_b32 v199, 2, v199\n" \
  "v_sub_u32 v199, v195, v199\n" \
  "v_mov_b32 v250, 0\n" \
  "v_cmp_eq_u32 vcc, 0, v199\n" \
  "v_mov_b32 v201, 0x3f80\n" \
  "s_nop 0\n" \
  "v_cndmask_b32 v250, v250, v201, vcc\n" \
  "v_cmp_eq_u32 vcc, 1, v199\n" \
  "v_mov_b32 v201, 0x3f800000\n" \
  "s_nop 0\n" \
  "v_cndmask_b32 v250, v250, v201, vcc\n" \
  "v_mov_b32 v251, 0\n" \
  "v_cmp_eq_u32 vcc, 2, v199\n" \
  "v_mov_b32 v201, 0x3f80\n" \
  "s_nop 0\n" \
  "v_cndmask_b32 v251, v251, v201, vcc\n" \
  "v_cmp_eq_u32 vcc, 3, v199\n" \
  "v_mov_b32 v201, 0x3f800000\n" \
  "s_nop 0\n" \
  "v_cndmask_b32 v251, v251, v201, vcc\n" \
  "v_mov_b32 v204, 0\n" \
  "v_cmp_eq_u32 vcc, 0, v195\n" \
  "v_mov_b32 v201, 0x3f80\n" \
  "s_nop 0\n" \
  "v_cndmask_b32 v204, v204, v201, vcc\n" \
  "v_cmp_eq_u32 vcc, 1, v195\n" \
  "v_mov_b32 v201, 0x3f800000\n" \
  "s_nop 0\n" \
  "v_cndmask_b32 v204, v204, v201, vcc\n" \
  "v_mov_b32 v205, 0\n" \
  "v_cmp_eq_u32 vcc, 2, v195\n" \
  "v_mov_b32 v201, 0x3f80\n" \
  "s_nop 0\n" \
  "v_cndmask_b32 v205, v205, v201, vcc\n" \
  "v_cmp_eq_u32 vcc, 3, v195\n" \
  "v_mov_b32 v201, 0x3f800000\n" \
  "s_nop 0\n" \
  "v_cndmask_b32 v205, v205, v201, vcc\n" \
  "v_mov_b32 v206, 0\n" \
  "v_cmp_eq_u32 vcc, 4, v195\n" \
  "v_mov_b32 v201, 0x3f80\n" \
  "s_nop 0\n" \
  "v_cndmask_b32 v206, v206, v201, vcc\n" \
  "v_cmp_eq_u32 vcc, 5, v195\n" \
  "v_mov_b32 v201, 0x3f800000\n" \
  "s_nop 0\n" \
  "v_cndmask_b32 v206, v206, v201, vcc\n" \
  "v_mov_b32 v207, 0\n" \
  "v_cmp_eq_u32 vcc, 6, v195\n" \
  "v_mov_b32 v201, 0x3f80\n" \
  "s_nop 0\n" \
  "v_cndmask_b32 v207, v207, v201, vcc\n" \
  "v_cmp_eq_u32 vcc, 7, v195\n" \
  "v_mov_b32 v201, 0x3f800000\n" \
  "s_nop 0\n" \
  "v_cndmask_b32 v207, v207, v201, vcc\n" \
  "ds_write_b128 v241, v[204:207] offset:8192\n" \
  "s_mov_b32 s58, 0\n" \
  "s_mov_b64 s[76:77], 0\n" \
  "s_cmp_lt_i32 s22, 0\n" \
  "s_cbranch_scc1 Lnr_%=\n" \
  "s_cmp_lg_u32 s56, 0\n" \
  "s_cbranch_scc1 Lnr_%=\n" \
  "s_mov_b32 s58, 1\n" \
  "Lnr_%=:\n" \
  "s_cmp_eq_u32 s22, 0\n" \
  "s_cselect_b32 s72, 2, 0\n" \
  "s_cmp_ge_u32 s56, 2\n" \
  "s_cselect_b32 s72, s72, 0\n" \
  "s_or_b32 s58, s58, s72\n" \
  "s_max_i32 s72, s22, 0\n" \
  "s_lshr_b32 s74, s72, 5\n" \
  "s_lshl_b32 s74, s74, 4\n" \
  "s_and_b32 s75, s72, 15\n" \
  "s_or_b32 s74, s74, s75\n" \
  "s_lshl_b32 s90, s74, 15\n" \
  "s_bfe_u32 s75, s74, 65539\n" \
  "s_lshl_b32 s75, s75, 11\n" \
  "s_or_b32 s90, s90, s75\n" \
  "s_bfe_u32 s75, s74, 65540\n" \
  "s_bfe_u32 s74, s72, 65540\n" \
  "s_xor_b32 s75, s75, s74\n" \
  "s_lshl_b32 s75, s75, 14\n" \
  "s_or_b32 s90, s90, s75\n" \
  "s_xor_b32 s48, s44, s90\n" \
  "s_mov_b32 s49, s45\n" \
  "s_add_u32 s48, s48, 2048\n" \
  "s_addc_u32 s49, s49, 0\n" \
  "s_andn2_b32 s46, s44, 26624\n" \
  "s_mov_b32 s47, s45\n" \
  "s_and_b32 s74, s29, 1\n" \
  "s_lshl_b32 s74, s74, 20\n" \
  "s_xor_b32 s48, s48, s74\n" \
  "s_xor_b32 s46, s46, s74\n" \
  "s_xor_b32 s44, s44, 2048\n" \
  "s_lshl_b32 s74, s29, 2\n" \
  "s_add_u32 s44, s44, s74\n" \
  "s_addc_u32 s45, s45, 0\n" \
  "s_sub_u32 s74, s30, s44\n" \
  "v_mov_b32 v247, s74\n" \
  "s_add_u32 s75, s74, 32768\n" \
  "v_writelane_b32 v247, s75, 2\n" \
  "s_add_u32 s75, s74, 36864\n" \
  "v_writelane_b32 v247, s75, 3\n" \
  "s_mov_b32 s66, 1\n" \
  "s_cmp_lt_u32 s56, 2\n" \
  "s_cselect_b32 s89, 0, 15\n" \
  "s_lshr_b32 s74, s72, 5\n" \
  "s_lshl_b32 s39, s74, 12\n" \
  "s_bfe_u32 s74, s72, 65540\n" \
  "s_lshl_b32 s74, s74, 15\n" \
  "s_or_b32 s39, s39, s74\n" \
  "s_and_b32 s74, s72, 15\n" \
  "s_lshl_b32 s74, s74, 7\n" \
  "s_or_b32 s39, s39, s74\n" \
  "s_bfe_u32 s38, s20, 65539\n" \
  "s_lshl_b32 s38, s38, 14\n" \
  "s_cmp_lt_u32 s56, 2\n" \
  "s_cbranch_scc1 Lnl_%=\n" \
  "s_and_b32 s39, s56, 1\n" \
  "s_lshl_b32 s39, s39, 15\n" \
  "s_or_b32 s39, s39, s38\n" \
  "Lnl_%=:\n" \
  "s_and_b32 s72, s98, 31\n" \
  "s_lshl_b32 s74, s72, 16\n" \
  "s_bfe_u32 s75, s72, 65538\n" \
  "s_lshl_b32 s75, s75, 11\n" \
  "s_or_b32 s74, s74, s75\n" \
  "s_bfe_u32 s75, s72, 65539\n" \
  "s_lshl_b32 s75, s75, 14\n" \
  "s_or_b32 s74, s74, s75\n" \
  "s_xor_b32 s74, s74, s39\n" \
  "s_cmp_lt_u32 s98, 32\n" \
  "s_cselect_b64 s[52:53], s[34:35], s[36:37]\n" \
  "s_xor_b32 s52, s52, s74\n" \
  "s_lshl_b32 s75, s56, 7\n" \
  "s_add_u32 s50, s16, s75\n" \
  "s_addc_u32 s51, s17, 0\n" \
  "s_lshl_b32 s93, s56, 12\n" \
  "s_add_u32 s40, s40, s93\n" \
  "s_addc_u32 s41, s41, 0\n" \
  "s_add_u32 s93, s93, 16384\n" \
  "s_mov_b32 s63, s93\n" \
  "s_mov_b32 s91, 0\n" \
  "s_mov_b32 s92, 16384\n" \
  "v_lshrrev_b32 v199, 3, v239\n" \
  "v_lshlrev_b32 v199, 4, v199\n" \
  "v_lshl_add_u32 v199, v195, 10, v199\n" \
  "s_lshl_b32 s72, s56, 8\n" \
  "s_mov_b64 s[70:71], s[14:15]\n" \
  "s_add_u32 s70, s70, s72\n" \
  "s_addc_u32 s71, s71, 0\n" \
  "s_lshl_b32 m0, s56, 11\n" \
  "s_nop 0\n" \
  "global_load_lds_dwordx4 v199, s[70:71]\n" \
  "s_add_u32 s70, s70, 128\n" \
  "s_addc_u32 s71, s71, 0\n" \
  "s_lshl_b32 s72, s56, 11\n" \
  "s_add_u32 m0, s72, 1024\n" \
  "s_nop 0\n" \
  "global_load_lds_dwordx4 v199, s[70:71]\n" \
  "s_cmp_eq_u32 s56, 0\n" \
  "s_cbranch_scc0 Lkw_%=\n" \
  "s_lshl_b32 s72, s20, 7\n" \
  "s_sub_u32 s70, s30, s72\n" \
  "s_subb_u32 s71, s31, 0\n" \
  "v_and_b32 v203, 31, v239\n" \
  "v_lshlrev_b32 v203, 7, v203\n" \
  "v_lshlrev_b32 v204, 7, v239\n" \
  "v_add_u32 v204, 32768, v204\n" \
  "Lst_%=:\n" \
  "global_load_dword v238, v204, s[70:71] sc1\n" \
  "global_load_dword v237, v203, s[70:71] sc1\n" \
  "s_waitcnt vmcnt(0)\n" \
  "v_min_u32 v238, v238, v237\n" \
  "v_cmp_gt_u32 vcc, 1, v238\n" \
  "s_nop 0\n" \
  "s_cbranch_vccz Lkw_%=\n" \
  "s_sleep 2\n" \
  "s_sub_u32 s61, s61, 1\n" \
  "s_cmp_gt_i32 s61, 0\n" \
  "s_cbranch_scc1 Lst_%=\n" \
  "Lkw_%=:\n" \
  "s_barrier\n" \
  "s_mov_b32 m0, s63\n" \
  "s_nop 0\n" \
  "global_load_lds_dwordx4 v241, s[40:41] sc1\n" \
  "global_load_lds_dwordx4 v241, s[40:41] offset:1024 sc1\n" \
  "global_load_lds_dwordx4 v241, s[40:41] offset:2048 sc1\n" \
  "global_load_lds_dwordx4 v241, s[40:41] offset:3072 sc1\n" \
  "s_add_u32 s63, s63, 16384\n" \
  "s_add_u32 s40, s40, 16384\n" \
  "s_addc_u32 s41, s41, 0\n" \
  "s_mov_b32 m0, s63\n" \
  "s_nop 0\n" \
  "global_load_lds_dwordx4 v241, s[40:41] sc1\n" \
  "global_load_lds_dwordx4 v241, s[40:41] offset:1024 sc1\n" \
  "global_load_lds_dwordx4 v241, s[40:41] offset:2048 sc1\n" \
  "global_load_lds_dwordx4 v241, s[40:41] offset:3072 sc1\n" \
  "s_add_u32 s63, s63, 16384\n" \
  "s_add_u32 s40, s40, 16384\n" \
  "s_addc_u32 s41, s41, 0\n" \
  "s_waitcnt vmcnt(4) lgkmcnt(0)\n" \
  "s_barrier\n" \
  "v_subrev_u32 v164, s64, v245\n" \
  "ds_read_b128 v[128:131], v164 offset:8192\n" \
  "ds_read_b128 v[144:147], v164 offset:8448\n" \
  "ds_read_b128 v[192:195], v242 offset:3072\n" \
  "ds_read_b128 v[208:211], v242 offset:7168\n" \
  "ds_read_b128 v[132:135], v164 offset:10240\n" \
  "ds_read_b128 v[148:151], v164 offset:10496\n" \
  "ds_read_b128 v[196:199], v242 offset:4096\n" \
  "ds_read_b128 v[212:215], v242 offset:8192\n" \
  "ds_read_b128 v[136:139], v164 offset:12288\n" \
  "ds_read_b128 v[152:155], v164 offset:12544\n" \
  "ds_read_b128 v[200:203], v242 offset:5120\n" \
  "ds_read_b128 v[216:219], v242 offset:9216\n" \
  "s_waitcnt lgkmcnt(8)\n" \
  "ds_read_b128 v[140:143], v164 offset:14336\n" \
  "ds_read_b128 v[156:159], v164 offset:14592\n" \
  "ds_read_b128 v[204:207], v242 offset:6144\n" \
  "ds_read_b128 v[220:223], v242 offset:10240\n" \
  "v_mfma_f32_16x16x32_bf16 v[160:163], v[128:131], v[192:195], 0\n" \
  "v_mfma_f32_16x16x32_bf16 v[164:167], v[128:131], v[208:211], 0\n" \
  "v_mfma_f32_16x16x32_bf16 v[168:171], v[144:147], v[192:195], 0\n" \
  "v_mfma_f32_16x16x32_bf16 v[172:175], v[144:147], v[208:211], 0\n" \
  "s_waitcnt lgkmcnt(8)\n" \
  "v_mfma_f32_16x16x32_bf16 v[160:163], v[132:135], v[196:199], v[160:163]\n" \
  "v_mfma_f32_16x16x32_bf16 v[164:167], v[132:135], v[212:215], v[164:167]\n" \
  "v_mfma_f32_16x16x32_bf16 v[168:171], v[148:151], v[196:199], v[168:171]\n" \
  "v_mfma_f32_16x16x32_bf16 v[172:175], v[148:151], v[212:215], v[172:175]\n" \
  "s_waitcnt lgkmcnt(4)\n" \
  "v_mfma_f32_16x16x32_bf16 v[160:163], v[136:139], v[200:203], v[160:163]\n" \
  "v_mfma_f32_16x16x32_bf16 v[164:167], v[136:139], v[216:219], v[164:167]\n" \
  "v_mfma_f32_16x16x32_bf16 v[168:171], v[152:155], v[200:203], v[168:171]\n" \
  "v_mfma_f32_16x16x32_bf16 v[172:175], v[152:155], v[216:219], v[172:175]\n" \
  "s_waitcnt lgkmcnt(0)\n" \
  "v_mfma_f32_16x16x32_bf16 v[160:163], v[140:143], v[204:207], v[160:163]\n" \
  "v_mfma_f32_16x16x32_bf16 v[164:167], v[140:143], v[220:223], v[164:167]\n" \
  "v_mfma_f32_16x16x32_bf16 v[168:171], v[156:159], v[204:207], v[168:171]\n" \
  "v_mfma_f32_16x16x32_bf16 v[172:175], v[156:159], v[220:223], v[172:175]\n" \
  "v_cvt_pk_bf16_f32 v184, v160, v161\n" \
  "v_cvt_pk_bf16_f32 v185, v162, v163\n" \
  "s_nop 1\n" \
  "v_cvt_pk_bf16_f32 v186, v168, v169\n" \
  "v_cvt_pk_bf16_f32 v187, v170, v171\n" \
  "v_cvt_pk_bf16_f32 v188, v164, v165\n" \
  "v_cvt_pk_bf16_f32 v189, v166, v167\n" \
  "v_cvt_pk_bf16_f32 v190, v172, v173\n" \
  "v_cvt_pk_bf16_f32 v191, v174, v175\n" \
  "v_mov_b32 v160, 0\n" \
  "v_mov_b32 v161, 0\n" \
  "v_mov_b32 v162, 0\n" \
  "v_mov_b32 v163, 0\n" \
  "v_mov_b32 v164, 0\n" \
  "v_mov_b32 v165, 0\n" \
  "v_mov_b32 v166, 0\n" \
  "v_mov_b32 v167, 0\n" \
  "v_mov_b32 v168, 0\n" \
  "v_mov_b32 v169, 0\n" \
  "v_mov_b32 v170, 0\n" \
  "v_mov_b32 v171, 0\n" \
  "v_mov_b32 v172, 0\n" \
  "v_mov_b32 v173, 0\n" \
  "v_mov_b32 v174, 0\n" \
  "v_mov_b32 v175, 0\n" \
  "v_mov_b32 v176, 0\n" \
  "v_mov_b32 v177, 0\n" \
  "v_mov_b32 v178, 0\n" \
  "v_mov_b32 v179, 0\n" \
  "v_mov_b32 v180, 0\n" \
  "v_mov_b32 v181, 0\n" \
  "v_mov_b32 v182, 0\n" \
  "v_mov_b32 v183, 0\n" \
  "s_mov_b64 s[94:95], s[52:53]\n" \
  "s_memrealtime s[26:27]\n" \
  "s_waitcnt lgkmcnt(0)\n" \
  "s_and_b32 s99, s26, -32\n" \
  "s_sub_u32 s99, s99, s60\n" \
  "s_sub_u32 s99, s99, 4096\n" \
  "s_memtime s[4:5]\n" \
  "s_waitcnt lgkmcnt(0)\n" \
  "v_mov_b32 v192, s99\n" \
  "v_mov_b32 v193, s4\n" \
  "ds_write_b64 v248, v[192:193] offset:9216\n" \
  "s_waitcnt lgkmcnt(0)\n" \
  "s_nop 7\n" \
  "s_nop 1\n" \
  "s_waitcnt vmcnt(0)\n" \
  "Ltop_%=:\n" \
  "s_waitcnt vmcnt(1)\n" \
  "s_barrier\n" \
  "ds_read_b128 v[128:131], v241\n" \
  "ds_read_b128 v[132:135], v241 offset:1024\n" \
  "ds_read_b128 v[136:139], v241 offset:2048\n" \
  "s_waitcnt lgkmcnt(2)\n" \
  "v_smfmac_f32_16x16x64_bf16 v[160:163], v[128:131], a[0:7], v240\n" \
  "ds_read_b128 v[140:143], v241 offset:3072\n" \
  "v_smfmac_f32_16x16x64_bf16 v[164:167], v[128:131], a[64:71], v240\n" \
  "ds_read_b128 v[144:147], v241 offset:4096\n" \
  "s_waitcnt lgkmcnt(3)\n" \
  "v_smfmac_f32_16x16x64_bf16 v[160:163], v[132:135], a[8:15], v240\n" \
  "ds_read_b128 v[148:151], v241 offset:5120\n" \
  "v_smfmac_f32_16x16x64_bf16 v[164:167], v[132:135], a[72:79], v240\n" \
  "ds_read_b128 v[152:155], v241 offset:6144\n" \
  "s_waitcnt lgkmcnt(4)\n" \
  "v_smfmac_f32_16x16x64_bf16 v[160:163], v[136:139], a[16:23], v240\n" \
  "ds_read_b128 v[156:159], v241 offset:7168\n" \
  "v_smfmac_f32_16x16x64_bf16 v[164:167], v[136:139], a[80:87], v240\n" \
  "ds_read_b128 v[224:227], v245\n" \
  "s_waitcnt lgkmcnt(5)\n" \
  "v_smfmac_f32_16x16x64_bf16 v[160:163], v[140:143], a[24:31], v240\n" \
  "ds_read_b128 v[228:231], v245 offset:256\n" \
  "v_smfmac_f32_16x16x64_bf16 v[164:167], v[140:143], a[88:95], v240\n" \
  "ds_read_b64 v[216:217], v248 offset:9216\n" \
  "s_waitcnt lgkmcnt(6)\n" \
  "v_smfmac_f32_16x16x64_bf16 v[160:163], v[144:147], a[32:39], v240\n" \
  "v_max3_u32 v232, v128, v133, v138\n" \
  "v_smfmac_f32_16x16x64_bf16 v[164:167], v[144:147], a[96:103], v240\n" \
  "v_max3_u32 v232, v232, v143, v144\n" \
  "s_waitcnt lgkmcnt(5)\n" \
  "v_smfmac_f32_16x16x64_bf16 v[160:163], v[148:151], a[40:47], v240\n" \
  "s_lshl_b32 s72, s98, 2\n" \
  "v_writelane_b32 v247, s72, 0\n" \
  "v_smfmac_f32_16x16x64_bf16 v[164:167], v[148:151], a[104:111], v240\n" \
  "s_mov_b32 m0, s63\n" \
  "s_waitcnt lgkmcnt(4)\n" \
  "v_smfmac_f32_16x16x64_bf16 v[160:163], v[152:155], a[48:55], v240\n" \
  "v_max3_u32 v232, v232, v149, v154\n" \
  "v_smfmac_f32_16x16x64_bf16 v[164:167], v[152:155], a[112:119], v240\n" \
  "s_waitcnt lgkmcnt(3)\n" \
  "v_smfmac_f32_16x16x64_bf16 v[160:163], v[156:159], a[56:63], v240\n" \
  "v_max_u32 v232, v232, v159\n" \
  "v_smfmac_f32_16x16x64_bf16 v[164:167], v[156:159], a[120:127], v240\n" \
  "v_cmp_eq_u32 vcc, -1, v232\n" \
  "v_smfmac_f32_16x16x64_bf16 v[168:171], v[128:131], a[128:135], v240\n" \
  "s_cbranch_vccnz Lfail_%=\n" \
  "Lfcont_%=:\n" \
  "v_cvt_pk_bf16_f32 v192, v160, v161\n" \
  "v_smfmac_f32_16x16x64_bf16 v[168:171], v[132:135], a[136:143], v240\n" \
  "v_cvt_pk_bf16_f32 v193, v162, v163\n" \
  "v_smfmac_f32_16x16x64_bf16 v[168:171], v[136:139], a[144:151], v240\n" \
  "v_cvt_pk_bf16_f32 v194, v164, v165\n" \
  "v_smfmac_f32_16x16x64_bf16 v[168:171], v[140:143], a[152:159], v240\n" \
  "v_cvt_pk_bf16_f32 v195, v166, v167\n" \
  "v_smfmac_f32_16x16x64_bf16 v[168:171], v[144:147], a[160:167], v240\n" \
  "v_smfmac_f32_16x16x64_bf16 v[168:171], v[148:151], a[168:175], v240\n" \
  "v_smfmac_f32_16x16x64_bf16 v[168:171], v[152:155], a[176:183], v240\n" \
  "v_smfmac_f32_16x16x64_bf16 v[168:171], v[156:159], a[184:191], v240\n" \
  "s_waitcnt lgkmcnt(0)\n" \
  "v_smfmac_f32_16x16x64_bf16 v[172:175], v[128:131], a[192:199], v240\n" \
  "v_mov_b32 v238, 0x3f003f00\n" \
  "v_smfmac_f32_16x16x64_bf16 v[172:175], v[132:135], a[200:207], v240\n" \
  "v_cvt_pk_bf16_f32 v196, v168, v169\n" \
  "v_smfmac_f32_16x16x64_bf16 v[172:175], v[136:139], a[208:215], v240\n" \
  "v_cvt_pk_bf16_f32 v197, v170, v171\n" \
  "v_smfmac_f32_16x16x64_bf16 v[172:175], v[140:143], a[216:223], v240\n" \
  "v_mov_b32 v239, 0x3f003f00\n" \
  "v_smfmac_f32_16x16x64_bf16 v[172:175], v[144:147], a[224:231], v240\n" \
  "v_add_u32 v245, s92, v245\n" \
  "v_smfmac_f32_16x16x64_bf16 v[172:175], v[148:151], a[232:239], v240\n" \
  "v_mov_b32 v234, 0x3f803f80\n" \
  "v_smfmac_f32_16x16x64_bf16 v[172:175], v[152:155], a[240:247], v240\n" \
  "v_readfirstlane_b32 s99, v216\n" \
  "v_smfmac_f32_16x16x64_bf16 v[172:175], v[156:159], a[248:255], v240\n" \
  "v_readfirstlane_b32 s97, v217\n" \
  "v_mfma_f32_16x16x32_bf16 v[200:203], v[192:195], v[250:253], 0\n" \
  "v_mfma_f32_16x16x32_bf16 v[204:207], v[192:195], v[248:251], 0\n" \
  "v_mfma_f32_16x16x32_bf16 v[208:211], v[196:199], v[250:253], 0\n" \
  "v_cvt_pk_bf16_f32 v198, v172, v173\n" \
  "v_cvt_pk_bf16_f32 v199, v174, v175\n" \
  "s_nop 1\n" \
  "v_mfma_f32_16x16x32_bf16 v[212:215], v[196:199], v[248:251], 0\n" \
  "v_cvt_pk_bf16_f32 v220, v200, v201\n" \
  "v_cvt_pk_bf16_f32 v221, v202, v203\n" \
  "v_smfmac_f32_16x16x64_bf16 v[176:179], v[128:131], v[0:7], v240\n" \
  "v_cvt_pk_bf16_f32 v222, v204, v205\n" \
  "v_smfmac_f32_16x16x64_bf16 v[180:183], v[128:131], v[64:71], v240\n" \
  "v_cvt_pk_bf16_f32 v223, v206, v207\n" \
  "v_cvt_pk_bf16_f32 v216, v208, v209\n" \
  "v_cvt_pk_bf16_f32 v217, v210, v211\n" \
  "v_mfma_f32_16x16x32_bf16 v[200:203], v[224:227], v[220:223], 0\n" \
  "v_cvt_pk_bf16_f32 v218, v212, v213\n" \
  "v_cvt_pk_bf16_f32 v219, v214, v215\n" \
  "v_mfma_f32_16x16x32_bf16 v[204:207], v[228:231], v[220:223], 0\n" \
  "v_mfma_f32_16x16x32_bf16 v[208:211], v[216:219], v[220:223], 0\n" \
  "v_smfmac_f32_16x16x64_bf16 v[176:179], v[132:135], v[8:15], v240\n" \
  "ds_write_b128 v242, v[200:203]\n" \
  "v_smfmac_f32_16x16x64_bf16 v[180:183], v[132:135], v[72:79], v240\n" \
  "ds_write_b128 v242, v[204:207] offset:1024\n" \
  "v_smfmac_f32_16x16x64_bf16 v[176:179], v[136:139], v[16:23], v240\n" \
  "ds_write_b128 v242, v[208:211] offset:2048\n" \
  "v_smfmac_f32_16x16x64_bf16 v[180:183], v[136:139], v[80:87], v240\n" \
  "v_mov_b32 v235, 0x3f803f80\n" \
  "v_smfmac_f32_16x16x64_bf16 v[176:179], v[140:143], v[24:31], v240\n" \
  "v_mov_b32 v236, 0x3f803f80\n" \
  "v_smfmac_f32_16x16x64_bf16 v[180:183], v[140:143], v[88:95], v240\n" \
  "v_mov_b32 v237, 0x3f803f80\n" \
  "s_waitcnt lgkmcnt(0)\n" \
  "s_barrier\n" \
  "ds_read_b128 v[192:195], v243\n" \
  "ds_read_b128 v[196:199], v243 offset:11264\n" \
  "ds_read_b128 v[200:203], v243 offset:22528\n" \
  "ds_read_b128 v[204:207], v243 offset:33792\n" \
  "v_smfmac_f32_16x16x64_bf16 v[176:179], v[144:147], v[32:39], v240\n" \
  "ds_read_b64 v[208:209], v244\n" \
  "v_smfmac_f32_16x16x64_bf16 v[180:183], v[144:147], v[96:103], v240\n" \
  "ds_read_b64 v[210:211], v244 offset:11264\n" \
  "v_smfmac_f32_16x16x64_bf16 v[176:179], v[148:151], v[40:47], v240\n" \
  "ds_read_b64 v[212:213], v244 offset:22528\n" \
  "v_smfmac_f32_16x16x64_bf16 v[180:183], v[148:151], v[104:111], v240\n" \
  "ds_read_b64 v[214:215], v244 offset:33792\n" \
  "v_smfmac_f32_16x16x64_bf16 v[176:179], v[152:155], v[48:55], v240\n" \
  "global_load_lds_dwordx4 v241, s[40:41] sc1\n" \
  "v_smfmac_f32_16x16x64_bf16 v[180:183], v[152:155], v[112:119], v240\n" \
  "global_load_lds_dwordx4 v241, s[40:41] offset:1024 sc1\n" \
  "v_smfmac_f32_16x16x64_bf16 v[176:179], v[156:159], v[56:63], v240\n" \
  "global_load_lds_dwordx4 v241, s[40:41] offset:2048 sc1\n" \
  "v_smfmac_f32_16x16x64_bf16 v[180:183], v[156:159], v[120:127], v240\n" \
  "global_load_lds_dwordx4 v241, s[40:41] offset:3072 sc1\n" \
  "s_waitcnt lgkmcnt(4)\n" \
  "v_pk_add_f32 v[192:193], v[192:193], v[196:197]\n" \
  "v_pk_add_f32 v[194:195], v[194:195], v[198:199]\n" \
  "v_pk_add_f32 v[200:201], v[200:201], v[204:205]\n" \
  "v_pk_add_f32 v[202:203], v[202:203], v[206:207]\n" \
  "s_waitcnt lgkmcnt(0)\n" \
  "v_pk_add_f32 v[208:209], v[208:209], v[210:211]\n" \
  "v_pk_add_f32 v[212:213], v[212:213], v[214:215]\n" \
  "v_pk_add_f32 v[192:193], v[192:193], v[200:201]\n" \
  "v_pk_add_f32 v[194:195], v[194:195], v[202:203]\n" \
  "v_pk_add_f32 v[208:209], v[208:209], v[212:213]\n" \
  "v_max3_f32 v232, v192, v193, v194\n" \
  "v_max3_f32 v232, v232, v195, v208\n" \
  "v_max_f32 v232, v232, v209\n" \
  "v_cvt_pk_bf16_f32 v220, v176, v177\n" \
  "v_cvt_pk_bf16_f32 v221, v178, v179\n" \
  "v_max_f32_dpp v232, v232, v232 row_ror:8 row_mask:0xf bank_mask:0xf\n" \
  "v_mov_b32 v233, v232\n" \
  "v_cvt_pk_bf16_f32 v222, v180, v181\n" \
  "v_cvt_pk_bf16_f32 v223, v182, v183\n" \
  "v_permlane16_swap_b32 v232, v233\n" \
  "v_max_f32 v232, v232, v233\n" \
  "v_mov_b32 v233, v232\n" \
  "s_nop 1\n" \
  "v_permlane32_swap_b32 v232, v233\n" \
  "v_max_f32 v232, v232, v233\n" \
  "v_mul_f32 v232, s28, v232\n" \
  "v_pk_fma_f32 v[192:193], v[192:193], s[62:63], v[232:233] op_sel_hi:[1,0,0]\n" \
  "v_pk_fma_f32 v[194:195], v[194:195], s[62:63], v[232:233] op_sel_hi:[1,0,0]\n" \
  "v_pk_fma_f32 v[208:209], v[208:209], s[62:63], v[232:233] op_sel_hi:[1,0,0]\n" \
  "v_exp_f32 v192, v192\n" \
  "v_exp_f32 v193, v193\n" \
  "v_exp_f32 v194, v194\n" \
  "v_exp_f32 v195, v195\n" \
  "v_exp_f32 v208, v208\n" \
  "v_exp_f32 v209, v209\n" \
  "v_cvt_pk_bf16_f32 v224, v192, v193\n" \
  "v_cvt_pk_bf16_f32 v225, v194, v195\n" \
  "v_cvt_pk_bf16_f32 v254, v208, v209\n" \
  "v_mov_b32_dpp v226, v224 row_ror:8 row_mask:0xf bank_mask:0xf\n" \
  "v_mov_b32_dpp v227, v225 row_ror:8 row_mask:0xf bank_mask:0xf\n" \
  "v_mov_b32_dpp v255, v254 row_ror:8 row_mask:0xf bank_mask:0xf\n" \
  "s_nop 0\n" \
  "v_mfma_f32_16x16x32_bf16 v[228:231], v[234:237], v[224:227], 0\n" \
  "v_mfma_f32_16x16x32_bf16 v[228:231], v[236:239], v[252:255], v[228:231]\n" \
  "v_mfma_f32_16x16x32_bf16 v[160:163], v[184:187], v[224:227], 0\n" \
  "v_mfma_f32_16x16x32_bf16 v[160:163], v[218:221], v[252:255], v[160:163]\n" \
  "v_rcp_f32 v232, v228\n" \
  "v_mfma_f32_16x16x32_bf16 v[164:167], v[188:191], v[224:227], 0\n" \
  "v_cmp_u_f32 vcc, v228, v228\n" \
  "v_mfma_f32_16x16x32_bf16 v[164:167], v[220:223], v[252:255], v[164:167]\n" \
  "s_cbranch_vccnz Lredo_%=\n" \
  "Lcont_%=:\n" \
  "v_mul_f32 v160, v160, v232\n" \
  "v_mul_f32 v161, v161, v232\n" \
  "v_mul_f32 v162, v162, v232\n" \
  "v_mul_f32 v163, v163, v232\n" \
  "v_cvt_pk_bf16_f32 v236, v160, v161\n" \
  "v_cvt_pk_bf16_f32 v237, v162, v163\n" \
  "v_and_or_b32 v236, v236, -2, s96\n" \
  "v_and_or_b32 v237, v237, -2, s96\n" \
  "v_pk_mul_f32 v[164:165], v[164:165], v[232:233] op_sel_hi:[1,0]\n" \
  "v_pk_mul_f32 v[166:167], v[166:167], v[232:233] op_sel_hi:[1,0]\n" \
  "v_cvt_pk_bf16_f32 v238, v164, v165\n" \
  "v_cvt_pk_bf16_f32 v239, v166, v167\n" \
  "v_and_or_b32 v238, v238, -2, s96\n" \
  "v_and_or_b32 v239, v239, -2, s96\n" \
  "s_mov_b64 exec, s[68:69]\n" \
  "global_store_dwordx4 v246, v[236:239], s[46:47] sc1\n" \
  "s_memtime s[26:27]\n" \
  "s_mov_b32 exec_hi, 0\n" \
  "s_mov_b32 exec_lo, s89\n" \
  "global_load_dword v233, v247, s[44:45] sc1\n" \
  "s_mov_b64 exec, -1\n" \
  "s_add_u32 s40, s40, 16384\n" \
  "s_addc_u32 s41, s41, 0\n" \
  "s_add_u32 s63, s93, s91\n" \
  "s_add_u32 s91, s91, s92\n" \
  "s_mov_b32 s92, 16384\n" \
  "s_cmp_eq_u32 s91, 32768\n" \
  "s_cselect_b32 s92, 0xffff8000, s92\n" \
  "s_waitcnt lgkmcnt(0)\n" \
  "s_sub_u32 s73, s26, s97\n" \
  "s_sub_u32 s74, s73, s88\n" \
  "s_ashr_i32 s74, s74, 2\n" \
  "s_cmp_lt_u32 s98, 2\n" \
  "s_cselect_b32 s74, 0, s74\n" \
  "s_add_u32 s88, s88, s74\n" \
  "s_cmp_eq_u32 s98, 0\n" \
  "s_cselect_b32 s73, s73, s88\n" \
  "s_add_u32 s73, s73, s59\n" \
  "s_add_u32 s73, s73, s97\n" \
  "s_sub_u32 s74, s73, s26\n" \
  "s_ashr_i32 s74, s74, 3\n" \
  "s_sub_u32 s74, s74, 28\n" \
  "s_max_i32 s74, s74, 0\n" \
  "s_min_i32 s74, s74, 160\n" \
  "s_lshl_b32 s74, s74, 2\n" \
  "v_subrev_u32 v164, s64, v245\n" \
  "ds_read_b128 v[128:131], v164 offset:8192\n" \
  "ds_read_b128 v[144:147], v164 offset:8448\n" \
  "ds_read_b128 v[192:195], v242 offset:3072\n" \
  "ds_read_b128 v[208:211], v242 offset:7168\n" \
  "ds_read_b128 v[132:135], v164 offset:10240\n" \
  "ds_read_b128 v[148:151], v164 offset:10496\n" \
  "ds_read_b128 v[196:199], v242 offset:4096\n" \
  "ds_read_b128 v[212:215], v242 offset:8192\n" \
  "ds_read_b128 v[136:139], v164 offset:12288\n" \
  "ds_read_b128 v[152:155], v164 offset:12544\n" \
  "ds_read_b128 v[200:203], v242 offset:5120\n" \
  "ds_read_b128 v[216:219], v242 offset:9216\n" \
  "s_bitcmp1_b32 s58, 0\n" \
  "s_cbranch_scc0 Lnored_%=\n" \
  "ds_read_b128 v[176:179], v241 offset:8192\n" \
  "s_getpc_b64 s[70:71]\n" \
  "Lh1_%=:\n" \
  "s_add_u32 s70, s70, Le1_%=-Lh1_%=\n" \
  "s_addc_u32 s71, s71, 0\n" \
  "s_sub_u32 s70, s70, s74\n" \
  "s_subb_u32 s71, s71, 0\n" \
  "s_setpc_b64 s[70:71]\n" \
  "s_nop 1\n" \
  "s_nop 1\n" \
  "s_nop 1\n" \
  "s_nop 1\n" \
  "s_nop 1\n" \
  "s_nop 1\n" \
  "s_nop 1\n" \
  "s_nop 1\n" \
  "s_nop 1\n" \
  "s_nop 1\n" \
  "s_nop 1\n" \
  "s_nop 1\n" \
  "s_nop 1\n" \
  "s_nop 1\n" \
  "s_nop 1\n" \
  "s_nop 1\n" \
  "s_nop 1\n" \
  "s_nop 1\n" \
  "s_nop 1\n" \
  "s_nop 1\n" \
  "s_nop 1\n" \
  "s_nop 1\n" \
  "s_nop 1\n" \
  "s_nop 1\n" \
  "s_nop 1\n" \
  "s_nop 1\n" \
  "s_nop 1\n" \
  "s_nop 1\n" \
  "s_nop 1\n" \
  "s_nop 1\n" \
  "s_nop 1\n" \
  "s_nop 1\n" \
  "s_nop 1\n" \
  "s_nop 1\n" \
  "s_nop 1\n" \
  "s_nop 1\n" \
  "s_nop 1\n" \
  "s_nop 1\n" \
  "s_nop 1\n" \
  "s_nop 1\n" \
  "s_nop 1\n" \
  "s_nop 1\n" \
  "s_nop 1\n" \
  "s_nop 1\n" \
  "s_nop 1\n" \
  "s_nop 1\n" \
  "s_nop 1\n" \
  "s_nop 1\n" \
  "s_nop 1\n" \
  "s_nop 1\n" \
  "s_nop 1\n" \
  "s_nop 1\n" \
  "s_nop 1\n" \
  "s_nop 1\n" \
  "s_nop 1\n" \
  "s_nop 1\n" \
  "s_nop 1\n" \
  "s_nop 1\n" \
  "s_nop 1\n" \
  "s_nop 1\n" \
  "s_nop 1\n" \
  "s_nop 1\n" \
  "s_nop 1\n" \
  "s_nop 1\n" \
  "s_nop 1\n" \
  "s_nop 1\n" \
  "s_nop 1\n" \
  "s_nop 1\n" \
  "s_nop 1\n" \
  "s_nop 1\n" \
  "s_nop 1\n" \
  "s_nop 1\n" \
  "s_nop 1\n" \
  "s_nop 1\n" \
  "s_nop 1\n" \
  "s_nop 1\n" \
  "s_nop 1\n" \
  "s_nop 1\n" \
  "s_nop 1\n" \
  "s_nop 1\n" \
  "s_nop 1\n" \
  "s_nop 1\n" \
  "s_nop 1\n" \
  "s_nop 1\n" \
  "s_nop 1\n" \
  "s_nop 1\n" \
  "s_nop 1\n" \
  "s_nop 1\n" \
  "s_nop 1\n" \
  "s_nop 1\n" \
  "s_nop 1\n" \
  "s_nop 1\n" \
  "s_nop 1\n" \
  "s_nop 1\n" \
  "s_nop 1\n" \
  "s_nop 1\n" \
  "s_nop 1\n" \
  "s_nop 1\n" \
  "s_nop 1\n" \
  "s_nop 1\n" \
  "s_nop 1\n" \
  "s_nop 1\n" \
  "s_nop 1\n" \
  "s_nop 1\n" \
  "s_nop 1\n" \
  "s_nop 1\n" \
  "s_nop 1\n" \
  "s_nop 1\n" \
  "s_nop 1\n" \
  "s_nop 1\n" \
  "s_nop 1\n" \
  "s_nop 1\n" \
  "s_nop 1\n" \
  "s_nop 1\n" \
  "s_nop 1\n" \
  "s_nop 1\n" \
  "s_nop 1\n" \
  "s_nop 1\n" \
  "s_nop 1\n" \
  "s_nop 1\n" \
  "s_nop 1\n" \
  "s_nop 1\n" \
  "s_nop 1\n" \
  "s_nop 1\n" \
  "s_nop 1\n" \
  "s_nop 1\n" \
  "s_nop 1\n" \
  "s_nop 1\n" \
  "s_nop 1\n" \
  "s_nop 1\n" \
  "s_nop 1\n" \
  "s_nop 1\n" \
  "s_nop 1\n" \
  "s_nop 1\n" \
  "s_nop 1\n" \
  "s_nop 1\n" \
  "s_nop 1\n" \
  "s_nop 1\n" \
  "s_nop 1\n" \
  "s_nop 1\n" \
  "s_nop 1\n" \
  "s_nop 1\n" \
  "s_nop 1\n" \
  "s_nop 1\n" \
  "s_nop 1\n" \
  "s_nop 1\n" \
  "s_nop 1\n" \
  "s_nop 1\n" \
  "s_nop 1\n" \
  "s_nop 1\n" \
  "s_nop 1\n" \
  "s_nop 1\n" \
  "s_nop 1\n" \
  "s_nop 1\n" \
  "s_nop 1\n" \
  "s_nop 1\n" \
  "s_nop 1\n" \
  "s_nop 1\n" \
  "s_nop 1\n" \
  "s_nop 1\n" \
  "Le1_%=:\n" \
  "global_load_dwordx4 v[224:227], v241, s[48:49] offset:-2048 sc1\n" \
  "global_load_dwordx4 v[228:231], v241, s[48:49] offset:-1024 sc1\n" \
  "global_load_dwordx4 v[232:235], v241, s[48:49] offset:2048 sc1\n" \
  "global_load_dwordx4 v[236:239], v241, s[48:49] offset:3072 sc1\n" \
  "s_xor_b32 s54, s52, 16384\n" \
  "s_mov_b32 s55, s53\n" \
  "s_xor_b32 s94, s52, 2048\n" \
  "s_mov_b32 s95, s53\n" \
  "s_waitcnt lgkmcnt(8)\n" \
  "ds_read_b128 v[140:143], v164 offset:14336\n" \
  "ds_read_b128 v[156:159], v164 offset:14592\n" \
  "ds_read_b128 v[204:207], v242 offset:6144\n" \
  "ds_read_b128 v[220:223], v242 offset:10240\n" \
  "v_mfma_f32_16x16x32_bf16 v[160:163], v[128:131], v[192:195], 0\n" \
  "v_mfma_f32_16x16x32_bf16 v[164:167], v[128:131], v[208:211], 0\n" \
  "v_mfma_f32_16x16x32_bf16 v[168:171], v[144:147], v[192:195], 0\n" \
  "v_mfma_f32_16x16x32_bf16 v[172:175], v[144:147], v[208:211], 0\n" \
  "s_waitcnt lgkmcnt(8)\n" \
  "v_mfma_f32_16x16x32_bf16 v[160:163], v[132:135], v[196:199], v[160:163]\n" \
  "v_mfma_f32_16x16x32_bf16 v[164:167], v[132:135], v[212:215], v[164:167]\n" \
  "v_mfma_f32_16x16x32_bf16 v[168:171], v[148:151], v[196:199], v[168:171]\n" \
  "v_mfma_f32_16x16x32_bf16 v[172:175], v[148:151], v[212:215], v[172:175]\n" \
  "s_waitcnt lgkmcnt(4)\n" \
  "v_mfma_f32_16x16x32_bf16 v[160:163], v[136:139], v[200:203], v[160:163]\n" \
  "v_mfma_f32_16x16x32_bf16 v[164:167], v[136:139], v[216:219], v[164:167]\n" \
  "v_mfma_f32_16x16x32_bf16 v[168:171], v[152:155], v[200:203], v[168:171]\n" \
  "v_mfma_f32_16x16x32_bf16 v[172:175], v[152:155], v[216:219], v[172:175]\n" \
  "s_waitcnt lgkmcnt(0)\n" \
  "v_mfma_f32_16x16x32_bf16 v[160:163], v[140:143], v[204:207], v[160:163]\n" \
  "v_mfma_f32_16x16x32_bf16 v[164:167], v[140:143], v[220:223], v[164:167]\n" \
  "v_mfma_f32_16x16x32_bf16 v[168:171], v[156:159], v[204:207], v[168:171]\n" \
  "v_mfma_f32_16x16x32_bf16 v[172:175], v[156:159], v[220:223], v[172:175]\n" \
  "v_cvt_pk_bf16_f32 v184, v160, v161\n" \
  "v_cvt_pk_bf16_f32 v185, v162, v163\n" \
  "s_nop 1\n" \
  "v_cvt_pk_bf16_f32 v186, v168, v169\n" \
  "v_cvt_pk_bf16_f32 v187, v170, v171\n" \
  "v_cvt_pk_bf16_f32 v188, v164, v165\n" \
  "v_cvt_pk_bf16_f32 v189, v166, v167\n" \
  "v_cvt_pk_bf16_f32 v190, v172, v173\n" \
  "v_cvt_pk_bf16_f32 v191, v174, v175\n" \
  "v_mov_b32 v168, 0\n" \
  "v_mov_b32 v169, 0\n" \
  "v_mov_b32 v170, 0\n" \
  "v_mov_b32 v171, 0\n" \
  "v_mov_b32 v172, 0\n" \
  "v_mov_b32 v173, 0\n" \
  "v_mov_b32 v174, 0\n" \
  "v_mov_b32 v175, 0\n" \
  "v_lshrrev_b32 v165, 5, v241\n" \
  "v_and_b32 v165, 8, v165\n" \
  "v_and_b32 v166, 112, v241\n" \
  "v_or_b32 v165, v165, v166\n" \
  "s_cmp_eq_u32 s96, 1\n" \
  "s_cbranch_scc1 Lred1_%=\n" \
  "Lred0_%=:\n" \
  "s_waitcnt vmcnt(3)\n" \
  "v_mfma_f32_16x16x32_bf16 v[180:183], v[176:179], v[224:227], 0\n" \
  "v_or3_b32 v164, v224, v225, v226\n" \
  "v_or3_b32 v164, v164, v227, v227\n" \
  "s_waitcnt vmcnt(2)\n" \
  "v_mfma_f32_16x16x32_bf16 v[180:183], v[176:179], v[228:231], v[180:183]\n" \
  "v_or3_b32 v164, v164, v228, v229\n" \
  "v_or3_b32 v164, v164, v230, v231\n" \
  "s_waitcnt vmcnt(1)\n" \
  "v_mfma_f32_16x16x32_bf16 v[180:183], v[176:179], v[232:235], v[180:183]\n" \
  "v_or3_b32 v164, v164, v232, v233\n" \
  "v_or3_b32 v164, v164, v234, v235\n" \
  "s_waitcnt vmcnt(0)\n" \
  "v_mfma_f32_16x16x32_bf16 v[180:183], v[176:179], v[236:239], v[180:183]\n" \
  "v_or3_b32 v164, v164, v236, v237\n" \
  "v_or3_b32 v164, v164, v238, v239\n" \
  "v_and_b32 v164, 1, v164\n" \
  "v_cmp_ne_u32 vcc, 0, v164\n" \
  "s_nop 0\n" \
  "s_cbranch_vccnz Lretry_%=\n" \
  "Lfin_%=:\n" \
  "s_nop 1\n" \
  "v_add_f32_dpp v180, v180, v180 row_ror:8 row_mask:0xf bank_mask:0xf\n" \
  "v_add_f32_dpp v181, v181, v181 row_ror:8 row_mask:0xf bank_mask:0xf\n" \
  "v_add_f32_dpp v182, v182, v182 row_ror:8 row_mask:0xf bank_mask:0xf\n" \
  "v_add_f32_dpp v183, v183, v183 row_ror:8 row_mask:0xf bank_mask:0xf\n" \
  "v_cvt_pk_bf16_f32 v160, v180, v181\n" \
  "v_cvt_pk_bf16_f32 v161, v182, v183\n" \
  "s_mov_b64 exec, 0xff00ff\n" \
  "global_store_dwordx2 v165, v[160:161], s[52:53] sc1\n" \
  "global_store_dwordx2 v165, v[160:161], s[54:55] sc1\n" \
  "global_store_dwordx2 v165, v[160:161], s[94:95] sc1\n" \
  "s_mov_b64 exec, -1\n" \
  "Lpub_%=:\n" \
  "s_mov_b64 s[76:77], 0xffffffff\n" \
  "v_mov_b32 v176, 0\n" \
  "v_mov_b32 v177, 0\n" \
  "v_mov_b32 v178, 0\n" \
  "v_mov_b32 v179, 0\n" \
  "v_mov_b32 v180, 0\n" \
  "v_mov_b32 v181, 0\n" \
  "v_mov_b32 v182, 0\n" \
  "v_mov_b32 v183, 0\n" \
  "v_mov_b32 v164, 0\n" \
  "v_mov_b32 v165, 0\n" \
  "v_mov_b32 v166, 0\n" \
  "v_mov_b32 v167, 0\n" \
  "v_mov_b32 v160, 0\n" \
  "v_mov_b32 v161, 0\n" \
  "v_mov_b32 v162, 0\n" \
  "v_mov_b32 v163, 0\n" \
  "s_branch Ldrop_%=\n" \
  "Lnored_%=:\n" \
  "s_nop 7\n" \
  "s_nop 1\n" \
  "s_waitcnt lgkmcnt(8)\n" \
  "ds_read_b128 v[140:143], v164 offset:14336\n" \
  "ds_read_b128 v[156:159], v164 offset:14592\n" \
  "ds_read_b128 v[204:207], v242 offset:6144\n" \
  "ds_read_b128 v[220:223], v242 offset:10240\n" \
  "v_mfma_f32_16x16x32_bf16 v[160:163], v[128:131], v[192:195], 0\n" \
  "v_mfma_f32_16x16x32_bf16 v[164:167], v[128:131], v[208:211], 0\n" \
  "v_mfma_f32_16x16x32_bf16 v[168:171], v[144:147], v[192:195], 0\n" \
  "v_mfma_f32_16x16x32_bf16 v[172:175], v[144:147], v[208:211], 0\n" \
  "s_waitcnt lgkmcnt(8)\n" \
  "v_mfma_f32_16x16x32_bf16 v[160:163], v[132:135], v[196:199], v[160:163]\n" \
  "v_mfma_f32_16x16x32_bf16 v[164:167], v[132:135], v[212:215], v[164:167]\n" \
  "v_mfma_f32_16x16x32_bf16 v[168:171], v[148:151], v[196:199], v[168:171]\n" \
  "v_mfma_f32_16x16x32_bf16 v[172:175], v[148:151], v[212:215], v[172:175]\n" \
  "s_waitcnt lgkmcnt(4)\n" \
  "v_mfma_f32_16x16x32_bf16 v[160:163], v[136:139], v[200:203], v[160:163]\n" \
  "v_mfma_f32_16x16x32_bf16 v[164:167], v[136:139], v[216:219], v[164:167]\n" \
  "v_mfma_f32_16x16x32_bf16 v[168:171], v[152:155], v[200:203], v[168:171]\n" \
  "v_mfma_f32_16x16x32_bf16 v[172:175], v[152:155], v[216:219], v[172:175]\n" \
  "s_waitcnt lgkmcnt(0)\n" \
  "v_mfma_f32_16x16x32_bf16 v[160:163], v[140:143], v[204:207], v[160:163]\n" \
  "v_mfma_f32_16x16x32_bf16 v[164:167], v[140:143], v[220:223], v[164:167]\n" \
  "v_mfma_f32_16x16x32_bf16 v[168:171], v[156:159], v[204:207], v[168:171]\n" \
  "v_mfma_f32_16x16x32_bf16 v[172:175], v[156:159], v[220:223], v[172:175]\n" \
  "v_cvt_pk_bf16_f32 v184, v160, v161\n" \
  "v_cvt_pk_bf16_f32 v185, v162, v163\n" \
  "s_nop 1\n" \
  "v_cvt_pk_bf16_f32 v186, v168, v169\n" \
  "v_cvt_pk_bf16_f32 v187, v170, v171\n" \
  "v_cvt_pk_bf16_f32 v188, v164, v165\n" \
  "v_cvt_pk_bf16_f32 v189, v166, v167\n" \
  "v_cvt_pk_bf16_f32 v190, v172, v173\n" \
  "v_cvt_pk_bf16_f32 v191, v174, v175\n" \
  "v_mov_b32 v160, 0\n" \
  "v_mov_b32 v161, 0\n" \
  "v_mov_b32 v162, 0\n" \
  "v_mov_b32 v163, 0\n" \
  "v_mov_b32 v164, 0\n" \
  "v_mov_b32 v165, 0\n" \
  "v_mov_b32 v166, 0\n" \
  "v_mov_b32 v167, 0\n" \
  "v_mov_b32 v168, 0\n" \
  "v_mov_b32 v169, 0\n" \
  "v_mov_b32 v170, 0\n" \
  "v_mov_b32 v171, 0\n" \
  "v_mov_b32 v172, 0\n" \
  "v_mov_b32 v173, 0\n" \
  "v_mov_b32 v174, 0\n" \
  "v_mov_b32 v175, 0\n" \
  "v_mov_b32 v176, 0\n" \
  "v_mov_b32 v177, 0\n" \
  "v_mov_b32 v178, 0\n" \
  "v_mov_b32 v179, 0\n" \
  "v_mov_b32 v180, 0\n" \
  "v_mov_b32 v181, 0\n" \
  "v_mov_b32 v182, 0\n" \
  "v_mov_b32 v183, 0\n" \
  "s_cmp_lt_u32 s56, 2\n" \
  "s_cbranch_scc0 Ldma_%=\n" \
  "Ldrop_%=:\n" \
  "v_lshlrev_b32 v192, 3, v241\n" \
  "v_and_b32 v194, 2048, v192\n" \
  "v_and_b32 v192, 4095, v192\n" \
  "v_add_u32 v192, v192, v194\n" \
  "v_mov_b32 v193, s96\n" \
  "s_mov_b64 exec, s[76:77]\n" \
  "global_store_dword v192, v193, s[48:49] offset:-2048 sc1\n" \
  "s_mov_b64 exec, -1\n" \
  "s_xor_b32 s46, s46, 1048576\n" \
  "s_xor_b32 s48, s48, 1048576\n" \
  "s_add_u32 s98, s98, 1\n" \
  "s_add_u32 s72, s98, s29\n" \
  "s_bfe_u32 s96, s72, 65537\n" \
  "s_and_b32 s72, s98, 31\n" \
  "s_lshl_b32 s74, s72, 16\n" \
  "s_bfe_u32 s75, s72, 65538\n" \
  "s_lshl_b32 s75, s75, 11\n" \
  "s_or_b32 s74, s74, s75\n" \
  "s_bfe_u32 s75, s72, 65539\n" \
  "s_lshl_b32 s75, s75, 14\n" \
  "s_or_b32 s74, s74, s75\n" \
  "s_xor_b32 s74, s74, s39\n" \
  "s_cmp_lt_u32 s98, 32\n" \
  "s_cselect_b64 s[52:53], s[34:35], s[36:37]\n" \
  "s_xor_b32 s52, s52, s74\n" \
  "s_sub_u32 s57, s57, 1\n" \
  "s_cmp_eq_u32 s57, 0\n" \
  "s_cbranch_scc0 Ltop_%=\n" \
  "s_branch Lend_%=\n" \
  "Ldma_%=:\n" \
  "s_cmp_eq_u32 s57, 1\n" \
  "s_cbranch_scc1 Lend_%=\n" \
  "s_and_b32 s72, s56, 1\n" \
  "s_lshl_b32 s72, s72, 11\n" \
  "s_add_u32 m0, s72, 2048\n" \
  "s_mov_b64 s[94:95], s[52:53]\n" \
  "s_add_u32 s54, s52, 2048\n" \
  "s_addc_u32 s55, s53, 0\n" \
  "s_memrealtime s[4:5]\n" \
  "s_nop 2\n" \
  "s_memrealtime s[6:7]\n" \
  "s_nop 2\n" \
  "s_memrealtime s[8:9]\n" \
  "s_nop 2\n" \
  "s_memrealtime s[10:11]\n" \
  "s_nop 2\n" \
  "s_memrealtime s[12:13]\n" \
  "s_nop 2\n" \
  "s_memrealtime s[14:15]\n" \
  "s_nop 2\n" \
  "s_memrealtime s[16:17]\n" \
  "s_memtime s[26:27]\n" \
  "s_waitcnt lgkmcnt(0)\n" \
  "s_mov_b32 s65, 0\n" \
  "s_cmp_eq_u32 s6, s4\n" \
  "s_cselect_b32 s75, 8, 0\n" \
  "s_add_u32 s65, s65, s75\n" \
  "s_cmp_eq_u32 s8, s4\n" \
  "s_cselect_b32 s75, 8, 0\n" \
  "s_add_u32 s65, s65, s75\n" \
  "s_cmp_eq_u32 s10, s4\n" \
  "s_cselect_b32 s75, 8, 0\n" \
  "s_add_u32 s65, s65, s75\n" \
  "s_cmp_eq_u32 s12, s4\n" \
  "s_cselect_b32 s75, 8, 0\n" \
  "s_add_u32 s65, s65, s75\n" \
  "s_cmp_eq_u32 s14, s4\n" \
  "s_cselect_b32 s75, 8, 0\n" \
  "s_add_u32 s65, s65, s75\n" \
  "s_cmp_eq_u32 s16, s4\n" \
  "s_cselect_b32 s75, 8, 0\n" \
  "s_add_u32 s65, s65, s75\n" \
  "s_sub_u32 s75, s4, s99\n" \
  "s_mul_i32 s75, s75, 12\n" \
  "s_sub_u32 s75, s75, s65\n" \
  "s_add_u32 s75, s75, s80\n" \
  "s_cmp_gt_u32 s75, s67\n" \
  "s_cselect_b32 s81, 1, 2\n" \
  "s_sub_u32 s72, s75, s67\n" \
  "s_ashr_i32 s72, s72, s81\n" \
  "s_add_u32 s72, s72, s67\n" \
  "s_cmp_lt_u32 s75, 4800\n" \
  "s_cselect_b32 s67, s72, s67\n" \
  "s_waitcnt vmcnt(0)\n" \
  "s_add_u32 s73, s98, 4\n" \
  "s_min_u32 s73, s73, 64\n" \
  "v_readlane_b32 s72, v233, s66\n" \
  "s_nop 0\n" \
  "s_cmp_ge_u32 s72, s73\n" \
  "s_cbranch_scc1 Lkb_%=\n" \
  "Lka_%=:\n" \
  "s_sleep 2\n" \
  "s_mov_b64 exec, 14\n" \
  "global_load_dword v233, v247, s[44:45] sc1\n" \
  "s_mov_b64 exec, -1\n" \
  "s_waitcnt vmcnt(0)\n" \
  "v_readlane_b32 s72, v233, s66\n" \
  "s_nop 0\n" \
  "s_cmp_ge_u32 s72, s73\n" \
  "s_cbranch_scc1 Lkb_%=\n" \
  "s_sub_u32 s61, s61, 1\n" \
  "s_cmp_gt_i32 s61, 0\n" \
  "s_cbranch_scc1 Lka_%=\n" \
  "Lkb_%=:\n" \
  "s_add_u32 s66, s66, 1\n" \
  "s_cmp_eq_u32 s66, 4\n" \
  "s_cselect_b32 s66, 1, s66\n" \
  "s_cmp_gt_u32 s73, 11\n" \
  "s_cselect_b32 s66, 1, s66\n" \
  "s_cselect_b32 s89, 3, s89\n" \
  "v_readfirstlane_b32 s73, v233\n" \
  "s_add_u32 s99, s99, s60\n" \
  "s_cmp_eq_u32 s73, -1\n" \
  "s_cselect_b32 s99, s99, s73\n" \
  "s_mov_b32 s73, s99\n" \
  "s_branch Lx2_%=\n" \
  "Lw2_%=:\n" \
  "s_memrealtime s[4:5]\n" \
  "s_nop 2\n" \
  "s_memrealtime s[6:7]\n" \
  "s_nop 2\n" \
  "s_memrealtime s[8:9]\n" \
  "s_nop 2\n" \
  "s_memrealtime s[10:11]\n" \
  "s_nop 2\n" \
  "s_memrealtime s[12:13]\n" \
  "s_nop 2\n" \
  "s_memrealtime s[14:15]\n" \
  "s_nop 2\n" \
  "s_memrealtime s[16:17]\n" \
  "s_memtime s[26:27]\n" \
  "s_waitcnt lgkmcnt(0)\n" \
  "s_mov_b32 s65, 0\n" \
  "s_cmp_eq_u32 s6, s4\n" \
  "s_cselect_b32 s75, 8, 0\n" \
  "s_add_u32 s65, s65, s75\n" \
  "s_cmp_eq_u32 s8, s4\n" \
  "s_cselect_b32 s75, 8, 0\n" \
  "s_add_u32 s65, s65, s75\n" \
  "s_cmp_eq_u32 s10, s4\n" \
  "s_cselect_b32 s75, 8, 0\n" \
  "s_add_u32 s65, s65, s75\n" \
  "s_cmp_eq_u32 s12, s4\n" \
  "s_cselect_b32 s75, 8, 0\n" \
  "s_add_u32 s65, s65, s75\n" \
  "s_cmp_eq_u32 s14, s4\n" \
  "s_cselect_b32 s75, 8, 0\n" \
  "s_add_u32 s65, s65, s75\n" \
  "s_cmp_eq_u32 s16, s4\n" \
  "s_cselect_b32 s75, 8, 0\n" \
  "s_add_u32 s65, s65, s75\n" \
  "Lx2_%=:\n" \
  "s_memtime s[6:7]\n" \
  "s_sub_u32 s74, s99, s4\n" \
  "s_mul_i32 s74, s74, 12\n" \
  "s_add_u32 s74, s74, s65\n" \
  "s_waitcnt lgkmcnt(0)\n" \
  "s_sub_u32 s75, s6, s26\n" \
  "s_lshr_b32 s75, s75, 1\n" \
  "s_add_u32 s75, s75, 132\n" \
  "s_sub_u32 s74, s74, s75\n" \
  "s_sub_u32 s75, 551, s74\n" \
  "s_mul_hi_u32 s75, s75, 11184811\n" \
  "s_mul_i32 s75, s75, 32\n" \
  "s_cmp_lt_i32 s74, -24\n" \
  "s_cselect_b32 s75, s75, 0\n" \
  "s_add_u32 s99, s99, s75\n" \
  "s_mul_i32 s75, s75, 12\n" \
  "s_add_u32 s74, s74, s75\n" \
  "s_cmp_gt_i32 s74, 12000\n" \
  "s_cselect_b32 s74, 0, s74\n" \
  "s_cmp_gt_i32 s74, 640\n" \
  "s_cbranch_scc1 Lw2_%=\n" \
  "s_max_i32 s74, s74, 0\n" \
  "s_and_b32 s74, s74, -4\n" \
  "s_getpc_b64 s[70:71]\n" \
  "Lh3_%=:\n" \
  "s_add_u32 s70, s70, Le3_%=-Lh3_%=\n" \
  "s_addc_u32 s71, s71, 0\n" \
  "s_sub_u32 s70, s70, s74\n" \
  "s_subb_u32 s71, s71, 0\n" \
  "s_setpc_b64 s[70:71]\n" \
  "s_nop 1\n" \
  "s_nop 1\n" \
  "s_nop 1\n" \
  "s_nop 1\n" \
  "s_nop 1\n" \
  "s_nop 1\n" \
  "s_nop 1\n" \
  "s_nop 1\n" \
  "s_nop 1\n" \
  "s_nop 1\n" \
  "s_nop 1\n" \
  "s_nop 1\n" \
  "s_nop 1\n" \
  "s_nop 1\n" \
  "s_nop 1\n" \
  "s_nop 1\n" \
  "s_nop 1\n" \
  "s_nop 1\n" \
  "s_nop 1\n" \
  "s_nop 1\n" \
  "s_nop 1\n" \
  "s_nop 1\n" \
  "s_nop 1\n" \
  "s_nop 1\n" \
  "s_nop 1\n" \
  "s_nop 1\n" \
  "s_nop 1\n" \
  "s_nop 1\n" \
  "s_nop 1\n" \
  "s_nop 1\n" \
  "s_nop 1\n" \
  "s_nop 1\n" \
  "s_nop 1\n" \
  "s_nop 1\n" \
  "s_nop 1\n" \
  "s_nop 1\n" \
  "s_nop 1\n" \
  "s_nop 1\n" \
  "s_nop 1\n" \
  "s_nop 1\n" \
  "s_nop 1\n" \
  "s_nop 1\n" \
  "s_nop 1\n" \
  "s_nop 1\n" \
  "s_nop 1\n" \
  "s_nop 1\n" \
  "s_nop 1\n" \
  "s_nop 1\n" \
  "s_nop 1\n" \
  "s_nop 1\n" \
  "s_nop 1\n" \
  "s_nop 1\n" \
  "s_nop 1\n" \
  "s_nop 1\n" \
  "s_nop 1\n" \
  "s_nop 1\n" \
  "s_nop 1\n" \
  "s_nop 1\n" \
  "s_nop 1\n" \
  "s_nop 1\n" \
  "s_nop 1\n" \
  "s_nop 1\n" \
  "s_nop 1\n" \
  "s_nop 1\n" \
  "s_nop 1\n" \
  "s_nop 1\n" \
  "s_nop 1\n" \
  "s_nop 1\n" \
  "s_nop 1\n" \
  "s_nop 1\n" \
  "s_nop 1\n" \
  "s_nop 1\n" \
  "s_nop 1\n" \
  "s_nop 1\n" \
  "s_nop 1\n" \
  "s_nop 1\n" \
  "s_nop 1\n" \
  "s_nop 1\n" \
  "s_nop 1\n" \
  "s_nop 1\n" \
  "s_nop 1\n" \
  "s_nop 1\n" \
  "s_nop 1\n" \
  "s_nop 1\n" \
  "s_nop 1\n" \
  "s_nop 1\n" \
  "s_nop 1\n" \
  "s_nop 1\n" \
  "s_nop 1\n" \
  "s_nop 1\n" \
  "s_nop 1\n" \
  "s_nop 1\n" \
  "s_nop 1\n" \
  "s_nop 1\n" \
  "s_nop 1\n" \
  "s_nop 1\n" \
  "s_nop 1\n" \
  "s_nop 1\n" \
  "s_nop 1\n" \
  "s_nop 1\n" \
  "s_nop 1\n" \
  "s_nop 1\n" \
  "s_nop 1\n" \
  "s_nop 1\n" \
  "s_nop 1\n" \
  "s_nop 1\n" \
  "s_nop 1\n" \
  "s_nop 1\n" \
  "s_nop 1\n" \
  "s_nop 1\n" \
  "s_nop 1\n" \
  "s_nop 1\n" \
  "s_nop 1\n" \
  "s_nop 1\n" \
  "s_nop 1\n" \
  "s_nop 1\n" \
  "s_nop 1\n" \
  "s_nop 1\n" \
  "s_nop 1\n" \
  "s_nop 1\n" \
  "s_nop 1\n" \
  "s_nop 1\n" \
  "s_nop 1\n" \
  "s_nop 1\n" \
  "s_nop 1\n" \
  "s_nop 1\n" \
  "s_nop 1\n" \
  "s_nop 1\n" \
  "s_nop 1\n" \
  "s_nop 1\n" \
  "s_nop 1\n" \
  "s_nop 1\n" \
  "s_nop 1\n" \
  "s_nop 1\n" \
  "s_nop 1\n" \
  "s_nop 1\n" \
  "s_nop 1\n" \
  "s_nop 1\n" \
  "s_nop 1\n" \
  "s_nop 1\n" \
  "s_nop 1\n" \
  "s_nop 1\n" \
  "s_nop 1\n" \
  "s_nop 1\n" \
  "s_nop 1\n" \
  "s_nop 1\n" \
  "s_nop 1\n" \
  "s_nop 1\n" \
  "s_nop 1\n" \
  "s_nop 1\n" \
  "s_nop 1\n" \
  "s_nop 1\n" \
  "s_nop 1\n" \
  "s_nop 1\n" \
  "s_nop 1\n" \
  "s_nop 1\n" \
  "s_nop 1\n" \
  "s_nop 1\n" \
  "s_nop 1\n" \
  "s_nop 1\n" \
  "Le3_%=:\n" \
  "global_load_lds_dwordx4 v241, s[54:55] offset:-2048 sc1\n" \
  "global_load_lds_dwordx4 v241, s[54:55] offset:-1024 sc1\n" \
  "global_load_lds_dwordx4 v241, s[54:55] offset:2048 sc1\n" \
  "global_load_lds_dwordx4 v241, s[54:55] offset:3072 sc1\n" \
  "s_memtime s[4:5]\n" \
  "s_waitcnt lgkmcnt(0)\n" \
  "v_mov_b32 v192, s99\n" \
  "v_mov_b32 v193, s4\n" \
  "s_cmp_eq_u32 s56, 2\n" \
  "s_cselect_b64 s[70:71], -1, 0\n" \
  "s_mov_b64 exec, s[70:71]\n" \
  "ds_write_b64 v248, v[192:193] offset:9216\n" \
  "s_mov_b64 exec, -1\n" \
  "s_waitcnt lgkmcnt(0)\n" \
  "s_add_u32 s60, s67, 11\n" \
  "s_mul_i32 s60, s60, 5462\n" \
  "s_lshr_b32 s60, s60, 16\n" \
  "s_lshl_b32 s72, s98, 2\n" \
  "s_add_u32 s72, s72, 4\n" \
  "s_add_u32 s70, s44, s72\n" \
  "s_addc_u32 s71, s45, 0\n" \
  "s_and_b32 s72, s56, 1\n" \
  "s_lshl_b32 s72, s72, 10\n" \
  "s_add_u32 s70, s70, s72\n" \
  "s_addc_u32 s71, s71, 0\n" \
  "s_bitcmp1_b32 s58, 1\n" \
  "s_cselect_b64 s[70:71], s[70:71], s[50:51]\n" \
  "s_add_u32 s72, s99, s60\n" \
  "s_cmp_eq_u32 s99, s73\n" \
  "s_cselect_b32 s73, 0, 32\n" \
  "s_add_u32 s72, s72, s73\n" \
  "v_mov_b32 v194, s72\n" \
  "s_mov_b64 exec, 1\n" \
  "global_store_dword v241, v194, s[70:71] sc1\n" \
  "s_mov_b64 exec, -1\n" \
  "s_xor_b32 s46, s46, 1048576\n" \
  "s_xor_b32 s48, s48, 1048576\n" \
  "s_add_u32 s98, s98, 1\n" \
  "s_add_u32 s72, s98, s29\n" \
  "s_bfe_u32 s96, s72, 65537\n" \
  "s_and_b32 s72, s98, 31\n" \
  "s_lshl_b32 s74, s72, 16\n" \
  "s_bfe_u32 s75, s72, 65538\n" \
  "s_lshl_b32 s75, s75, 11\n" \
  "s_or_b32 s74, s74, s75\n" \
  "s_bfe_u32 s75, s72, 65539\n" \
  "s_lshl_b32 s75, s75, 14\n" \
  "s_or_b32 s74, s74, s75\n" \
  "s_xor_b32 s74, s74, s39\n" \
  "s_cmp_lt_u32 s98, 32\n" \
  "s_cselect_b64 s[52:53], s[34:35], s[36:37]\n" \
  "s_xor_b32 s52, s52, s74\n" \
  "s_sub_u32 s57, s57, 1\n" \
  "s_cmp_eq_u32 s57, 0\n" \
  "s_cbranch_scc0 Ltop_%=\n" \
  "s_branch Lend_%=\n" \
  "Lred1_%=:\n" \
  "s_waitcnt vmcnt(3)\n" \
  "v_mfma_f32_16x16x32_bf16 v[180:183], v[176:179], v[224:227], 0\n" \
  "v_bitop3_b32 v164, v224, v225, v226 bitop3:0x80\n" \
  "v_bitop3_b32 v164, v164, v227, v227 bitop3:0x80\n" \
  "s_waitcnt vmcnt(2)\n" \
  "v_mfma_f32_16x16x32_bf16 v[180:183], v[176:179], v[228:231], v[180:183]\n" \
  "v_bitop3_b32 v164, v164, v228, v229 bitop3:0x80\n" \
  "v_bitop3_b32 v164, v164, v230, v231 bitop3:0x80\n" \
  "s_waitcnt vmcnt(1)\n" \
  "v_mfma_f32_16x16x32_bf16 v[180:183], v[176:179], v[232:235], v[180:183]\n" \
  "v_bitop3_b32 v164, v164, v232, v233 bitop3:0x80\n" \
  "v_bitop3_b32 v164, v164, v234, v235 bitop3:0x80\n" \
  "s_waitcnt vmcnt(0)\n" \
  "v_mfma_f32_16x16x32_bf16 v[180:183], v[176:179], v[236:239], v[180:183]\n" \
  "v_bitop3_b32 v164, v164, v236, v237 bitop3:0x80\n" \
  "v_bitop3_b32 v164, v164, v238, v239 bitop3:0x80\n" \
  "v_and_b32 v164, 1, v164\n" \
  "v_cmp_ne_u32 vcc, 1, v164\n" \
  "s_nop 0\n" \
  "s_cbranch_vccnz Lretry_%=\n" \
  "s_branch Lfin_%=\n" \
  "Lretry_%=:\n" \
  "s_nop 7\n" \
  "s_nop 1\n" \
  "s_cmp_gt_u32 s98, 3\n" \
  "s_cselect_b32 s72, 8, 0\n" \
  "s_add_u32 s59, s59, s72\n" \
  "s_min_u32 s59, s59, 640\n" \
  "s_sub_u32 s61, s61, 1\n" \
  "s_cmp_lt_i32 s61, 0\n" \
  "s_cbranch_scc1 Lfin_%=\n" \
  "global_load_dwordx4 v[224:227], v241, s[48:49] offset:-2048 sc1\n" \
  "global_load_dwordx4 v[228:231], v241, s[48:49] offset:-1024 sc1\n" \
  "global_load_dwordx4 v[232:235], v241, s[48:49] offset:2048 sc1\n" \
  "global_load_dwordx4 v[236:239], v241, s[48:49] offset:3072 sc1\n" \
  "s_cmp_eq_u32 s96, 1\n" \
  "s_cbranch_scc1 Lred1_%=\n" \
  "s_branch Lred0_%=\n" \
  "Lfail_%=:\n" \
  "s_cmp_gt_u32 s98, 3\n" \
  "s_cselect_b32 s72, 8, 0\n" \
  "s_add_u32 s80, s80, s72\n" \
  "s_sub_u32 s61, s61, 1\n" \
  "s_cmp_lt_i32 s61, 0\n" \
  "s_cbranch_scc1 Lfcont_%=\n" \
  "s_waitcnt lgkmcnt(0)\n" \
  "s_barrier\n" \
  "s_cmp_lt_u32 s56, 2\n" \
  "s_cbranch_scc1 Lrd4_%=\n" \
  "s_and_b32 s72, s56, 1\n" \
  "s_lshl_b32 s72, s72, 11\n" \
  "s_add_u32 m0, s72, 2048\n" \
  "s_or_b32 s72, s38, 2048\n" \
  "s_xor_b32 s94, s94, s72\n" \
  "s_add_u32 s54, s94, 2048\n" \
  "s_addc_u32 s55, s95, 0\n" \
  "Lw5_%=:\n" \
  "s_memrealtime s[4:5]\n" \
  "s_nop 2\n" \
  "s_memrealtime s[6:7]\n" \
  "s_nop 2\n" \
  "s_memrealtime s[8:9]\n" \
  "s_nop 2\n" \
  "s_memrealtime s[10:11]\n" \
  "s_nop 2\n" \
  "s_memrealtime s[12:13]\n" \
  "s_nop 2\n" \
  "s_memrealtime s[14:15]\n" \
  "s_nop 2\n" \
  "s_memrealtime s[16:17]\n" \
  "s_memtime s[26:27]\n" \
  "s_waitcnt lgkmcnt(0)\n" \
  "s_mov_b32 s65, 0\n" \
  "s_cmp_eq_u32 s6, s4\n" \
  "s_cselect_b32 s75, 8, 0\n" \
  "s_add_u32 s65, s65, s75\n" \
  "s_cmp_eq_u32 s8, s4\n" \
  "s_cselect_b32 s75, 8, 0\n" \
  "s_add_u32 s65, s65, s75\n" \
  "s_cmp_eq_u32 s10, s4\n" \
  "s_cselect_b32 s75, 8, 0\n" \
  "s_add_u32 s65, s65, s75\n" \
  "s_cmp_eq_u32 s12, s4\n" \
  "s_cselect_b32 s75, 8, 0\n" \
  "s_add_u32 s65, s65, s75\n" \
  "s_cmp_eq_u32 s14, s4\n" \
  "s_cselect_b32 s75, 8, 0\n" \
  "s_add_u32 s65, s65, s75\n" \
  "s_cmp_eq_u32 s16, s4\n" \
  "s_cselect_b32 s75, 8, 0\n" \
  "s_add_u32 s65, s65, s75\n" \
  "Lx5_%=:\n" \
  "s_memtime s[6:7]\n" \
  "s_sub_u32 s74, s99, s4\n" \
  "s_mul_i32 s74, s74, 12\n" \
  "s_add_u32 s74, s74, s65\n" \
  "s_waitcnt lgkmcnt(0)\n" \
  "s_sub_u32 s75, s6, s26\n" \
  "s_lshr_b32 s75, s75, 1\n" \
  "s_add_u32 s75, s75, 132\n" \
  "s_sub_u32 s74, s74, s75\n" \
  "s_sub_u32 s75, 383, s74\n" \
  "s_mul_hi_u32 s75, s75, 11184811\n" \
  "s_mul_i32 s75, s75, 32\n" \
  "s_cmp_lt_i32 s74, -24\n" \
  "s_cselect_b32 s75, s75, 0\n" \
  "s_add_u32 s99, s99, s75\n" \
  "s_mul_i32 s75, s75, 12\n" \
  "s_add_u32 s74, s74, s75\n" \
  "s_cmp_gt_i32 s74, 12000\n" \
  "s_cselect_b32 s74, 0, s74\n" \
  "s_cmp_gt_i32 s74, 640\n" \
  "s_cbranch_scc1 Lw5_%=\n" \
  "s_max_i32 s74, s74, 0\n" \
  "s_and_b32 s74, s74, -4\n" \
  "s_getpc_b64 s[70:71]\n" \
  "Lh6_%=:\n" \
  "s_add_u32 s70, s70, Le6_%=-Lh6_%=\n" \
  "s_addc_u32 s71, s71, 0\n" \
  "s_sub_u32 s70, s70, s74\n" \
  "s_subb_u32 s71, s71, 0\n" \
  "s_setpc_b64 s[70:71]\n" \
  "s_nop 1\n" \
  "s_nop 1\n" \
  "s_nop 1\n" \
  "s_nop 1\n" \
  "s_nop 1\n" \
  "s_nop 1\n" \
  "s_nop 1\n" \
  "s_nop 1\n" \
  "s_nop 1\n" \
  "s_nop 1\n" \
  "s_nop 1\n" \
  "s_nop 1\n" \
  "s_nop 1\n" \
  "s_nop 1\n" \
  "s_nop 1\n" \
  "s_nop 1\n" \
  "s_nop 1\n" \
  "s_nop 1\n" \
  "s_nop 1\n" \
  "s_nop 1\n" \
  "s_nop 1\n" \
  "s_nop 1\n" \
  "s_nop 1\n" \
  "s_nop 1\n" \
  "s_nop 1\n" \
  "s_nop 1\n" \
  "s_nop 1\n" \
  "s_nop 1\n" \
  "s_nop 1\n" \
  "s_nop 1\n" \
  "s_nop 1\n" \
  "s_nop 1\n" \
  "s_nop 1\n" \
  "s_nop 1\n" \
  "s_nop 1\n" \
  "s_nop 1\n" \
  "s_nop 1\n" \
  "s_nop 1\n" \
  "s_nop 1\n" \
  "s_nop 1\n" \
  "s_nop 1\n" \
  "s_nop 1\n" \
  "s_nop 1\n" \
  "s_nop 1\n" \
  "s_nop 1\n" \
  "s_nop 1\n" \
  "s_nop 1\n" \
  "s_nop 1\n" \
  "s_nop 1\n" \
  "s_nop 1\n" \
  "s_nop 1\n" \
  "s_nop 1\n" \
  "s_nop 1\n" \
  "s_nop 1\n" \
  "s_nop 1\n" \
  "s_nop 1\n" \
  "s_nop 1\n" \
  "s_nop 1\n" \
  "s_nop 1\n" \
  "s_nop 1\n" \
  "s_nop 1\n" \
  "s_nop 1\n" \
  "s_nop 1\n" \
  "s_nop 1\n" \
  "s_nop 1\n" \
  "s_nop 1\n" \
  "s_nop 1\n" \
  "s_nop 1\n" \
  "s_nop 1\n" \
  "s_nop 1\n" \
  "s_nop 1\n" \
  "s_nop 1\n" \
  "s_nop 1\n" \
  "s_nop 1\n" \
  "s_nop 1\n" \
  "s_nop 1\n" \
  "s_nop 1\n" \
  "s_nop 1\n" \
  "s_nop 1\n" \
  "s_nop 1\n" \
  "s_nop 1\n" \
  "s_nop 1\n" \
  "s_nop 1\n" \
  "s_nop 1\n" \
  "s_nop 1\n" \
  "s_nop 1\n" \
  "s_nop 1\n" \
  "s_nop 1\n" \
  "s_nop 1\n" \
  "s_nop 1\n" \
  "s_nop 1\n" \
  "s_nop 1\n" \
  "s_nop 1\n" \
  "s_nop 1\n" \
  "s_nop 1\n" \
  "s_nop 1\n" \
  "s_nop 1\n" \
  "s_nop 1\n" \
  "s_nop 1\n" \
  "s_nop 1\n" \
  "s_nop 1\n" \
  "s_nop 1\n" \
  "s_nop 1\n" \
  "s_nop 1\n" \
  "s_nop 1\n" \
  "s_nop 1\n" \
  "s_nop 1\n" \
  "s_nop 1\n" \
  "s_nop 1\n" \
  "s_nop 1\n" \
  "s_nop 1\n" \
  "s_nop 1\n" \
  "s_nop 1\n" \
  "s_nop 1\n" \
  "s_nop 1\n" \
  "s_nop 1\n" \
  "s_nop 1\n" \
  "s_nop 1\n" \
  "s_nop 1\n" \
  "s_nop 1\n" \
  "s_nop 1\n" \
  "s_nop 1\n" \
  "s_nop 1\n" \
  "s_nop 1\n" \
  "s_nop 1\n" \
  "s_nop 1\n" \
  "s_nop 1\n" \
  "s_nop 1\n" \
  "s_nop 1\n" \
  "s_nop 1\n" \
  "s_nop 1\n" \
  "s_nop 1\n" \
  "s_nop 1\n" \
  "s_nop 1\n" \
  "s_nop 1\n" \
  "s_nop 1\n" \
  "s_nop 1\n" \
  "s_nop 1\n" \
  "s_nop 1\n" \
  "s_nop 1\n" \
  "s_nop 1\n" \
  "s_nop 1\n" \
  "s_nop 1\n" \
  "s_nop 1\n" \
  "s_nop 1\n" \
  "s_nop 1\n" \
  "s_nop 1\n" \
  "s_nop 1\n" \
  "s_nop 1\n" \
  "s_nop 1\n" \
  "s_nop 1\n" \
  "s_nop 1\n" \
  "s_nop 1\n" \
  "s_nop 1\n" \
  "s_nop 1\n" \
  "s_nop 1\n" \
  "s_nop 1\n" \
  "s_nop 1\n" \
  "s_nop 1\n" \
  "s_nop 1\n" \
  "Le6_%=:\n" \
  "global_load_lds_dwordx4 v241, s[54:55] offset:-2048 sc1\n" \
  "global_load_lds_dwordx4 v241, s[54:55] offset:-1024 sc1\n" \
  "global_load_lds_dwordx4 v241, s[54:55] offset:2048 sc1\n" \
  "global_load_lds_dwordx4 v241, s[54:55] offset:3072 sc1\n" \
  "s_memtime s[4:5]\n" \
  "s_waitcnt lgkmcnt(0)\n" \
  "v_mov_b32 v192, s99\n" \
  "v_mov_b32 v193, s4\n" \
  "s_cmp_eq_u32 s56, 2\n" \
  "s_cselect_b64 s[70:71], -1, 0\n" \
  "s_mov_b64 exec, s[70:71]\n" \
  "ds_write_b64 v248, v[192:193] offset:9216\n" \
  "s_mov_b64 exec, -1\n" \
  "s_waitcnt lgkmcnt(0)\n" \
  "s_lshl_b32 s72, s98, 2\n" \
  "s_add_u32 s72, s72, 0\n" \
  "s_add_u32 s70, s44, s72\n" \
  "s_addc_u32 s71, s45, 0\n" \
  "s_and_b32 s72, s56, 1\n" \
  "s_lshl_b32 s72, s72, 10\n" \
  "s_add_u32 s70, s70, s72\n" \
  "s_addc_u32 s71, s71, 0\n" \
  "s_bitcmp1_b32 s58, 1\n" \
  "s_cselect_b64 s[70:71], s[70:71], s[50:51]\n" \
  "s_add_u32 s72, s99, s60\n" \
  "s_add_u32 s72, s72, 32\n" \
  "v_mov_b32 v194, s72\n" \
  "s_mov_b64 exec, 1\n" \
  "global_store_dword v241, v194, s[70:71] sc1\n" \
  "s_mov_b64 exec, -1\n" \
  "Lrd4_%=:\n" \
  "s_nop 7\n" \
  "s_nop 1\n" \
  "v_mov_b32 v160, 0\n" \
  "v_mov_b32 v161, 0\n" \
  "v_mov_b32 v162, 0\n" \
  "v_mov_b32 v163, 0\n" \
  "v_mov_b32 v164, 0\n" \
  "v_mov_b32 v165, 0\n" \
  "v_mov_b32 v166, 0\n" \
  "v_mov_b32 v167, 0\n" \
  "v_mov_b32 v168, 0\n" \
  "v_mov_b32 v169, 0\n" \
  "v_mov_b32 v170, 0\n" \
  "v_mov_b32 v171, 0\n" \
  "s_branch Ltop_%=\n" \
  "Lredo_%=:\n" \
  "s_nop 7\n" \
  "s_nop 1\n" \
  "s_sub_u32 s61, s61, 1\n" \
  "s_cmp_lt_i32 s61, 0\n" \
  "s_cbranch_scc1 Lcont_%=\n" \
  "v_subrev_u32 v245, s92, v245\n" \
  "v_mov_b32 v160, 0\n" \
  "v_mov_b32 v161, 0\n" \
  "v_mov_b32 v162, 0\n" \
  "v_mov_b32 v163, 0\n" \
  "v_mov_b32 v164, 0\n" \
  "v_mov_b32 v165, 0\n" \
  "v_mov_b32 v166, 0\n" \
  "v_mov_b32 v167, 0\n" \
  "v_mov_b32 v168, 0\n" \
  "v_mov_b32 v169, 0\n" \
  "v_mov_b32 v170, 0\n" \
  "v_mov_b32 v171, 0\n" \
  "v_mov_b32 v172, 0\n" \
  "v_mov_b32 v173, 0\n" \
  "v_mov_b32 v174, 0\n" \
  "v_mov_b32 v175, 0\n" \
  "v_mov_b32 v176, 0\n" \
  "v_mov_b32 v177, 0\n" \
  "v_mov_b32 v178, 0\n" \
  "v_mov_b32 v179, 0\n" \
  "v_mov_b32 v180, 0\n" \
  "v_mov_b32 v181, 0\n" \
  "v_mov_b32 v182, 0\n" \
  "v_mov_b32 v183, 0\n" \
  "v_mov_b32 v196, 0\n" \
  "v_mov_b32 v197, 0\n" \
  "v_mov_b32 v198, 0\n" \
  "v_mov_b32 v199, 0\n" \
  "s_cmp_lt_u32 s56, 2\n" \
  "s_cbranch_scc1 Lrd7_%=\n" \
  "s_and_b32 s72, s56, 1\n" \
  "s_lshl_b32 s72, s72, 11\n" \
  "s_add_u32 m0, s72, 2048\n" \
  "s_or_b32 s72, s38, 2048\n" \
  "s_xor_b32 s94, s94, s72\n" \
  "s_add_u32 s54, s94, 2048\n" \
  "s_addc_u32 s55, s95, 0\n" \
  "Lw8_%=:\n" \
  "s_memrealtime s[4:5]\n" \
  "s_nop 2\n" \
  "s_memrealtime s[6:7]\n" \
  "s_nop 2\n" \
  "s_memrealtime s[8:9]\n" \
  "s_nop 2\n" \
  "s_memrealtime s[10:11]\n" \
  "s_nop 2\n" \
  "s_memrealtime s[12:13]\n" \
  "s_nop 2\n" \
  "s_memrealtime s[14:15]\n" \
  "s_nop 2\n" \
  "s_memrealtime s[16:17]\n" \
  "s_memtime s[26:27]\n" \
  "s_waitcnt lgkmcnt(0)\n" \
  "s_mov_b32 s65, 0\n" \
  "s_cmp_eq_u32 s6, s4\n" \
  "s_cselect_b32 s75, 8, 0\n" \
  "s_add_u32 s65, s65, s75\n" \
  "s_cmp_eq_u32 s8, s4\n" \
  "s_cselect_b32 s75, 8, 0\n" \
  "s_add_u32 s65, s65, s75\n" \
  "s_cmp_eq_u32 s10, s4\n" \
  "s_cselect_b32 s75, 8, 0\n" \
  "s_add_u32 s65, s65, s75\n" \
  "s_cmp_eq_u32 s12, s4\n" \
  "s_cselect_b32 s75, 8, 0\n" \
  "s_add_u32 s65, s65, s75\n" \
  "s_cmp_eq_u32 s14, s4\n" \
  "s_cselect_b32 s75, 8, 0\n" \
  "s_add_u32 s65, s65, s75\n" \
  "s_cmp_eq_u32 s16, s4\n" \
  "s_cselect_b32 s75, 8, 0\n" \
  "s_add_u32 s65, s65, s75\n" \
  "Lx8_%=:\n" \
  "s_memtime s[6:7]\n" \
  "s_sub_u32 s74, s99, s4\n" \
  "s_mul_i32 s74, s74, 12\n" \
  "s_add_u32 s74, s74, s65\n" \
  "s_waitcnt lgkmcnt(0)\n" \
  "s_sub_u32 s75, s6, s26\n" \
  "s_lshr_b32 s75, s75, 1\n" \
  "s_add_u32 s75, s75, 132\n" \
  "s_sub_u32 s74, s74, s75\n" \
  "s_sub_u32 s75, 383, s74\n" \
  "s_mul_hi_u32 s75, s75, 11184811\n" \
  "s_mul_i32 s75, s75, 32\n" \
  "s_cmp_lt_i32 s74, -24\n" \
  "s_cselect_b32 s75, s75, 0\n" \
  "s_add_u32 s99, s99, s75\n" \
  "s_mul_i32 s75, s75, 12\n" \
  "s_add_u32 s74, s74, s75\n" \
  "s_cmp_gt_i32 s74, 12000\n" \
  "s_cselect_b32 s74, 0, s74\n" \
  "s_cmp_gt_i32 s74, 640\n" \
  "s_cbranch_scc1 Lw8_%=\n" \
  "s_max_i32 s74, s74, 0\n" \
  "s_and_b32 s74, s74, -4\n" \
  "s_getpc_b64 s[70:71]\n" \
  "Lh9_%=:\n" \
  "s_add_u32 s70, s70, Le9_%=-Lh9_%=\n" \
  "s_addc_u32 s71, s71, 0\n" \
  "s_sub_u32 s70, s70, s74\n" \
  "s_subb_u32 s71, s71, 0\n" \
  "s_setpc_b64 s[70:71]\n" \
  "s_nop 1\n" \
  "s_nop 1\n" \
  "s_nop 1\n" \
  "s_nop 1\n" \
  "s_nop 1\n" \
  "s_nop 1\n" \
  "s_nop 1\n" \
  "s_nop 1\n" \
  "s_nop 1\n" \
  "s_nop 1\n" \
  "s_nop 1\n" \
  "s_nop 1\n" \
  "s_nop 1\n" \
  "s_nop 1\n" \
  "s_nop 1\n" \
  "s_nop 1\n" \
  "s_nop 1\n" \
  "s_nop 1\n" \
  "s_nop 1\n" \
  "s_nop 1\n" \
  "s_nop 1\n" \
  "s_nop 1\n" \
  "s_nop 1\n" \
  "s_nop 1\n" \
  "s_nop 1\n" \
  "s_nop 1\n" \
  "s_nop 1\n" \
  "s_nop 1\n" \
  "s_nop 1\n" \
  "s_nop 1\n" \
  "s_nop 1\n" \
  "s_nop 1\n" \
  "s_nop 1\n" \
  "s_nop 1\n" \
  "s_nop 1\n" \
  "s_nop 1\n" \
  "s_nop 1\n" \
  "s_nop 1\n" \
  "s_nop 1\n" \
  "s_nop 1\n" \
  "s_nop 1\n" \
  "s_nop 1\n" \
  "s_nop 1\n" \
  "s_nop 1\n" \
  "s_nop 1\n" \
  "s_nop 1\n" \
  "s_nop 1\n" \
  "s_nop 1\n" \
  "s_nop 1\n" \
  "s_nop 1\n" \
  "s_nop 1\n" \
  "s_nop 1\n" \
  "s_nop 1\n" \
  "s_nop 1\n" \
  "s_nop 1\n" \
  "s_nop 1\n" \
  "s_nop 1\n" \
  "s_nop 1\n" \
  "s_nop 1\n" \
  "s_nop 1\n" \
  "s_nop 1\n" \
  "s_nop 1\n" \
  "s_nop 1\n" \
  "s_nop 1\n" \
  "s_nop 1\n" \
  "s_nop 1\n" \
  "s_nop 1\n" \
  "s_nop 1\n" \
  "s_nop 1\n" \
  "s_nop 1\n" \
  "s_nop 1\n" \
  "s_nop 1\n" \
  "s_nop 1\n" \
  "s_nop 1\n" \
  "s_nop 1\n" \
  "s_nop 1\n" \
  "s_nop 1\n" \
  "s_nop 1\n" \
  "s_nop 1\n" \
  "s_nop 1\n" \
  "s_nop 1\n" \
  "s_nop 1\n" \
  "s_nop 1\n" \
  "s_nop 1\n" \
  "s_nop 1\n" \
  "s_nop 1\n" \
  "s_nop 1\n" \
  "s_nop 1\n" \
  "s_nop 1\n" \
  "s_nop 1\n" \
  "s_nop 1\n" \
  "s_nop 1\n" \
  "s_nop 1\n" \
  "s_nop 1\n" \
  "s_nop 1\n" \
  "s_nop 1\n" \
  "s_nop 1\n" \
  "s_nop 1\n" \
  "s_nop 1\n" \
  "s_nop 1\n" \
  "s_nop 1\n" \
  "s_nop 1\n" \
  "s_nop 1\n" \
  "s_nop 1\n" \
  "s_nop 1\n" \
  "s_nop 1\n" \
  "s_nop 1\n" \
  "s_nop 1\n" \
  "s_nop 1\n" \
  "s_nop 1\n" \
  "s_nop 1\n" \
  "s_nop 1\n" \
  "s_nop 1\n" \
  "s_nop 1\n" \
  "s_nop 1\n" \
  "s_nop 1\n" \
  "s_nop 1\n" \
  "s_nop 1\n" \
  "s_nop 1\n" \
  "s_nop 1\n" \
  "s_nop 1\n" \
  "s_nop 1\n" \
  "s_nop 1\n" \
  "s_nop 1\n" \
  "s_nop 1\n" \
  "s_nop 1\n" \
  "s_nop 1\n" \
  "s_nop 1\n" \
  "s_nop 1\n" \
  "s_nop 1\n" \
  "s_nop 1\n" \
  "s_nop 1\n" \
  "s_nop 1\n" \
  "s_nop 1\n" \
  "s_nop 1\n" \
  "s_nop 1\n" \
  "s_nop 1\n" \
  "s_nop 1\n" \
  "s_nop 1\n" \
  "s_nop 1\n" \
  "s_nop 1\n" \
  "s_nop 1\n" \
  "s_nop 1\n" \
  "s_nop 1\n" \
  "s_nop 1\n" \
  "s_nop 1\n" \
  "s_nop 1\n" \
  "s_nop 1\n" \
  "s_nop 1\n" \
  "s_nop 1\n" \
  "s_nop 1\n" \
  "s_nop 1\n" \
  "s_nop 1\n" \
  "s_nop 1\n" \
  "s_nop 1\n" \
  "s_nop 1\n" \
  "s_nop 1\n" \
  "s_nop 1\n" \
  "s_nop 1\n" \
  "s_nop 1\n" \
  "Le9_%=:\n" \
  "global_load_lds_dwordx4 v241, s[54:55] offset:-2048 sc1\n" \
  "global_load_lds_dwordx4 v241, s[54:55] offset:-1024 sc1\n" \
  "global_load_lds_dwordx4 v241, s[54:55] offset:2048 sc1\n" \
  "global_load_lds_dwordx4 v241, s[54:55] offset:3072 sc1\n" \
  "s_memtime s[4:5]\n" \
  "s_waitcnt lgkmcnt(0)\n" \
  "v_mov_b32 v192, s99\n" \
  "v_mov_b32 v193, s4\n" \
  "s_cmp_eq_u32 s56, 2\n" \
  "s_cselect_b64 s[70:71], -1, 0\n" \
  "s_mov_b64 exec, s[70:71]\n" \
  "ds_write_b64 v248, v[192:193] offset:9216\n" \
  "s_mov_b64 exec, -1\n" \
  "s_waitcnt lgkmcnt(0)\n" \
  "s_lshl_b32 s72, s98, 2\n" \
  "s_add_u32 s72, s72, 0\n" \
  "s_add_u32 s70, s44, s72\n" \
  "s_addc_u32 s71, s45, 0\n" \
  "s_and_b32 s72, s56, 1\n" \
  "s_lshl_b32 s72, s72, 10\n" \
  "s_add_u32 s70, s70, s72\n" \
  "s_addc_u32 s71, s71, 0\n" \
  "s_bitcmp1_b32 s58, 1\n" \
  "s_cselect_b64 s[70:71], s[70:71], s[50:51]\n" \
  "s_add_u32 s72, s99, s60\n" \
  "s_add_u32 s72, s72, 32\n" \
  "v_mov_b32 v194, s72\n" \
  "s_mov_b64 exec, 1\n" \
  "global_store_dword v241, v194, s[70:71] sc1\n" \
  "s_mov_b64 exec, -1\n" \
  "Lrd7_%=:\n" \
  "s_branch Ltop_%=\n" \
  "Lend_%=:\n" \
  "s_waitcnt vmcnt(0) lgkmcnt(0)\n" \
  "s_cmp_eq_u32 s29, 0\n" \
  "s_cbranch_scc0 Lfin2_%=\n" \
  "s_barrier\n" \
  "v_mbcnt_lo_u32_b32 v193, -1, 0\n" \
  "v_mbcnt_hi_u32_b32 v193, -1, v193\n" \
  "v_lshlrev_b32 v192, 2, v193\n" \
  "v_add_u32 v192, 162944, v192\n" \
  "ds_read_b32 v192, v192\n" \
  "s_waitcnt lgkmcnt(0)\n" \
  "v_readlane_b32 s4, v192, 0\n" \
  "v_readlane_b32 s5, v192, 1\n" \
  "v_readlane_b32 s6, v192, 2\n" \
  "v_readlane_b32 s7, v192, 3\n" \
  "v_readlane_b32 s8, v192, 4\n" \
  "v_readlane_b32 s9, v192, 5\n" \
  "v_readlane_b32 s10, v192, 6\n" \
  "v_readlane_b32 s11, v192, 7\n" \
  "v_readlane_b32 s12, v192, 8\n" \
  "v_readlane_b32 s13, v192, 9\n" \
  "v_readlane_b32 s14, v192, 10\n" \
  "v_readlane_b32 s15, v192, 11\n" \
  "v_readlane_b32 s16, v192, 12\n" \
  "v_readlane_b32 s17, v192, 13\n" \
  "v_readlane_b32 s18, v192, 14\n" \
  "v_readlane_b32 s19, v192, 15\n" \
  "v_readlane_b32 s20, v192, 16\n" \
  "v_readlane_b32 s21, v192, 17\n" \
  "v_readlane_b32 s22, v192, 18\n" \
  "v_readlane_b32 s23, v192, 19\n" \
  "v_readlane_b32 s24, v192, 20\n" \
  "v_readlane_b32 s25, v192, 21\n" \
  "v_readlane_b32 s34, v192, 22\n" \
  "v_readlane_b32 s35, v192, 23\n" \
  "v_readlane_b32 s36, v192, 24\n" \
  "v_readlane_b32 s37, v192, 25\n" \
  "v_readlane_b32 s30, v192, 26\n" \
  "v_readlane_b32 s31, v192, 27\n" \
  "v_readlane_b32 s29, v192, 28\n" \
  "v_readlane_b32 s64, v192, 29\n" \
  "v_readlane_b32 s82, v192, 30\n" \
  "v_readlane_b32 s83, v192, 31\n" \
  "s_nop 7\n" \
  "s_nop 1\n" \
  "s_mov_b64 s[40:41], s[10:11]\n" \
  "s_mov_b64 s[44:45], s[12:13]\n" \
  "s_mov_b32 s57, s25\n" \
  "s_bfe_u32 s96, s29, 65537\n" \
  "s_mov_b32 s98, 0\n" \
  "s_branch Llayer_%=\n" \
  "Lfin2_%=:\n" \
  "s_endpgm\n"
#define CHAIN_CLOBBERS "v255", "a255", "s4", "s5", "s6", "s7", "s8", "s9", "s10", "s11", "s12", "s13", "s14", "s15", "s16", "s17", "s18", "s19", "s20", "s21", "s22", "s23", "s24", "s25", "s26", "s27", "s28", "s29", "s30", "s31", "s34", "s35", "s36", "s37", "s38", "s39", "s40", "s41", "s42", "s43", "s44", "s45", "s46", "s47", "s48", "s49", "s50", "s51", "s52", "s53", "s54", "s55", "s56", "s57", "s58", "s59", "s60", "s61", "s62", "s63", "s64", "s65", "s66", "s67", "s68", "s69", "s70", "s71", "s72", "s73", "s74", "s75", "s76", "s77", "s78", "s79", "s80", "s81", "s82", "s83", "s84", "s85", "s86", "s87", "s88", "s89", "s90", "s91", "s92", "s93", "s94", "s95", "s96", "s97", "s98", "s99", "vcc", "scc", "memory"
#define CHAIN_LDS 163840
#define CHAIN_LDS_PAR 162816
#define KV_ASM \
  "s_mov_b32 s40, %[w]\n" \
  "v_mbcnt_lo_u32_b32 v254, -1, 0\n" \
  "v_mbcnt_hi_u32_b32 v254, -1, v254\n" \
  "v_lshlrev_b32 v255, 2, v254\n" \
  "v_add_u32 v255, 162816, v255\n" \
  "ds_read_b32 v255, v255\n" \
  "s_waitcnt lgkmcnt(0)\n" \
  "v_readlane_b32 s70, v255, 0\n" \
  "v_readlane_b32 s71, v255, 1\n" \
  "v_readlane_b32 s72, v255, 2\n" \
  "v_readlane_b32 s73, v255, 3\n" \
  "v_readlane_b32 s74, v255, 4\n" \
  "v_readlane_b32 s75, v255, 5\n" \
  "v_readlane_b32 s76, v255, 6\n" \
  "v_readlane_b32 s77, v255, 7\n" \
  "v_readlane_b32 s78, v255, 8\n" \
  "v_readlane_b32 s79, v255, 9\n" \
  "v_readlane_b32 s80, v255, 10\n" \
  "v_readlane_b32 s81, v255, 11\n" \
  "v_readlane_b32 s82, v255, 12\n" \
  "v_readlane_b32 s83, v255, 13\n" \
  "v_readlane_b32 s84, v255, 14\n" \
  "v_readlane_b32 s85, v255, 15\n" \
  "v_readlane_b32 s86, v255, 16\n" \
  "v_readlane_b32 s87, v255, 17\n" \
  "v_readlane_b32 s88, v255, 18\n" \
  "v_readlane_b32 s89, v255, 19\n" \
  "v_readlane_b32 s90, v255, 20\n" \
  "v_readlane_b32 s91, v255, 21\n" \
  "v_readlane_b32 s92, v255, 22\n" \
  "v_readlane_b32 s93, v255, 23\n" \
  "v_readlane_b32 s94, v255, 24\n" \
  "v_readlane_b32 s95, v255, 25\n" \
  "v_readlane_b32 s96, v255, 26\n" \
  "v_readlane_b32 s97, v255, 27\n" \
  "v_readlane_b32 s98, v255, 28\n" \
  "v_readlane_b32 s99, v255, 29\n" \
  "v_readlane_b32 s34, v255, 32\n" \
  "v_readlane_b32 s35, v255, 33\n" \
  "v_readlane_b32 s36, v255, 34\n" \
  "v_readlane_b32 s37, v255, 35\n" \
  "v_readlane_b32 s38, v255, 36\n" \
  "v_readlane_b32 s39, v255, 37\n" \
  "s_mov_b32 s69, 0\n" \
  "s_nop 7\n" \
  "s_nop 1\n" \
  "s_mov_b64 s[4:5], s[70:71]\n" \
  "s_mov_b64 s[6:7], s[72:73]\n" \
  "s_mov_b64 s[8:9], s[74:75]\n" \
  "s_mov_b64 s[10:11], s[76:77]\n" \
  "s_mov_b64 s[12:13], s[78:79]\n" \
  "s_mov_b64 s[14:15], s[80:81]\n" \
  "s_mov_b64 s[16:17], s[82:83]\n" \
  "s_mov_b64 s[18:19], s[84:85]\n" \
  "v_mov_b32 v255, 0\n" \
  "s_mov_b32 s41, 400000\n" \
  "s_cmp_eq_u32 s40, 0\n" \
  "s_cselect_b64 s[60:61], 1, 0\n" \
  "s_nop 7\n" \
  "s_nop 1\n" \
  "s_barrier\n" \
  "s_and_b32 s43, s40, 1\n" \
  "s_lshl_b32 s43, s43, 7\n" \
  "s_cmp_lt_u32 s40, 2\n" \
  "s_cselect_b64 s[48:49], s[4:5], s[6:7]\n" \
  "s_add_u32 s48, s48, s43\n" \
  "s_addc_u32 s49, s49, 0\n" \
  "s_lshl_b32 s44, s40, 15\n" \
  "s_add_u32 s44, s44, 32768\n" \
  "v_lshrrev_b32 v121, 3, v254\n" \
  "v_and_b32 v120, 7, v254\n" \
  "v_bfe_u32 v122, v121, 1, 1\n" \
  "v_lshlrev_b32 v122, 2, v122\n" \
  "v_xor_b32 v120, v120, v122\n" \
  "v_lshlrev_b32 v120, 4, v120\n" \
  "v_mul_u32_u24 v121, 8192, v121\n" \
  "v_add_u32 v120, v120, v121\n" \
  "v_lshrrev_b32 v121, 4, v254\n" \
  "v_lshlrev_b32 v121, 3, v121\n" \
  "v_bfe_u32 v122, v254, 2, 2\n" \
  "v_add_u32 v121, v121, v122\n" \
  "v_mul_u32_u24 v121, 128, v121\n" \
  "v_and_b32 v122, 3, v254\n" \
  "v_lshl_add_u32 v121, v122, 4, v121\n" \
  "v_bfe_u32 v122, v254, 3, 1\n" \
  "v_lshl_add_u32 v121, v122, 6, v121\n" \
  "v_add_u32 v121, s44, v121\n" \
  "v_xor_b32 v122, 64, v121\n" \
  "s_add_u32 m0, s44, 0\n" \
  "s_nop 0\n" \
  "global_load_lds_dwordx4 v120, s[48:49]\n" \
  "s_add_u32 s48, s48, 65536\n" \
  "s_addc_u32 s49, s49, 0\n" \
  "s_add_u32 m0, s44, 1024\n" \
  "s_nop 0\n" \
  "global_load_lds_dwordx4 v120, s[48:49]\n" \
  "s_add_u32 s48, s48, 65536\n" \
  "s_addc_u32 s49, s49, 0\n" \
  "s_add_u32 m0, s44, 2048\n" \
  "s_nop 0\n" \
  "global_load_lds_dwordx4 v120, s[48:49]\n" \
  "s_add_u32 s48, s48, 65536\n" \
  "s_addc_u32 s49, s49, 0\n" \
  "s_add_u32 m0, s44, 3072\n" \
  "s_nop 0\n" \
  "global_load_lds_dwordx4 v120, s[48:49]\n" \
  "s_add_u32 s48, s48, 65536\n" \
  "s_addc_u32 s49, s49, 0\n" \
  "s_add_u32 m0, s44, 4096\n" \
  "s_nop 0\n" \
  "global_load_lds_dwordx4 v120, s[48:49]\n" \
  "s_add_u32 s48, s48, 65536\n" \
  "s_addc_u32 s49, s49, 0\n" \
  "s_add_u32 m0, s44, 5120\n" \
  "s_nop 0\n" \
  "global_load_lds_dwordx4 v120, s[48:49]\n" \
  "s_add_u32 s48, s48, 65536\n" \
  "s_addc_u32 s49, s49, 0\n" \
  "s_add_u32 m0, s44, 6144\n" \
  "s_nop 0\n" \
  "global_load_lds_dwordx4 v120, s[48:49]\n" \
  "s_add_u32 s48, s48, 65536\n" \
  "s_addc_u32 s49, s49, 0\n" \
  "s_add_u32 m0, s44, 7168\n" \
  "s_nop 0\n" \
  "global_load_lds_dwordx4 v120, s[48:49]\n" \
  "s_add_u32 s48, s48, 65536\n" \
  "s_addc_u32 s49, s49, 0\n" \
  "s_add_u32 m0, s44, 8192\n" \
  "s_nop 0\n" \
  "global_load_lds_dwordx4 v120, s[48:49]\n" \
  "s_add_u32 s48, s48, 65536\n" \
  "s_addc_u32 s49, s49, 0\n" \
  "s_add_u32 m0, s44, 9216\n" \
  "s_nop 0\n" \
  "global_load_lds_dwordx4 v120, s[48:49]\n" \
  "s_add_u32 s48, s48, 65536\n" \
  "s_addc_u32 s49, s49, 0\n" \
  "s_add_u32 m0, s44, 10240\n" \
  "s_nop 0\n" \
  "global_load_lds_dwordx4 v120, s[48:49]\n" \
  "s_add_u32 s48, s48, 65536\n" \
  "s_addc_u32 s49, s49, 0\n" \
  "s_add_u32 m0, s44, 11264\n" \
  "s_nop 0\n" \
  "global_load_lds_dwordx4 v120, s[48:49]\n" \
  "s_add_u32 s48, s48, 65536\n" \
  "s_addc_u32 s49, s49, 0\n" \
  "s_add_u32 m0, s44, 12288\n" \
  "s_nop 0\n" \
  "global_load_lds_dwordx4 v120, s[48:49]\n" \
  "s_add_u32 s48, s48, 65536\n" \
  "s_addc_u32 s49, s49, 0\n" \
  "s_add_u32 m0, s44, 13312\n" \
  "s_nop 0\n" \
  "global_load_lds_dwordx4 v120, s[48:49]\n" \
  "s_add_u32 s48, s48, 65536\n" \
  "s_addc_u32 s49, s49, 0\n" \
  "s_add_u32 m0, s44, 14336\n" \
  "s_nop 0\n" \
  "global_load_lds_dwordx4 v120, s[48:49]\n" \
  "s_add_u32 s48, s48, 65536\n" \
  "s_addc_u32 s49, s49, 0\n" \
  "s_add_u32 m0, s44, 15360\n" \
  "s_nop 0\n" \
  "global_load_lds_dwordx4 v120, s[48:49]\n" \
  "s_add_u32 s48, s48, 65536\n" \
  "s_addc_u32 s49, s49, 0\n" \
  "s_add_u32 m0, s44, 16384\n" \
  "s_nop 0\n" \
  "global_load_lds_dwordx4 v120, s[48:49]\n" \
  "s_add_u32 s48, s48, 65536\n" \
  "s_addc_u32 s49, s49, 0\n" \
  "s_add_u32 m0, s44, 17408\n" \
  "s_nop 0\n" \
  "global_load_lds_dwordx4 v120, s[48:49]\n" \
  "s_add_u32 s48, s48, 65536\n" \
  "s_addc_u32 s49, s49, 0\n" \
  "s_add_u32 m0, s44, 18432\n" \
  "s_nop 0\n" \
  "global_load_lds_dwordx4 v120, s[48:49]\n" \
  "s_add_u32 s48, s48, 65536\n" \
  "s_addc_u32 s49, s49, 0\n" \
  "s_add_u32 m0, s44, 19456\n" \
  "s_nop 0\n" \
  "global_load_lds_dwordx4 v120, s[48:49]\n" \
  "s_add_u32 s48, s48, 65536\n" \
  "s_addc_u32 s49, s49, 0\n" \
  "s_add_u32 m0, s44, 20480\n" \
  "s_nop 0\n" \
  "global_load_lds_dwordx4 v120, s[48:49]\n" \
  "s_add_u32 s48, s48, 65536\n" \
  "s_addc_u32 s49, s49, 0\n" \
  "s_add_u32 m0, s44, 21504\n" \
  "s_nop 0\n" \
  "global_load_lds_dwordx4 v120, s[48:49]\n" \
  "s_add_u32 s48, s48, 65536\n" \
  "s_addc_u32 s49, s49, 0\n" \
  "s_add_u32 m0, s44, 22528\n" \
  "s_nop 0\n" \
  "global_load_lds_dwordx4 v120, s[48:49]\n" \
  "s_add_u32 s48, s48, 65536\n" \
  "s_addc_u32 s49, s49, 0\n" \
  "s_add_u32 m0, s44, 23552\n" \
  "s_nop 0\n" \
  "global_load_lds_dwordx4 v120, s[48:49]\n" \
  "s_add_u32 s48, s48, 65536\n" \
  "s_addc_u32 s49, s49, 0\n" \
  "s_waitcnt vmcnt(16)\n" \
  "ds_read_b64_tr_b16 a[0:1], v121\n" \
  "ds_read_b64_tr_b16 a[2:3], v121 offset:512\n" \
  "ds_read_b64_tr_b16 a[4:5], v121 offset:8\n" \
  "ds_read_b64_tr_b16 a[6:7], v121 offset:520\n" \
  "ds_read_b64_tr_b16 a[8:9], v122\n" \
  "ds_read_b64_tr_b16 a[10:11], v122 offset:512\n" \
  "ds_read_b64_tr_b16 a[12:13], v122 offset:8\n" \
  "ds_read_b64_tr_b16 a[14:15], v122 offset:520\n" \
  "ds_read_b64_tr_b16 a[16:17], v121 offset:4096\n" \
  "ds_read_b64_tr_b16 a[18:19], v121 offset:4608\n" \
  "ds_read_b64_tr_b16 a[20:21], v121 offset:4104\n" \
  "ds_read_b64_tr_b16 a[22:23], v121 offset:4616\n" \
  "s_waitcnt lgkmcnt(4)\n" \
  "ds_read_b64_tr_b16 a[24:25], v122 offset:4096\n" \
  "ds_read_b64_tr_b16 a[26:27], v122 offset:4608\n" \
  "ds_read_b64_tr_b16 a[28:29], v122 offset:4104\n" \
  "ds_read_b64_tr_b16 a[30:31], v122 offset:4616\n" \
  "s_add_u32 m0, s44, 24576\n" \
  "s_nop 0\n" \
  "global_load_lds_dwordx4 v120, s[48:49]\n" \
  "s_add_u32 s48, s48, 65536\n" \
  "s_addc_u32 s49, s49, 0\n" \
  "s_add_u32 m0, s44, 25600\n" \
  "s_nop 0\n" \
  "global_load_lds_dwordx4 v120, s[48:49]\n" \
  "s_add_u32 s48, s48, 65536\n" \
  "s_addc_u32 s49, s49, 0\n" \
  "s_add_u32 m0, s44, 26624\n" \
  "s_nop 0\n" \
  "global_load_lds_dwordx4 v120, s[48:49]\n" \
  "s_add_u32 s48, s48, 65536\n" \
  "s_addc_u32 s49, s49, 0\n" \
  "s_add_u32 m0, s44, 27648\n" \
  "s_nop 0\n" \
  "global_load_lds_dwordx4 v120, s[48:49]\n" \
  "s_add_u32 s48, s48, 65536\n" \
  "s_addc_u32 s49, s49, 0\n" \
  "s_add_u32 m0, s44, 28672\n" \
  "s_nop 0\n" \
  "global_load_lds_dwordx4 v120, s[48:49]\n" \
  "s_add_u32 s48, s48, 65536\n" \
  "s_addc_u32 s49, s49, 0\n" \
  "s_add_u32 m0, s44, 29696\n" \
  "s_nop 0\n" \
  "global_load_lds_dwordx4 v120, s[48:49]\n" \
  "s_add_u32 s48, s48, 65536\n" \
  "s_addc_u32 s49, s49, 0\n" \
  "s_add_u32 m0, s44, 30720\n" \
  "s_nop 0\n" \
  "global_load_lds_dwordx4 v120, s[48:49]\n" \
  "s_add_u32 s48, s48, 65536\n" \
  "s_addc_u32 s49, s49, 0\n" \
  "s_add_u32 m0, s44, 31744\n" \
  "s_nop 0\n" \
  "global_load_lds_dwordx4 v120, s[48:49]\n" \
  "s_add_u32 s48, s48, 65536\n" \
  "s_addc_u32 s49, s49, 0\n" \
  "s_waitcnt lgkmcnt(0)\n" \
  "s_waitcnt vmcnt(16)\n" \
  "ds_read_b64_tr_b16 a[32:33], v121 offset:8192\n" \
  "ds_read_b64_tr_b16 a[34:35], v121 offset:8704\n" \
  "ds_read_b64_tr_b16 a[36:37], v121 offset:8200\n" \
  "ds_read_b64_tr_b16 a[38:39], v121 offset:8712\n" \
  "ds_read_b64_tr_b16 a[40:41], v122 offset:8192\n" \
  "ds_read_b64_tr_b16 a[42:43], v122 offset:8704\n" \
  "ds_read_b64_tr_b16 a[44:45], v122 offset:8200\n" \
  "ds_read_b64_tr_b16 a[46:47], v122 offset:8712\n" \
  "ds_read_b64_tr_b16 a[48:49], v121 offset:12288\n" \
  "ds_read_b64_tr_b16 a[50:51], v121 offset:12800\n" \
  "ds_read_b64_tr_b16 a[52:53], v121 offset:12296\n" \
  "ds_read_b64_tr_b16 a[54:55], v121 offset:12808\n" \
  "s_waitcnt lgkmcnt(4)\n" \
  "ds_read_b64_tr_b16 a[56:57], v122 offset:12288\n" \
  "ds_read_b64_tr_b16 a[58:59], v122 offset:12800\n" \
  "ds_read_b64_tr_b16 a[60:61], v122 offset:12296\n" \
  "ds_read_b64_tr_b16 a[62:63], v122 offset:12808\n" \
  "s_add_u32 m0, s44, 0\n" \
  "s_nop 0\n" \
  "global_load_lds_dwordx4 v120, s[48:49]\n" \
  "s_add_u32 s48, s48, 65536\n" \
  "s_addc_u32 s49, s49, 0\n" \
  "s_add_u32 m0, s44, 1024\n" \
  "s_nop 0\n" \
  "global_load_lds_dwordx4 v120, s[48:49]\n" \
  "s_add_u32 s48, s48, 65536\n" \
  "s_addc_u32 s49, s49, 0\n" \
  "s_add_u32 m0, s44, 2048\n" \
  "s_nop 0\n" \
  "global_load_lds_dwordx4 v120, s[48:49]\n" \
  "s_add_u32 s48, s48, 65536\n" \
  "s_addc_u32 s49, s49, 0\n" \
  "s_add_u32 m0, s44, 3072\n" \
  "s_nop 0\n" \
  "global_load_lds_dwordx4 v120, s[48:49]\n" \
  "s_add_u32 s48, s48, 65536\n" \
  "s_addc_u32 s49, s49, 0\n" \
  "s_add_u32 m0, s44, 4096\n" \
  "s_nop 0\n" \
  "global_load_lds_dwordx4 v120, s[48:49]\n" \
  "s_add_u32 s48, s48, 65536\n" \
  "s_addc_u32 s49, s49, 0\n" \
  "s_add_u32 m0, s44, 5120\n" \
  "s_nop 0\n" \
  "global_load_lds_dwordx4 v120, s[48:49]\n" \
  "s_add_u32 s48, s48, 65536\n" \
  "s_addc_u32 s49, s49, 0\n" \
  "s_add_u32 m0, s44, 6144\n" \
  "s_nop 0\n" \
  "global_load_lds_dwordx4 v120, s[48:49]\n" \
  "s_add_u32 s48, s48, 65536\n" \
  "s_addc_u32 s49, s49, 0\n" \
  "s_add_u32 m0, s44, 7168\n" \
  "s_nop 0\n" \
  "global_load_lds_dwordx4 v120, s[48:49]\n" \
  "s_add_u32 s48, s48, 65536\n" \
  "s_addc_u32 s49, s49, 0\n" \
  "s_waitcnt lgkmcnt(0)\n" \
  "s_waitcnt vmcnt(16)\n" \
  "ds_read_b64_tr_b16 a[64:65], v121 offset:16384\n" \
  "ds_read_b64_tr_b16 a[66:67], v121 offset:16896\n" \
  "ds_read_b64_tr_b16 a[68:69], v121 offset:16392\n" \
  "ds_read_b64_tr_b16 a[70:71], v121 offset:16904\n" \
  "ds_read_b64_tr_b16 a[72:73], v122 offset:16384\n" \
  "ds_read_b64_tr_b16 a[74:75], v122 offset:16896\n" \
  "ds_read_b64_tr_b16 a[76:77], v122 offset:16392\n" \
  "ds_read_b64_tr_b16 a[78:79], v122 offset:16904\n" \
  "ds_read_b64_tr_b16 a[80:81], v121 offset:20480\n" \
  "ds_read_b64_tr_b16 a[82:83], v121 offset:20992\n" \
  "ds_read_b64_tr_b16 a[84:85], v121 offset:20488\n" \
  "ds_read_b64_tr_b16 a[86:87], v121 offset:21000\n" \
  "s_waitcnt lgkmcnt(4)\n" \
  "ds_read_b64_tr_b16 a[88:89], v122 offset:20480\n" \
  "ds_read_b64_tr_b16 a[90:91], v122 offset:20992\n" \
  "ds_read_b64_tr_b16 a[92:93], v122 offset:20488\n" \
  "ds_read_b64_tr_b16 a[94:95], v122 offset:21000\n" \
  "s_add_u32 m0, s44, 8192\n" \
  "s_nop 0\n" \
  "global_load_lds_dwordx4 v120, s[48:49]\n" \
  "s_add_u32 s48, s48, 65536\n" \
  "s_addc_u32 s49, s49, 0\n" \
  "s_add_u32 m0, s44, 9216\n" \
  "s_nop 0\n" \
  "global_load_lds_dwordx4 v120, s[48:49]\n" \
  "s_add_u32 s48, s48, 65536\n" \
  "s_addc_u32 s49, s49, 0\n" \
  "s_add_u32 m0, s44, 10240\n" \
  "s_nop 0\n" \
  "global_load_lds_dwordx4 v120, s[48:49]\n" \
  "s_add_u32 s48, s48, 65536\n" \
  "s_addc_u32 s49, s49, 0\n" \
  "s_add_u32 m0, s44, 11264\n" \
  "s_nop 0\n" \
  "global_load_lds_dwordx4 v120, s[48:49]\n" \
  "s_add_u32 s48, s48, 65536\n" \
  "s_addc_u32 s49, s49, 0\n" \
  "s_add_u32 m0, s44, 12288\n" \
  "s_nop 0\n" \
  "global_load_lds_dwordx4 v120, s[48:49]\n" \
  "s_add_u32 s48, s48, 65536\n" \
  "s_addc_u32 s49, s49, 0\n" \
  "s_add_u32 m0, s44, 13312\n" \
  "s_nop 0\n" \
  "global_load_lds_dwordx4 v120, s[48:49]\n" \
  "s_add_u32 s48, s48, 65536\n" \
  "s_addc_u32 s49, s49, 0\n" \
  "s_add_u32 m0, s44, 14336\n" \
  "s_nop 0\n" \
  "global_load_lds_dwordx4 v120, s[48:49]\n" \
  "s_add_u32 s48, s48, 65536\n" \
  "s_addc_u32 s49, s49, 0\n" \
  "s_add_u32 m0, s44, 15360\n" \
  "s_nop 0\n" \
  "global_load_lds_dwordx4 v120, s[48:49]\n" \
  "s_add_u32 s48, s48, 65536\n" \
  "s_addc_u32 s49, s49, 0\n" \
  "s_waitcnt lgkmcnt(0)\n" \
  "s_waitcnt vmcnt(16)\n" \
  "ds_read_b64_tr_b16 a[96:97], v121 offset:24576\n" \
  "ds_read_b64_tr_b16 a[98:99], v121 offset:25088\n" \
  "ds_read_b64_tr_b16 a[100:101], v121 offset:24584\n" \
  "ds_read_b64_tr_b16 a[102:103], v121 offset:25096\n" \
  "ds_read_b64_tr_b16 a[104:105], v122 offset:24576\n" \
  "ds_read_b64_tr_b16 a[106:107], v122 offset:25088\n" \
  "ds_read_b64_tr_b16 a[108:109], v122 offset:24584\n" \
  "ds_read_b64_tr_b16 a[110:111], v122 offset:25096\n" \
  "ds_read_b64_tr_b16 a[112:113], v121 offset:28672\n" \
  "ds_read_b64_tr_b16 a[114:115], v121 offset:29184\n" \
  "ds_read_b64_tr_b16 a[116:117], v121 offset:28680\n" \
  "ds_read_b64_tr_b16 a[118:119], v121 offset:29192\n" \
  "s_waitcnt lgkmcnt(4)\n" \
  "ds_read_b64_tr_b16 a[120:121], v122 offset:28672\n" \
  "ds_read_b64_tr_b16 a[122:123], v122 offset:29184\n" \
  "ds_read_b64_tr_b16 a[124:125], v122 offset:28680\n" \
  "ds_read_b64_tr_b16 a[126:127], v122 offset:29192\n" \
  "s_add_u32 m0, s44, 16384\n" \
  "s_nop 0\n" \
  "global_load_lds_dwordx4 v120, s[48:49]\n" \
  "s_add_u32 s48, s48, 65536\n" \
  "s_addc_u32 s49, s49, 0\n" \
  "s_add_u32 m0, s44, 17408\n" \
  "s_nop 0\n" \
  "global_load_lds_dwordx4 v120, s[48:49]\n" \
  "s_add_u32 s48, s48, 65536\n" \
  "s_addc_u32 s49, s49, 0\n" \
  "s_add_u32 m0, s44, 18432\n" \
  "s_nop 0\n" \
  "global_load_lds_dwordx4 v120, s[48:49]\n" \
  "s_add_u32 s48, s48, 65536\n" \
  "s_addc_u32 s49, s49, 0\n" \
  "s_add_u32 m0, s44, 19456\n" \
  "s_nop 0\n" \
  "global_load_lds_dwordx4 v120, s[48:49]\n" \
  "s_add_u32 s48, s48, 65536\n" \
  "s_addc_u32 s49, s49, 0\n" \
  "s_add_u32 m0, s44, 20480\n" \
  "s_nop 0\n" \
  "global_load_lds_dwordx4 v120, s[48:49]\n" \
  "s_add_u32 s48, s48, 65536\n" \
  "s_addc_u32 s49, s49, 0\n" \
  "s_add_u32 m0, s44, 21504\n" \
  "s_nop 0\n" \
  "global_load_lds_dwordx4 v120, s[48:49]\n" \
  "s_add_u32 s48, s48, 65536\n" \
  "s_addc_u32 s49, s49, 0\n" \
  "s_add_u32 m0, s44, 22528\n" \
  "s_nop 0\n" \
  "global_load_lds_dwordx4 v120, s[48:49]\n" \
  "s_add_u32 s48, s48, 65536\n" \
  "s_addc_u32 s49, s49, 0\n" \
  "s_add_u32 m0, s44, 23552\n" \
  "s_nop 0\n" \
  "global_load_lds_dwordx4 v120, s[48:49]\n" \
  "s_add_u32 s48, s48, 65536\n" \
  "s_addc_u32 s49, s49, 0\n" \
  "s_waitcnt lgkmcnt(0)\n" \
  "s_waitcnt vmcnt(16)\n" \
  "ds_read_b64_tr_b16 a[128:129], v121\n" \
  "ds_read_b64_tr_b16 a[130:131], v121 offset:512\n" \
  "ds_read_b64_tr_b16 a[132:133], v121 offset:8\n" \
  "ds_read_b64_tr_b16 a[134:135], v121 offset:520\n" \
  "ds_read_b64_tr_b16 a[136:137], v122\n" \
  "ds_read_b64_tr_b16 a[138:139], v122 offset:512\n" \
  "ds_read_b64_tr_b16 a[140:141], v122 offset:8\n" \
  "ds_read_b64_tr_b16 a[142:143], v122 offset:520\n" \
  "ds_read_b64_tr_b16 a[144:145], v121 offset:4096\n" \
  "ds_read_b64_tr_b16 a[146:147], v121 offset:4608\n" \
  "ds_read_b64_tr_b16 a[148:149], v121 offset:4104\n" \
  "ds_read_b64_tr_b16 a[150:151], v121 offset:4616\n" \
  "s_waitcnt lgkmcnt(4)\n" \
  "ds_read_b64_tr_b16 a[152:153], v122 offset:4096\n" \
  "ds_read_b64_tr_b16 a[154:155], v122 offset:4608\n" \
  "ds_read_b64_tr_b16 a[156:157], v122 offset:4104\n" \
  "ds_read_b64_tr_b16 a[158:159], v122 offset:4616\n" \
  "s_add_u32 m0, s44, 24576\n" \
  "s_nop 0\n" \
  "global_load_lds_dwordx4 v120, s[48:49]\n" \
  "s_add_u32 s48, s48, 65536\n" \
  "s_addc_u32 s49, s49, 0\n" \
  "s_add_u32 m0, s44, 25600\n" \
  "s_nop 0\n" \
  "global_load_lds_dwordx4 v120, s[48:49]\n" \
  "s_add_u32 s48, s48, 65536\n" \
  "s_addc_u32 s49, s49, 0\n" \
  "s_add_u32 m0, s44, 26624\n" \
  "s_nop 0\n" \
  "global_load_lds_dwordx4 v120, s[48:49]\n" \
  "s_add_u32 s48, s48, 65536\n" \
  "s_addc_u32 s49, s49, 0\n" \
  "s_add_u32 m0, s44, 27648\n" \
  "s_nop 0\n" \
  "global_load_lds_dwordx4 v120, s[48:49]\n" \
  "s_add_u32 s48, s48, 65536\n" \
  "s_addc_u32 s49, s49, 0\n" \
  "s_add_u32 m0, s44, 28672\n" \
  "s_nop 0\n" \
  "global_load_lds_dwordx4 v120, s[48:49]\n" \
  "s_add_u32 s48, s48, 65536\n" \
  "s_addc_u32 s49, s49, 0\n" \
  "s_add_u32 m0, s44, 29696\n" \
  "s_nop 0\n" \
  "global_load_lds_dwordx4 v120, s[48:49]\n" \
  "s_add_u32 s48, s48, 65536\n" \
  "s_addc_u32 s49, s49, 0\n" \
  "s_add_u32 m0, s44, 30720\n" \
  "s_nop 0\n" \
  "global_load_lds_dwordx4 v120, s[48:49]\n" \
  "s_add_u32 s48, s48, 65536\n" \
  "s_addc_u32 s49, s49, 0\n" \
  "s_add_u32 m0, s44, 31744\n" \
  "s_nop 0\n" \
  "global_load_lds_dwordx4 v120, s[48:49]\n" \
  "s_add_u32 s48, s48, 65536\n" \
  "s_addc_u32 s49, s49, 0\n" \
  "s_waitcnt lgkmcnt(0)\n" \
  "s_waitcnt vmcnt(16)\n" \
  "ds_read_b64_tr_b16 a[160:161], v121 offset:8192\n" \
  "ds_read_b64_tr_b16 a[162:163], v121 offset:8704\n" \
  "ds_read_b64_tr_b16 a[164:165], v121 offset:8200\n" \
  "ds_read_b64_tr_b16 a[166:167], v121 offset:8712\n" \
  "ds_read_b64_tr_b16 a[168:169], v122 offset:8192\n" \
  "ds_read_b64_tr_b16 a[170:171], v122 offset:8704\n" \
  "ds_read_b64_tr_b16 a[172:173], v122 offset:8200\n" \
  "ds_read_b64_tr_b16 a[174:175], v122 offset:8712\n" \
  "ds_read_b64_tr_b16 a[176:177], v121 offset:12288\n" \
  "ds_read_b64_tr_b16 a[178:179], v121 offset:12800\n" \
  "ds_read_b64_tr_b16 a[180:181], v121 offset:12296\n" \
  "ds_read_b64_tr_b16 a[182:183], v121 offset:12808\n" \
  "s_waitcnt lgkmcnt(4)\n" \
  "ds_read_b64_tr_b16 a[184:185], v122 offset:12288\n" \
  "ds_read_b64_tr_b16 a[186:187], v122 offset:12800\n" \
  "ds_read_b64_tr_b16 a[188:189], v122 offset:12296\n" \
  "ds_read_b64_tr_b16 a[190:191], v122 offset:12808\n" \
  "s_waitcnt lgkmcnt(0)\n" \
  "s_waitcnt vmcnt(8)\n" \
  "ds_read_b64_tr_b16 a[192:193], v121 offset:16384\n" \
  "ds_read_b64_tr_b16 a[194:195], v121 offset:16896\n" \
  "ds_read_b64_tr_b16 a[196:197], v121 offset:16392\n" \
  "ds_read_b64_tr_b16 a[198:199], v121 offset:16904\n" \
  "ds_read_b64_tr_b16 a[200:201], v122 offset:16384\n" \
  "ds_read_b64_tr_b16 a[202:203], v122 offset:16896\n" \
  "ds_read_b64_tr_b16 a[204:205], v122 offset:16392\n" \
  "ds_read_b64_tr_b16 a[206:207], v122 offset:16904\n" \
  "ds_read_b64_tr_b16 a[208:209], v121 offset:20480\n" \
  "ds_read_b64_tr_b16 a[210:211], v121 offset:20992\n" \
  "ds_read_b64_tr_b16 a[212:213], v121 offset:20488\n" \
  "ds_read_b64_tr_b16 a[214:215], v121 offset:21000\n" \
  "s_waitcnt lgkmcnt(4)\n" \
  "ds_read_b64_tr_b16 a[216:217], v122 offset:20480\n" \
  "ds_read_b64_tr_b16 a[218:219], v122 offset:20992\n" \
  "ds_read_b64_tr_b16 a[220:221], v122 offset:20488\n" \
  "ds_read_b64_tr_b16 a[222:223], v122 offset:21000\n" \
  "s_waitcnt lgkmcnt(0)\n" \
  "s_waitcnt vmcnt(0)\n" \
  "ds_read_b64_tr_b16 a[224:225], v121 offset:24576\n" \
  "ds_read_b64_tr_b16 a[226:227], v121 offset:25088\n" \
  "ds_read_b64_tr_b16 a[228:229], v121 offset:24584\n" \
  "ds_read_b64_tr_b16 a[230:231], v121 offset:25096\n" \
  "ds_read_b64_tr_b16 a[232:233], v122 offset:24576\n" \
  "ds_read_b64_tr_b16 a[234:235], v122 offset:25088\n" \
  "ds_read_b64_tr_b16 a[236:237], v122 offset:24584\n" \
  "ds_read_b64_tr_b16 a[238:239], v122 offset:25096\n" \
  "ds_read_b64_tr_b16 a[240:241], v121 offset:28672\n" \
  "ds_read_b64_tr_b16 a[242:243], v121 offset:29184\n" \
  "ds_read_b64_tr_b16 a[244:245], v121 offset:28680\n" \
  "ds_read_b64_tr_b16 a[246:247], v121 offset:29192\n" \
  "s_waitcnt lgkmcnt(4)\n" \
  "ds_read_b64_tr_b16 a[248:249], v122 offset:28672\n" \
  "ds_read_b64_tr_b16 a[250:251], v122 offset:29184\n" \
  "ds_read_b64_tr_b16 a[252:253], v122 offset:28680\n" \
  "ds_read_b64_tr_b16 a[254:255], v122 offset:29192\n" \
  "s_waitcnt lgkmcnt(0)\n" \
  "s_barrier\n" \
  "Lkvlayer_%=:\n" \
  "s_mov_b64 s[4:5], s[70:71]\n" \
  "s_mov_b64 s[6:7], s[72:73]\n" \
  "s_mov_b64 s[8:9], s[74:75]\n" \
  "s_mov_b64 s[10:11], s[76:77]\n" \
  "s_mov_b64 s[12:13], s[78:79]\n" \
  "s_mov_b64 s[14:15], s[80:81]\n" \
  "s_mov_b64 s[16:17], s[82:83]\n" \
  "s_mov_b64 s[18:19], s[84:85]\n" \
  "s_lshl_b32 s42, s69, 16\n" \
  "s_add_u32 s14, s14, s42\n" \
  "s_addc_u32 s15, s15, 0\n" \
  "s_add_u32 s16, s16, s42\n" \
  "s_addc_u32 s17, s17, 0\n" \
  "s_and_b32 s42, s40, 1\n" \
  "s_lshl_b32 s42, s42, 3\n" \
  "v_xor_b32 v112, s42, v254\n" \
  "v_lshlrev_b32 v112, 4, v112\n" \
  "v_xor_b32 v113, 16, v112\n" \
  "v_xor_b32 v114, 32, v112\n" \
  "v_xor_b32 v115, 48, v112\n" \
  "v_xor_b32 v116, 64, v112\n" \
  "v_xor_b32 v117, 80, v112\n" \
  "v_xor_b32 v118, 96, v112\n" \
  "v_xor_b32 v119, 112, v112\n" \
  "s_lshl_b32 s53, s40, 13\n" \
  "s_add_u32 s8, s8, s53\n" \
  "s_addc_u32 s9, s9, 0\n" \
  "v_and_b32 v107, 15, v254\n" \
  "v_lshrrev_b32 v108, 4, v254\n" \
  "v_lshlrev_b32 v105, 3, v254\n" \
  "v_lshlrev_b32 v99, 3, v254\n" \
  "s_cmp_eq_u32 s40, 0\n" \
  "s_cselect_b64 s[26:27], 0xffff, 0\n" \
  "v_lshlrev_b32 v106, 4, v107\n" \
  "v_lshl_add_u32 v106, v108, 9, v106\n" \
  "s_lshl_b32 s42, s40, 12\n" \
  "v_add_u32 v106, s42, v106\n" \
  "s_and_b32 s43, s40, 1\n" \
  "s_lshl_b32 s43, s43, 7\n" \
  "v_and_b32 v107, 15, v254\n" \
  "v_lshrrev_b32 v100, 4, v254\n" \
  "v_xor_b32 v100, v100, v107\n" \
  "v_lshlrev_b32 v100, 4, v100\n" \
  "v_lshl_add_u32 v100, v107, 10, v100\n" \
  "v_xor_b32 v101, 64, v100\n" \
  "v_xor_b32 v102, 128, v100\n" \
  "v_xor_b32 v103, 192, v100\n" \
  "v_mov_b32 v109, 1\n" \
  "v_mov_b32 v32, 0x3f803f80\n" \
  "v_mov_b32 v33, 0x3f803f80\n" \
  "v_mov_b32 v34, 0x3f803f80\n" \
  "v_mov_b32 v35, 0x3f803f80\n" \
  "s_nop 1\n" \
  "v_mfma_f32_16x16x32_bf16 v[64:67], a[0:3], v[32:35], 0\n" \
  "v_mfma_f32_16x16x32_bf16 v[68:71], a[4:7], v[32:35], 0\n" \
  "v_mfma_f32_16x16x32_bf16 v[72:75], a[8:11], v[32:35], 0\n" \
  "v_mfma_f32_16x16x32_bf16 v[76:79], a[12:15], v[32:35], 0\n" \
  "v_mfma_f32_16x16x32_bf16 v[64:67], a[16:19], v[32:35], v[64:67]\n" \
  "v_mfma_f32_16x16x32_bf16 v[68:71], a[20:23], v[32:35], v[68:71]\n" \
  "v_mfma_f32_16x16x32_bf16 v[72:75], a[24:27], v[32:35], v[72:75]\n" \
  "v_mfma_f32_16x16x32_bf16 v[76:79], a[28:31], v[32:35], v[76:79]\n" \
  "v_mfma_f32_16x16x32_bf16 v[64:67], a[32:35], v[32:35], v[64:67]\n" \
  "v_mfma_f32_16x16x32_bf16 v[68:71], a[36:39], v[32:35], v[68:71]\n" \
  "v_mfma_f32_16x16x32_bf16 v[72:75], a[40:43], v[32:35], v[72:75]\n" \
  "v_mfma_f32_16x16x32_bf16 v[76:79], a[44:47], v[32:35], v[76:79]\n" \
  "v_mfma_f32_16x16x32_bf16 v[64:67], a[48:51], v[32:35], v[64:67]\n" \
  "v_mfma_f32_16x16x32_bf16 v[68:71], a[52:55], v[32:35], v[68:71]\n" \
  "v_mfma_f32_16x16x32_bf16 v[72:75], a[56:59], v[32:35], v[72:75]\n" \
  "v_mfma_f32_16x16x32_bf16 v[76:79], a[60:63], v[32:35], v[76:79]\n" \
  "v_mfma_f32_16x16x32_bf16 v[64:67], a[64:67], v[32:35], v[64:67]\n" \
  "v_mfma_f32_16x16x32_bf16 v[68:71], a[68:71], v[32:35], v[68:71]\n" \
  "v_mfma_f32_16x16x32_bf16 v[72:75], a[72:75], v[32:35], v[72:75]\n" \
  "v_mfma_f32_16x16x32_bf16 v[76:79], a[76:79], v[32:35], v[76:79]\n" \
  "v_mfma_f32_16x16x32_bf16 v[64:67], a[80:83], v[32:35], v[64:67]\n" \
  "v_mfma_f32_16x16x32_bf16 v[68:71], a[84:87], v[32:35], v[68:71]\n" \
  "v_mfma_f32_16x16x32_bf16 v[72:75], a[88:91], v[32:35], v[72:75]\n" \
  "v_mfma_f32_16x16x32_bf16 v[76:79], a[92:95], v[32:35], v[76:79]\n" \
  "v_mfma_f32_16x16x32_bf16 v[64:67], a[96:99], v[32:35], v[64:67]\n" \
  "v_mfma_f32_16x16x32_bf16 v[68:71], a[100:103], v[32:35], v[68:71]\n" \
  "v_mfma_f32_16x16x32_bf16 v[72:75], a[104:107], v[32:35], v[72:75]\n" \
  "v_mfma_f32_16x16x32_bf16 v[76:79], a[108:111], v[32:35], v[76:79]\n" \
  "v_mfma_f32_16x16x32_bf16 v[64:67], a[112:115], v[32:35], v[64:67]\n" \
  "v_mfma_f32_16x16x32_bf16 v[68:71], a[116:119], v[32:35], v[68:71]\n" \
  "v_mfma_f32_16x16x32_bf16 v[72:75], a[120:123], v[32:35], v[72:75]\n" \
  "v_mfma_f32_16x16x32_bf16 v[76:79], a[124:127], v[32:35], v[76:79]\n" \
  "v_mfma_f32_16x16x32_bf16 v[64:67], a[128:131], v[32:35], v[64:67]\n" \
  "v_mfma_f32_16x16x32_bf16 v[68:71], a[132:135], v[32:35], v[68:71]\n" \
  "v_mfma_f32_16x16x32_bf16 v[72:75], a[136:139], v[32:35], v[72:75]\n" \
  "v_mfma_f32_16x16x32_bf16 v[76:79], a[140:143], v[32:35], v[76:79]\n" \
  "v_mfma_f32_16x16x32_bf16 v[64:67], a[144:147], v[32:35], v[64:67]\n" \
  "v_mfma_f32_16x16x32_bf16 v[68:71], a[148:151], v[32:35], v[68:71]\n" \
  "v_mfma_f32_16x16x32_bf16 v[72:75], a[152:155], v[32:35], v[72:75]\n" \
  "v_mfma_f32_16x16x32_bf16 v[76:79], a[156:159], v[32:35], v[76:79]\n" \
  "v_mfma_f32_16x16x32_bf16 v[64:67], a[160:163], v[32:35], v[64:67]\n" \
  "v_mfma_f32_16x16x32_bf16 v[68:71], a[164:167], v[32:35], v[68:71]\n" \
  "v_mfma_f32_16x16x32_bf16 v[72:75], a[168:171], v[32:35], v[72:75]\n" \
  "v_mfma_f32_16x16x32_bf16 v[76:79], a[172:175], v[32:35], v[76:79]\n" \
  "v_mfma_f32_16x16x32_bf16 v[64:67], a[176:179], v[32:35], v[64:67]\n" \
  "v_mfma_f32_16x16x32_bf16 v[68:71], a[180:183], v[32:35], v[68:71]\n" \
  "v_mfma_f32_16x16x32_bf16 v[72:75], a[184:187], v[32:35], v[72:75]\n" \
  "v_mfma_f32_16x16x32_bf16 v[76:79], a[188:191], v[32:35], v[76:79]\n" \
  "v_mfma_f32_16x16x32_bf16 v[64:67], a[192:195], v[32:35], v[64:67]\n" \
  "v_mfma_f32_16x16x32_bf16 v[68:71], a[196:199], v[32:35], v[68:71]\n" \
  "v_mfma_f32_16x16x32_bf16 v[72:75], a[200:203], v[32:35], v[72:75]\n" \
  "v_mfma_f32_16x16x32_bf16 v[76:79], a[204:207], v[32:35], v[76:79]\n" \
  "v_mfma_f32_16x16x32_bf16 v[64:67], a[208:211], v[32:35], v[64:67]\n" \
  "v_mfma_f32_16x16x32_bf16 v[68:71], a[212:215], v[32:35], v[68:71]\n" \
  "v_mfma_f32_16x16x32_bf16 v[72:75], a[216:219], v[32:35], v[72:75]\n" \
  "v_mfma_f32_16x16x32_bf16 v[76:79], a[220:223], v[32:35], v[76:79]\n" \
  "v_mfma_f32_16x16x32_bf16 v[64:67], a[224:227], v[32:35], v[64:67]\n" \
  "v_mfma_f32_16x16x32_bf16 v[68:71], a[228:231], v[32:35], v[68:71]\n" \
  "v_mfma_f32_16x16x32_bf16 v[72:75], a[232:235], v[32:35], v[72:75]\n" \
  "v_mfma_f32_16x16x32_bf16 v[76:79], a[236:239], v[32:35], v[76:79]\n" \
  "v_mfma_f32_16x16x32_bf16 v[64:67], a[240:243], v[32:35], v[64:67]\n" \
  "v_mfma_f32_16x16x32_bf16 v[68:71], a[244:247], v[32:35], v[68:71]\n" \
  "v_mfma_f32_16x16x32_bf16 v[72:75], a[248:251], v[32:35], v[72:75]\n" \
  "v_mfma_f32_16x16x32_bf16 v[76:79], a[252:255], v[32:35], v[76:79]\n" \
  "v_and_b32 v111, 3, v254\n" \
  "v_lshlrev_b32 v111, 7, v111\n" \
  "s_mov_b32 s52, 0\n" \
  "s_add_u32 s30, s52, 1\n" \
  "s_cmp_lt_u32 s52, 12\n" \
  "s_cselect_b32 s47, 2, 0\n" \
  "s_add_u32 s30, s30, s47\n" \
  "s_add_u32 s31, s30, 1\n" \
  "s_cmp_lt_u32 s30, 12\n" \
  "s_cselect_b32 s47, 2, 0\n" \
  "s_add_u32 s31, s31, s47\n" \
  "s_mov_b32 s21, 1\n" \
  "s_lshl_b32 s28, s30, 15\n" \
  "s_lshl_b32 s29, s30, 10\n" \
  "s_and_b32 s47, s52, 7\n" \
  "s_lshl_b32 s47, s47, 9\n" \
  "v_or_b32 v110, s47, v111\n" \
  "s_cmp_eq_u32 s40, 0\n" \
  "s_cbranch_scc0 Lkvw1_%=\n" \
  "Lkvs2_%=:\n" \
  "global_load_dword v107, v110, s[16:17] sc1\n" \
  "s_waitcnt vmcnt(0)\n" \
  "v_cmp_gt_u32 vcc, s21, v107\n" \
  "s_nop 0\n" \
  "s_cbranch_vccz Lkvs2b_%=\n" \
  "s_sleep 2\n" \
  "s_sub_u32 s41, s41, 1\n" \
  "s_cmp_gt_i32 s41, 0\n" \
  "s_cbranch_scc1 Lkvs2_%=\n" \
  "Lkvs2b_%=:\n" \
  "Lkvw1_%=:\n" \
  "s_barrier\n" \
  "s_mov_b32 m0, s53\n" \
  "s_add_u32 s22, s8, 4096\n" \
  "s_addc_u32 s23, s9, 0\n" \
  "global_load_lds_dwordx4 v112, s[8:9] sc1\n" \
  "global_load_lds_dwordx4 v113, s[8:9] offset:1024 sc1\n" \
  "global_load_lds_dwordx4 v114, s[8:9] offset:2048 sc1\n" \
  "global_load_lds_dwordx4 v115, s[8:9] offset:3072 sc1\n" \
  "s_add_u32 m0, s53, 4096\n" \
  "s_nop 0\n" \
  "global_load_lds_dwordx4 v116, s[22:23] sc1\n" \
  "global_load_lds_dwordx4 v117, s[22:23] offset:1024 sc1\n" \
  "global_load_lds_dwordx4 v118, s[22:23] offset:2048 sc1\n" \
  "global_load_lds_dwordx4 v119, s[22:23] offset:3072 sc1\n" \
  "s_add_u32 s8, s8, s28\n" \
  "s_addc_u32 s9, s9, 0\n" \
  "global_load_dwordx2 v[88:89], v105, s[10:11] sc1\n" \
  "global_load_dwordx2 v[90:91], v105, s[10:11] offset:512 sc1\n" \
  "s_add_u32 s10, s10, s29\n" \
  "s_addc_u32 s11, s11, 0\n" \
  "s_add_u32 s53, s53, 32768\n" \
  "s_sub_u32 s28, s31, s30\n" \
  "s_lshl_b32 s28, s28, 15\n" \
  "s_and_b32 s47, s30, 7\n" \
  "s_lshl_b32 s47, s47, 9\n" \
  "v_or_b32 v110, s47, v111\n" \
  "Lkvs3_%=:\n" \
  "global_load_dword v107, v110, s[16:17] sc1\n" \
  "s_waitcnt vmcnt(0)\n" \
  "v_cmp_gt_u32 vcc, s21, v107\n" \
  "s_nop 0\n" \
  "s_cbranch_vccz Lkvs3b_%=\n" \
  "s_sleep 2\n" \
  "s_sub_u32 s41, s41, 1\n" \
  "s_cmp_gt_i32 s41, 0\n" \
  "s_cbranch_scc1 Lkvs3_%=\n" \
  "Lkvs3b_%=:\n" \
  "s_mov_b32 m0, s53\n" \
  "s_add_u32 s22, s8, 4096\n" \
  "s_addc_u32 s23, s9, 0\n" \
  "global_load_lds_dwordx4 v112, s[8:9] sc1\n" \
  "global_load_lds_dwordx4 v113, s[8:9] offset:1024 sc1\n" \
  "global_load_lds_dwordx4 v114, s[8:9] offset:2048 sc1\n" \
  "global_load_lds_dwordx4 v115, s[8:9] offset:3072 sc1\n" \
  "s_add_u32 m0, s53, 4096\n" \
  "s_nop 0\n" \
  "global_load_lds_dwordx4 v116, s[22:23] sc1\n" \
  "global_load_lds_dwordx4 v117, s[22:23] offset:1024 sc1\n" \
  "global_load_lds_dwordx4 v118, s[22:23] offset:2048 sc1\n" \
  "global_load_lds_dwordx4 v119, s[22:23] offset:3072 sc1\n" \
  "s_add_u32 s8, s8, s28\n" \
  "s_addc_u32 s9, s9, 0\n" \
  "s_add_u32 s53, s53, 32768\n" \
  "s_and_b32 s47, s31, 7\n" \
  "s_lshl_b32 s47, s47, 9\n" \
  "v_or_b32 v110, s47, v111\n" \
  "global_load_dword v98, v110, s[16:17] sc1\n" \
  "s_mov_b32 s56, 0\n" \
  "s_waitcnt vmcnt(0)\n" \
  "s_barrier\n" \
  "s_mov_b64 s[54:55], 0\n" \
  "s_mov_b64 s[58:59], s[12:13]\n" \
  "s_mov_b32 s24, 0\n" \
  "s_mov_b32 s20, 32768\n" \
  "s_nop 7\n" \
  "s_nop 1\n" \
  "Lkvtop_%=:\n" \
  "ds_read_b128 v[32:35], v100\n" \
  "ds_read_b128 v[36:39], v101\n" \
  "ds_read_b128 v[40:43], v102\n" \
  "ds_read_b128 v[44:47], v103\n" \
  "s_add_u32 s21, s31, 8\n" \
  "s_lshr_b32 s21, s21, 3\n" \
  "s_add_u32 s46, s31, 1\n" \
  "s_cmp_lt_u32 s31, 12\n" \
  "s_cselect_b32 s47, 2, 0\n" \
  "s_add_u32 s46, s46, s47\n" \
  "s_sub_u32 s28, s46, s31\n" \
  "s_lshl_b32 s28, s28, 15\n" \
  "s_sub_u32 s29, s31, s30\n" \
  "s_lshl_b32 s29, s29, 10\n" \
  "s_waitcnt vmcnt(5)\n" \
  "v_cmp_gt_u32 vcc, s21, v98\n" \
  "s_nop 0\n" \
  "s_cbranch_vccz Lkvxok_%=\n" \
  "s_cmp_ge_u32 s31, 64\n" \
  "s_cbranch_scc1 Lkvxok_%=\n" \
  "Lkvs4_%=:\n" \
  "global_load_dword v107, v110, s[16:17] sc1\n" \
  "s_waitcnt vmcnt(0)\n" \
  "v_cmp_gt_u32 vcc, s21, v107\n" \
  "s_nop 0\n" \
  "s_cbranch_vccz Lkvs4b_%=\n" \
  "s_sleep 2\n" \
  "s_sub_u32 s41, s41, 1\n" \
  "s_cmp_gt_i32 s41, 0\n" \
  "s_cbranch_scc1 Lkvs4_%=\n" \
  "Lkvs4b_%=:\n" \
  "Lkvxok_%=:\n" \
  "s_waitcnt lgkmcnt(3)\n" \
  "v_mfma_f32_16x16x32_bf16 v[0:3], a[0:3], v[32:35], 0\n" \
  "ds_read_b128 v[48:51], v100 offset:256\n" \
  "v_mfma_f32_16x16x32_bf16 v[4:7], a[4:7], v[32:35], 0\n" \
  "v_mfma_f32_16x16x32_bf16 v[8:11], a[8:11], v[32:35], 0\n" \
  "v_mul_f32 v16, v16, v96\n" \
  "v_fmac_f32 v16, v97, v64\n" \
  "v_mfma_f32_16x16x32_bf16 v[12:15], a[12:15], v[32:35], 0\n" \
  "v_mul_f32 v17, v17, v96\n" \
  "s_waitcnt lgkmcnt(3)\n" \
  "v_mfma_f32_16x16x32_bf16 v[0:3], a[16:19], v[36:39], v[0:3]\n" \
  "ds_read_b128 v[52:55], v101 offset:256\n" \
  "v_fmac_f32 v17, v97, v65\n" \
  "v_mfma_f32_16x16x32_bf16 v[4:7], a[20:23], v[36:39], v[4:7]\n" \
  "v_mul_f32 v18, v18, v96\n" \
  "v_mfma_f32_16x16x32_bf16 v[8:11], a[24:27], v[36:39], v[8:11]\n" \
  "v_fmac_f32 v18, v97, v66\n" \
  "s_mov_b32 m0, s53\n" \
  "s_add_u32 s22, s8, 4096\n" \
  "s_addc_u32 s23, s9, 0\n" \
  "global_load_lds_dwordx4 v112, s[8:9] sc1\n" \
  "v_mfma_f32_16x16x32_bf16 v[12:15], a[28:31], v[36:39], v[12:15]\n" \
  "v_mul_f32 v19, v19, v96\n" \
  "s_waitcnt lgkmcnt(3)\n" \
  "v_mfma_f32_16x16x32_bf16 v[0:3], a[32:35], v[40:43], v[0:3]\n" \
  "ds_read_b128 v[56:59], v102 offset:256\n" \
  "v_fmac_f32 v19, v97, v67\n" \
  "v_mfma_f32_16x16x32_bf16 v[4:7], a[36:39], v[40:43], v[4:7]\n" \
  "v_cvt_pk_bf16_f32 v80, v16, v17\n" \
  "v_mfma_f32_16x16x32_bf16 v[8:11], a[40:43], v[40:43], v[8:11]\n" \
  "v_cvt_pk_bf16_f32 v81, v18, v19\n" \
  "v_mul_f32 v20, v20, v96\n" \
  "v_mfma_f32_16x16x32_bf16 v[12:15], a[44:47], v[40:43], v[12:15]\n" \
  "v_fmac_f32 v20, v97, v68\n" \
  "s_waitcnt lgkmcnt(3)\n" \
  "v_mfma_f32_16x16x32_bf16 v[0:3], a[48:51], v[44:47], v[0:3]\n" \
  "ds_read_b128 v[60:63], v103 offset:256\n" \
  "v_mul_f32 v21, v21, v96\n" \
  "v_mfma_f32_16x16x32_bf16 v[4:7], a[52:55], v[44:47], v[4:7]\n" \
  "v_fmac_f32 v21, v97, v69\n" \
  "v_mfma_f32_16x16x32_bf16 v[8:11], a[56:59], v[44:47], v[8:11]\n" \
  "v_mul_f32 v22, v22, v96\n" \
  "v_fmac_f32 v22, v97, v70\n" \
  "v_mfma_f32_16x16x32_bf16 v[12:15], a[60:63], v[44:47], v[12:15]\n" \
  "v_mul_f32 v23, v23, v96\n" \
  "s_waitcnt lgkmcnt(3)\n" \
  "v_mfma_f32_16x16x32_bf16 v[0:3], a[64:67], v[48:51], v[0:3]\n" \
  "ds_read_b128 v[32:35], v100 offset:512\n" \
  "v_fmac_f32 v23, v97, v71\n" \
  "v_mfma_f32_16x16x32_bf16 v[4:7], a[68:71], v[48:51], v[4:7]\n" \
  "v_cvt_pk_bf16_f32 v82, v20, v21\n" \
  "v_mfma_f32_16x16x32_bf16 v[8:11], a[72:75], v[48:51], v[8:11]\n" \
  "v_cvt_pk_bf16_f32 v83, v22, v23\n" \
  "s_mov_b64 exec, s[54:55]\n" \
  "global_store_dwordx4 v106, v[80:83], s[58:59] offset:256 sc1\n" \
  "s_mov_b64 exec, -1\n" \
  "v_mfma_f32_16x16x32_bf16 v[12:15], a[76:79], v[48:51], v[12:15]\n" \
  "v_mul_f32 v24, v24, v96\n" \
  "s_waitcnt lgkmcnt(3)\n" \
  "v_mfma_f32_16x16x32_bf16 v[0:3], a[80:83], v[52:55], v[0:3]\n" \
  "ds_read_b128 v[36:39], v101 offset:512\n" \
  "global_load_lds_dwordx4 v113, s[8:9] offset:1024 sc1\n" \
  "v_mfma_f32_16x16x32_bf16 v[4:7], a[84:87], v[52:55], v[4:7]\n" \
  "v_fmac_f32 v24, v97, v72\n" \
  "v_mfma_f32_16x16x32_bf16 v[8:11], a[88:91], v[52:55], v[8:11]\n" \
  "v_mul_f32 v25, v25, v96\n" \
  "v_fmac_f32 v25, v97, v73\n" \
  "v_mfma_f32_16x16x32_bf16 v[12:15], a[92:95], v[52:55], v[12:15]\n" \
  "v_mul_f32 v26, v26, v96\n" \
  "s_waitcnt lgkmcnt(3)\n" \
  "v_mfma_f32_16x16x32_bf16 v[0:3], a[96:99], v[56:59], v[0:3]\n" \
  "ds_read_b128 v[40:43], v102 offset:512\n" \
  "v_fmac_f32 v26, v97, v74\n" \
  "v_mfma_f32_16x16x32_bf16 v[4:7], a[100:103], v[56:59], v[4:7]\n" \
  "v_mul_f32 v27, v27, v96\n" \
  "v_mfma_f32_16x16x32_bf16 v[8:11], a[104:107], v[56:59], v[8:11]\n" \
  "v_fmac_f32 v27, v97, v75\n" \
  "v_cvt_pk_bf16_f32 v84, v24, v25\n" \
  "v_mfma_f32_16x16x32_bf16 v[12:15], a[108:111], v[56:59], v[12:15]\n" \
  "v_cvt_pk_bf16_f32 v85, v26, v27\n" \
  "s_waitcnt lgkmcnt(3)\n" \
  "v_mfma_f32_16x16x32_bf16 v[0:3], a[112:115], v[60:63], v[0:3]\n" \
  "ds_read_b128 v[44:47], v103 offset:512\n" \
  "v_mul_f32 v28, v28, v96\n" \
  "v_mfma_f32_16x16x32_bf16 v[4:7], a[116:119], v[60:63], v[4:7]\n" \
  "v_fmac_f32 v28, v97, v76\n" \
  "v_mfma_f32_16x16x32_bf16 v[8:11], a[120:123], v[60:63], v[8:11]\n" \
  "v_mul_f32 v29, v29, v96\n" \
  "v_fmac_f32 v29, v97, v77\n" \
  "v_mfma_f32_16x16x32_bf16 v[12:15], a[124:127], v[60:63], v[12:15]\n" \
  "v_mul_f32 v30, v30, v96\n" \
  "s_waitcnt lgkmcnt(3)\n" \
  "v_mfma_f32_16x16x32_bf16 v[0:3], a[128:131], v[32:35], v[0:3]\n" \
  "ds_read_b128 v[48:51], v100 offset:768\n" \
  "v_fmac_f32 v30, v97, v78\n" \
  "v_mfma_f32_16x16x32_bf16 v[4:7], a[132:135], v[32:35], v[4:7]\n" \
  "v_mul_f32 v31, v31, v96\n" \
  "v_mfma_f32_16x16x32_bf16 v[8:11], a[136:139], v[32:35], v[8:11]\n" \
  "global_load_lds_dwordx4 v114, s[8:9] offset:2048 sc1\n" \
  "v_fmac_f32 v31, v97, v79\n" \
  "v_mfma_f32_16x16x32_bf16 v[12:15], a[140:143], v[32:35], v[12:15]\n" \
  "v_cvt_pk_bf16_f32 v86, v28, v29\n" \
  "s_waitcnt lgkmcnt(3)\n" \
  "v_mfma_f32_16x16x32_bf16 v[0:3], a[144:147], v[36:39], v[0:3]\n" \
  "ds_read_b128 v[52:55], v101 offset:768\n" \
  "v_cvt_pk_bf16_f32 v87, v30, v31\n" \
  "v_mfma_f32_16x16x32_bf16 v[4:7], a[148:151], v[36:39], v[4:7]\n" \
  "s_mov_b64 exec, s[54:55]\n" \
  "global_store_dwordx4 v106, v[84:87], s[58:59] offset:2304 sc1\n" \
  "s_mov_b64 exec, -1\n" \
  "v_mfma_f32_16x16x32_bf16 v[8:11], a[152:155], v[36:39], v[8:11]\n" \
  "s_waitcnt vmcnt(11)\n" \
  "v_mov_b32 v107, v88\n" \
  "v_mfma_f32_16x16x32_bf16 v[12:15], a[156:159], v[36:39], v[12:15]\n" \
  "v_permlane16_swap_b32 v88, v107\n" \
  "s_waitcnt lgkmcnt(3)\n" \
  "v_mfma_f32_16x16x32_bf16 v[0:3], a[160:163], v[40:43], v[0:3]\n" \
  "ds_read_b128 v[56:59], v102 offset:768\n" \
  "v_add_f32 v88, v88, v107\n" \
  "v_mfma_f32_16x16x32_bf16 v[4:7], a[164:167], v[40:43], v[4:7]\n" \
  "v_mov_b32 v107, v88\n" \
  "v_mfma_f32_16x16x32_bf16 v[8:11], a[168:171], v[40:43], v[8:11]\n" \
  "v_permlane32_swap_b32 v88, v107\n" \
  "v_add_f32 v88, v88, v107\n" \
  "v_mfma_f32_16x16x32_bf16 v[12:15], a[172:175], v[40:43], v[12:15]\n" \
  "v_mov_b32 v107, v89\n" \
  "s_waitcnt lgkmcnt(3)\n" \
  "v_mfma_f32_16x16x32_bf16 v[0:3], a[176:179], v[44:47], v[0:3]\n" \
  "ds_read_b128 v[60:63], v103 offset:768\n" \
  "v_permlane16_swap_b32 v89, v107\n" \
  "v_mfma_f32_16x16x32_bf16 v[4:7], a[180:183], v[44:47], v[4:7]\n" \
  "v_add_f32 v89, v89, v107\n" \
  "v_mfma_f32_16x16x32_bf16 v[8:11], a[184:187], v[44:47], v[8:11]\n" \
  "v_mov_b32 v107, v89\n" \
  "s_nop 1\n" \
  "v_permlane32_swap_b32 v89, v107\n" \
  "v_mfma_f32_16x16x32_bf16 v[12:15], a[188:191], v[44:47], v[12:15]\n" \
  "global_load_lds_dwordx4 v115, s[8:9] offset:3072 sc1\n" \
  "s_waitcnt lgkmcnt(3)\n" \
  "v_mfma_f32_16x16x32_bf16 v[0:3], a[192:195], v[48:51], v[0:3]\n" \
  "ds_read_b128 v[32:35], v100 offset:16384\n" \
  "v_add_f32 v89, v89, v107\n" \
  "v_mfma_f32_16x16x32_bf16 v[4:7], a[196:199], v[48:51], v[4:7]\n" \
  "v_mul_f32 v95, 0x3b000000, v88\n" \
  "v_mfma_f32_16x16x32_bf16 v[8:11], a[200:203], v[48:51], v[8:11]\n" \
  "v_mul_f32 v89, 0x3b000000, v89\n" \
  "v_fma_f32 v89, -v95, v95, v89\n" \
  "v_mfma_f32_16x16x32_bf16 v[12:15], a[204:207], v[48:51], v[12:15]\n" \
  "v_max_f32 v89, 0, v89\n" \
  "s_waitcnt lgkmcnt(3)\n" \
  "v_mfma_f32_16x16x32_bf16 v[0:3], a[208:211], v[52:55], v[0:3]\n" \
  "ds_read_b128 v[36:39], v101 offset:16384\n" \
  "v_add_f32 v89, 0x3727c5ac, v89\n" \
  "v_mfma_f32_16x16x32_bf16 v[4:7], a[212:215], v[52:55], v[4:7]\n" \
  "v_rsq_f32 v94, v89\n" \
  "v_mfma_f32_16x16x32_bf16 v[8:11], a[216:219], v[52:55], v[8:11]\n" \
  "v_mul_f32_e64 v95, -v95, v94\n" \
  "s_mov_b64 exec, s[26:27]\n" \
  "global_store_dwordx2 v99, v[94:95], s[18:19] sc1\n" \
  "s_mov_b64 exec, -1\n" \
  "v_mfma_f32_16x16x32_bf16 v[12:15], a[220:223], v[52:55], v[12:15]\n" \
  "s_waitcnt lgkmcnt(3)\n" \
  "v_mfma_f32_16x16x32_bf16 v[0:3], a[224:227], v[56:59], v[0:3]\n" \
  "ds_read_b128 v[40:43], v102 offset:16384\n" \
  "v_mfma_f32_16x16x32_bf16 v[4:7], a[228:231], v[56:59], v[4:7]\n" \
  "v_mfma_f32_16x16x32_bf16 v[8:11], a[232:235], v[56:59], v[8:11]\n" \
  "v_mfma_f32_16x16x32_bf16 v[12:15], a[236:239], v[56:59], v[12:15]\n" \
  "s_waitcnt lgkmcnt(3)\n" \
  "v_mfma_f32_16x16x32_bf16 v[0:3], a[240:243], v[60:63], v[0:3]\n" \
  "ds_read_b128 v[44:47], v103 offset:16384\n" \
  "v_mfma_f32_16x16x32_bf16 v[4:7], a[244:247], v[60:63], v[4:7]\n" \
  "v_mfma_f32_16x16x32_bf16 v[8:11], a[248:251], v[60:63], v[8:11]\n" \
  "v_mfma_f32_16x16x32_bf16 v[12:15], a[252:255], v[60:63], v[12:15]\n" \
  "s_waitcnt lgkmcnt(3)\n" \
  "v_mfma_f32_16x16x32_bf16 v[16:19], a[0:3], v[32:35], 0\n" \
  "ds_read_b128 v[48:51], v100 offset:16640\n" \
  "v_mfma_f32_16x16x32_bf16 v[20:23], a[4:7], v[32:35], 0\n" \
  "v_mfma_f32_16x16x32_bf16 v[24:27], a[8:11], v[32:35], 0\n" \
  "v_mov_b32 v107, v90\n" \
  "s_nop 1\n" \
  "v_permlane16_swap_b32 v90, v107\n" \
  "v_mfma_f32_16x16x32_bf16 v[28:31], a[12:15], v[32:35], 0\n" \
  "v_add_f32 v90, v90, v107\n" \
  "s_waitcnt lgkmcnt(3)\n" \
  "v_mfma_f32_16x16x32_bf16 v[16:19], a[16:19], v[36:39], v[16:19]\n" \
  "ds_read_b128 v[52:55], v101 offset:16640\n" \
  "v_mov_b32 v107, v90\n" \
  "v_mfma_f32_16x16x32_bf16 v[20:23], a[20:23], v[36:39], v[20:23]\n" \
  "s_add_u32 m0, s53, 4096\n" \
  "s_nop 0\n" \
  "global_load_lds_dwordx4 v116, s[22:23] sc1\n" \
  "v_mfma_f32_16x16x32_bf16 v[24:27], a[24:27], v[36:39], v[24:27]\n" \
  "v_permlane32_swap_b32 v90, v107\n" \
  "v_add_f32 v90, v90, v107\n" \
  "v_mfma_f32_16x16x32_bf16 v[28:31], a[28:31], v[36:39], v[28:31]\n" \
  "v_mov_b32 v107, v91\n" \
  "s_waitcnt lgkmcnt(3)\n" \
  "v_mfma_f32_16x16x32_bf16 v[16:19], a[32:35], v[40:43], v[16:19]\n" \
  "ds_read_b128 v[56:59], v102 offset:16640\n" \
  "v_permlane16_swap_b32 v91, v107\n" \
  "v_mfma_f32_16x16x32_bf16 v[20:23], a[36:39], v[40:43], v[20:23]\n" \
  "v_add_f32 v91, v91, v107\n" \
  "v_mfma_f32_16x16x32_bf16 v[24:27], a[40:43], v[40:43], v[24:27]\n" \
  "v_mov_b32 v107, v91\n" \
  "s_nop 1\n" \
  "v_permlane32_swap_b32 v91, v107\n" \
  "v_mfma_f32_16x16x32_bf16 v[28:31], a[44:47], v[40:43], v[28:31]\n" \
  "v_add_f32 v91, v91, v107\n" \
  "s_waitcnt lgkmcnt(3)\n" \
  "v_mfma_f32_16x16x32_bf16 v[16:19], a[48:51], v[44:47], v[16:19]\n" \
  "ds_read_b128 v[60:63], v103 offset:16640\n" \
  "v_mul_f32 v97, 0x3b000000, v90\n" \
  "v_mfma_f32_16x16x32_bf16 v[20:23], a[52:55], v[44:47], v[20:23]\n" \
  "v_mul_f32 v91, 0x3b000000, v91\n" \
  "v_mfma_f32_16x16x32_bf16 v[24:27], a[56:59], v[44:47], v[24:27]\n" \
  "v_fma_f32 v91, -v97, v97, v91\n" \
  "v_max_f32 v91, 0, v91\n" \
  "v_mfma_f32_16x16x32_bf16 v[28:31], a[60:63], v[44:47], v[28:31]\n" \
  "v_add_f32 v91, 0x3727c5ac, v91\n" \
  "s_waitcnt lgkmcnt(3)\n" \
  "v_mfma_f32_16x16x32_bf16 v[16:19], a[64:67], v[48:51], v[16:19]\n" \
  "ds_read_b128 v[32:35], v100 offset:16896\n" \
  "v_rsq_f32 v96, v91\n" \
  "v_mfma_f32_16x16x32_bf16 v[20:23], a[68:71], v[48:51], v[20:23]\n" \
  "v_mul_f32_e64 v97, -v97, v96\n" \
  "v_mfma_f32_16x16x32_bf16 v[24:27], a[72:75], v[48:51], v[24:27]\n" \
  "s_mov_b64 exec, s[26:27]\n" \
  "global_store_dwordx2 v99, v[96:97], s[18:19] offset:128 sc1\n" \
  "s_mov_b64 exec, -1\n" \
  "s_sub_u32 s47, s30, s52\n" \
  "s_lshl_b32 s47, s47, 8\n" \
  "s_add_u32 s18, s18, s47\n" \
  "s_addc_u32 s19, s19, 0\n" \
  "global_load_lds_dwordx4 v117, s[22:23] offset:1024 sc1\n" \
  "v_mfma_f32_16x16x32_bf16 v[28:31], a[76:79], v[48:51], v[28:31]\n" \
  "global_load_dwordx2 v[88:89], v105, s[10:11] sc1\n" \
  "global_load_dwordx2 v[90:91], v105, s[10:11] offset:512 sc1\n" \
  "s_add_u32 s10, s10, s29\n" \
  "s_addc_u32 s11, s11, 0\n" \
  "s_waitcnt lgkmcnt(3)\n" \
  "v_mfma_f32_16x16x32_bf16 v[16:19], a[80:83], v[52:55], v[16:19]\n" \
  "ds_read_b128 v[36:39], v101 offset:16896\n" \
  "s_and_b32 s47, s46, 7\n" \
  "s_lshl_b32 s47, s47, 9\n" \
  "v_or_b32 v110, s47, v111\n" \
  "v_mfma_f32_16x16x32_bf16 v[20:23], a[84:87], v[52:55], v[20:23]\n" \
  "v_mfma_f32_16x16x32_bf16 v[24:27], a[88:91], v[52:55], v[24:27]\n" \
  "global_load_dword v98, v110, s[16:17] sc1\n" \
  "v_mul_f32 v0, v0, v94\n" \
  "v_mfma_f32_16x16x32_bf16 v[28:31], a[92:95], v[52:55], v[28:31]\n" \
  "v_fmac_f32 v0, v95, v64\n" \
  "s_waitcnt lgkmcnt(3)\n" \
  "v_mfma_f32_16x16x32_bf16 v[16:19], a[96:99], v[56:59], v[16:19]\n" \
  "ds_read_b128 v[40:43], v102 offset:16896\n" \
  "v_mul_f32 v1, v1, v94\n" \
  "v_mfma_f32_16x16x32_bf16 v[20:23], a[100:103], v[56:59], v[20:23]\n" \
  "v_fmac_f32 v1, v95, v65\n" \
  "v_mfma_f32_16x16x32_bf16 v[24:27], a[104:107], v[56:59], v[24:27]\n" \
  "v_mul_f32 v2, v2, v94\n" \
  "v_fmac_f32 v2, v95, v66\n" \
  "v_mfma_f32_16x16x32_bf16 v[28:31], a[108:111], v[56:59], v[28:31]\n" \
  "v_mul_f32 v3, v3, v94\n" \
  "s_waitcnt lgkmcnt(3)\n" \
  "v_mfma_f32_16x16x32_bf16 v[16:19], a[112:115], v[60:63], v[16:19]\n" \
  "ds_read_b128 v[44:47], v103 offset:16896\n" \
  "v_fmac_f32 v3, v95, v67\n" \
  "v_mfma_f32_16x16x32_bf16 v[20:23], a[116:119], v[60:63], v[20:23]\n" \
  "v_cvt_pk_bf16_f32 v80, v0, v1\n" \
  "v_mfma_f32_16x16x32_bf16 v[24:27], a[120:123], v[60:63], v[24:27]\n" \
  "v_cvt_pk_bf16_f32 v81, v2, v3\n" \
  "v_mul_f32 v4, v4, v94\n" \
  "v_mfma_f32_16x16x32_bf16 v[28:31], a[124:127], v[60:63], v[28:31]\n" \
  "v_fmac_f32 v4, v95, v68\n" \
  "s_waitcnt lgkmcnt(3)\n" \
  "v_mfma_f32_16x16x32_bf16 v[16:19], a[128:131], v[32:35], v[16:19]\n" \
  "ds_read_b128 v[48:51], v100 offset:17152\n" \
  "global_load_lds_dwordx4 v118, s[22:23] offset:2048 sc1\n" \
  "v_mfma_f32_16x16x32_bf16 v[20:23], a[132:135], v[32:35], v[20:23]\n" \
  "v_mul_f32 v5, v5, v94\n" \
  "v_mfma_f32_16x16x32_bf16 v[24:27], a[136:139], v[32:35], v[24:27]\n" \
  "v_fmac_f32 v5, v95, v69\n" \
  "v_mul_f32 v6, v6, v94\n" \
  "v_mfma_f32_16x16x32_bf16 v[28:31], a[140:143], v[32:35], v[28:31]\n" \
  "v_fmac_f32 v6, v95, v70\n" \
  "s_waitcnt lgkmcnt(3)\n" \
  "v_mfma_f32_16x16x32_bf16 v[16:19], a[144:147], v[36:39], v[16:19]\n" \
  "ds_read_b128 v[52:55], v101 offset:17152\n" \
  "v_mul_f32 v7, v7, v94\n" \
  "v_mfma_f32_16x16x32_bf16 v[20:23], a[148:151], v[36:39], v[20:23]\n" \
  "v_fmac_f32 v7, v95, v71\n" \
  "v_mfma_f32_16x16x32_bf16 v[24:27], a[152:155], v[36:39], v[24:27]\n" \
  "v_cvt_pk_bf16_f32 v82, v4, v5\n" \
  "v_cvt_pk_bf16_f32 v83, v6, v7\n" \
  "v_mfma_f32_16x16x32_bf16 v[28:31], a[156:159], v[36:39], v[28:31]\n" \
  "global_store_dwordx4 v106, v[80:83], s[12:13] sc1\n" \
  "s_waitcnt lgkmcnt(3)\n" \
  "v_mfma_f32_16x16x32_bf16 v[16:19], a[160:163], v[40:43], v[16:19]\n" \
  "ds_read_b128 v[56:59], v102 offset:17152\n" \
  "v_mul_f32 v8, v8, v94\n" \
  "v_mfma_f32_16x16x32_bf16 v[20:23], a[164:167], v[40:43], v[20:23]\n" \
  "v_fmac_f32 v8, v95, v72\n" \
  "v_mfma_f32_16x16x32_bf16 v[24:27], a[168:171], v[40:43], v[24:27]\n" \
  "v_mul_f32 v9, v9, v94\n" \
  "v_fmac_f32 v9, v95, v73\n" \
  "v_mfma_f32_16x16x32_bf16 v[28:31], a[172:175], v[40:43], v[28:31]\n" \
  "v_mul_f32 v10, v10, v94\n" \
  "s_waitcnt lgkmcnt(3)\n" \
  "v_mfma_f32_16x16x32_bf16 v[16:19], a[176:179], v[44:47], v[16:19]\n" \
  "ds_read_b128 v[60:63], v103 offset:17152\n" \
  "v_fmac_f32 v10, v95, v74\n" \
  "v_mfma_f32_16x16x32_bf16 v[20:23], a[180:183], v[44:47], v[20:23]\n" \
  "v_mul_f32 v11, v11, v94\n" \
  "v_mfma_f32_16x16x32_bf16 v[24:27], a[184:187], v[44:47], v[24:27]\n" \
  "global_load_lds_dwordx4 v119, s[22:23] offset:3072 sc1\n" \
  "s_add_u32 s8, s8, s28\n" \
  "s_addc_u32 s9, s9, 0\n" \
  "v_fmac_f32 v11, v95, v75\n" \
  "v_mfma_f32_16x16x32_bf16 v[28:31], a[188:191], v[44:47], v[28:31]\n" \
  "v_cvt_pk_bf16_f32 v84, v8, v9\n" \
  "s_waitcnt lgkmcnt(3)\n" \
  "v_mfma_f32_16x16x32_bf16 v[16:19], a[192:195], v[48:51], v[16:19]\n" \
  "v_cvt_pk_bf16_f32 v85, v10, v11\n" \
  "v_mfma_f32_16x16x32_bf16 v[20:23], a[196:199], v[48:51], v[20:23]\n" \
  "v_mul_f32 v12, v12, v94\n" \
  "v_mfma_f32_16x16x32_bf16 v[24:27], a[200:203], v[48:51], v[24:27]\n" \
  "v_fmac_f32 v12, v95, v76\n" \
  "v_mul_f32 v13, v13, v94\n" \
  "v_mfma_f32_16x16x32_bf16 v[28:31], a[204:207], v[48:51], v[28:31]\n" \
  "v_fmac_f32 v13, v95, v77\n" \
  "s_waitcnt lgkmcnt(2)\n" \
  "v_mfma_f32_16x16x32_bf16 v[16:19], a[208:211], v[52:55], v[16:19]\n" \
  "v_mul_f32 v14, v14, v94\n" \
  "v_mfma_f32_16x16x32_bf16 v[20:23], a[212:215], v[52:55], v[20:23]\n" \
  "v_fmac_f32 v14, v95, v78\n" \
  "v_mfma_f32_16x16x32_bf16 v[24:27], a[216:219], v[52:55], v[24:27]\n" \
  "v_mul_f32 v15, v15, v94\n" \
  "v_fmac_f32 v15, v95, v79\n" \
  "v_mfma_f32_16x16x32_bf16 v[28:31], a[220:223], v[52:55], v[28:31]\n" \
  "v_cvt_pk_bf16_f32 v86, v12, v13\n" \
  "s_waitcnt lgkmcnt(1)\n" \
  "v_mfma_f32_16x16x32_bf16 v[16:19], a[224:227], v[56:59], v[16:19]\n" \
  "v_cvt_pk_bf16_f32 v87, v14, v15\n" \
  "v_mfma_f32_16x16x32_bf16 v[20:23], a[228:231], v[56:59], v[20:23]\n" \
  "global_store_dwordx4 v106, v[84:87], s[12:13] offset:2048 sc1\n" \
  "v_mfma_f32_16x16x32_bf16 v[24:27], a[232:235], v[56:59], v[24:27]\n" \
  "v_mfma_f32_16x16x32_bf16 v[28:31], a[236:239], v[56:59], v[28:31]\n" \
  "s_waitcnt lgkmcnt(0)\n" \
  "v_mfma_f32_16x16x32_bf16 v[16:19], a[240:243], v[60:63], v[16:19]\n" \
  "v_mfma_f32_16x16x32_bf16 v[20:23], a[244:247], v[60:63], v[20:23]\n" \
  "v_mfma_f32_16x16x32_bf16 v[24:27], a[248:251], v[60:63], v[24:27]\n" \
  "v_mfma_f32_16x16x32_bf16 v[28:31], a[252:255], v[60:63], v[28:31]\n" \
  "s_add_u32 s24, s24, 32768\n" \
  "s_cmp_eq_u32 s24, 98304\n" \
  "s_cselect_b32 s25, 0xffff0000, s20\n" \
  "s_cselect_b32 s24, 0, s24\n" \
  "v_add_u32 v100, s25, v100\n" \
  "v_add_u32 v101, s25, v101\n" \
  "v_add_u32 v102, s25, v102\n" \
  "v_add_u32 v103, s25, v103\n" \
  "s_cmp_eq_u32 s24, 32768\n" \
  "s_cselect_b32 s25, 0xffff0000, s20\n" \
  "s_add_u32 s53, s53, s25\n" \
  "s_waitcnt vmcnt(12)\n" \
  "s_mov_b64 s[58:59], s[12:13]\n" \
  "s_sub_u32 s47, s30, s52\n" \
  "s_lshl_b32 s47, s47, 14\n" \
  "s_add_u32 s12, s12, s47\n" \
  "s_addc_u32 s13, s13, 0\n" \
  "s_mov_b64 s[54:55], -1\n" \
  "s_barrier\n" \
  "v_mov_b32 v109, s56\n" \
  "s_mov_b64 exec, s[60:61]\n" \
  "global_store_dword v255, v109, s[14:15] sc1\n" \
  "s_mov_b64 exec, -1\n" \
  "s_cmp_eq_u32 s52, 0\n" \
  "s_cbranch_scc0 Lkvn0_%=\n" \
  "s_nop 7\n" \
  "s_nop 1\n" \
  "v_mul_f32 v16, v16, v96\n" \
  "v_fmac_f32 v16, v97, v64\n" \
  "v_mul_f32 v17, v17, v96\n" \
  "v_fmac_f32 v17, v97, v65\n" \
  "v_mul_f32 v18, v18, v96\n" \
  "v_fmac_f32 v18, v97, v66\n" \
  "v_mul_f32 v19, v19, v96\n" \
  "v_fmac_f32 v19, v97, v67\n" \
  "v_cvt_pk_bf16_f32 v80, v16, v17\n" \
  "v_cvt_pk_bf16_f32 v81, v18, v19\n" \
  "v_mul_f32 v20, v20, v96\n" \
  "v_fmac_f32 v20, v97, v68\n" \
  "v_mul_f32 v21, v21, v96\n" \
  "v_fmac_f32 v21, v97, v69\n" \
  "v_mul_f32 v22, v22, v96\n" \
  "v_fmac_f32 v22, v97, v70\n" \
  "v_mul_f32 v23, v23, v96\n" \
  "v_fmac_f32 v23, v97, v71\n" \
  "v_cvt_pk_bf16_f32 v82, v20, v21\n" \
  "v_cvt_pk_bf16_f32 v83, v22, v23\n" \
  "global_store_dwordx4 v106, v[80:83], s[58:59] offset:256 sc1\n" \
  "v_mul_f32 v24, v24, v96\n" \
  "v_fmac_f32 v24, v97, v72\n" \
  "v_mul_f32 v25, v25, v96\n" \
  "v_fmac_f32 v25, v97, v73\n" \
  "v_mul_f32 v26, v26, v96\n" \
  "v_fmac_f32 v26, v97, v74\n" \
  "v_mul_f32 v27, v27, v96\n" \
  "v_fmac_f32 v27, v97, v75\n" \
  "v_cvt_pk_bf16_f32 v84, v24, v25\n" \
  "v_cvt_pk_bf16_f32 v85, v26, v27\n" \
  "v_mul_f32 v28, v28, v96\n" \
  "v_fmac_f32 v28, v97, v76\n" \
  "v_mul_f32 v29, v29, v96\n" \
  "v_fmac_f32 v29, v97, v77\n" \
  "v_mul_f32 v30, v30, v96\n" \
  "v_fmac_f32 v30, v97, v78\n" \
  "v_mul_f32 v31, v31, v96\n" \
  "v_fmac_f32 v31, v97, v79\n" \
  "v_cvt_pk_bf16_f32 v86, v28, v29\n" \
  "v_cvt_pk_bf16_f32 v87, v30, v31\n" \
  "global_store_dwordx4 v106, v[84:87], s[58:59] offset:2304 sc1\n" \
  "s_waitcnt vmcnt(0)\n" \
  "s_barrier\n" \
  "v_mov_b32 v109, 1\n" \
  "s_mov_b64 exec, s[60:61]\n" \
  "global_store_dword v255, v109, s[14:15] sc1\n" \
  "s_mov_b64 exec, -1\n" \
  "s_mov_b64 s[54:55], 0\n" \
  "Lkvn0_%=:\n" \
  "s_add_u32 s56, s52, 1\n" \
  "s_mov_b32 s52, s30\n" \
  "s_mov_b32 s30, s31\n" \
  "s_mov_b32 s31, s46\n" \
  "s_cmp_lt_u32 s52, 64\n" \
  "s_cbranch_scc1 Lkvtop_%=\n" \
  "s_nop 7\n" \
  "s_nop 1\n" \
  "v_mul_f32 v16, v16, v96\n" \
  "v_fmac_f32 v16, v97, v64\n" \
  "v_mul_f32 v17, v17, v96\n" \
  "v_fmac_f32 v17, v97, v65\n" \
  "v_mul_f32 v18, v18, v96\n" \
  "v_fmac_f32 v18, v97, v66\n" \
  "v_mul_f32 v19, v19, v96\n" \
  "v_fmac_f32 v19, v97, v67\n" \
  "v_cvt_pk_bf16_f32 v80, v16, v17\n" \
  "v_cvt_pk_bf16_f32 v81, v18, v19\n" \
  "v_mul_f32 v20, v20, v96\n" \
  "v_fmac_f32 v20, v97, v68\n" \
  "v_mul_f32 v21, v21, v96\n" \
  "v_fmac_f32 v21, v97, v69\n" \
  "v_mul_f32 v22, v22, v96\n" \
  "v_fmac_f32 v22, v97, v70\n" \
  "v_mul_f32 v23, v23, v96\n" \
  "v_fmac_f32 v23, v97, v71\n" \
  "v_cvt_pk_bf16_f32 v82, v20, v21\n" \
  "v_cvt_pk_bf16_f32 v83, v22, v23\n" \
  "global_store_dwordx4 v106, v[80:83], s[58:59] offset:256 sc1\n" \
  "v_mul_f32 v24, v24, v96\n" \
  "v_fmac_f32 v24, v97, v72\n" \
  "v_mul_f32 v25, v25, v96\n" \
  "v_fmac_f32 v25, v97, v73\n" \
  "v_mul_f32 v26, v26, v96\n" \
  "v_fmac_f32 v26, v97, v74\n" \
  "v_mul_f32 v27, v27, v96\n" \
  "v_fmac_f32 v27, v97, v75\n" \
  "v_cvt_pk_bf16_f32 v84, v24, v25\n" \
  "v_cvt_pk_bf16_f32 v85, v26, v27\n" \
  "v_mul_f32 v28, v28, v96\n" \
  "v_fmac_f32 v28, v97, v76\n" \
  "v_mul_f32 v29, v29, v96\n" \
  "v_fmac_f32 v29, v97, v77\n" \
  "v_mul_f32 v30, v30, v96\n" \
  "v_fmac_f32 v30, v97, v78\n" \
  "v_mul_f32 v31, v31, v96\n" \
  "v_fmac_f32 v31, v97, v79\n" \
  "v_cvt_pk_bf16_f32 v86, v28, v29\n" \
  "v_cvt_pk_bf16_f32 v87, v30, v31\n" \
  "global_store_dwordx4 v106, v[84:87], s[58:59] offset:2304 sc1\n" \
  "s_waitcnt vmcnt(0)\n" \
  "s_barrier\n" \
  "v_mov_b32 v109, 64\n" \
  "s_mov_b64 exec, s[60:61]\n" \
  "global_store_dword v255, v109, s[14:15] sc1\n" \
  "s_mov_b64 exec, -1\n" \
  "s_waitcnt vmcnt(0)\n" \
  "s_cmp_eq_u32 s69, 1\n" \
  "s_cbranch_scc1 Lkvend_%=\n" \
  "v_lshlrev_b32 v249, 4, v254\n" \
  "v_lshlrev_b32 v250, 3, v254\n" \
  "v_lshrrev_b32 v192, 4, v254\n" \
  "v_bfe_u32 v193, v254, 1, 3\n" \
  "v_lshl_add_u32 v192, v192, 7, v193\n" \
  "v_mul_u32_u24 v192, 3072, v192\n" \
  "v_and_b32 v193, 1, v254\n" \
  "v_lshl_add_u32 v192, v193, 4, v192\n" \
  "s_mul_i32 s42, s40, 98304\n" \
  "s_add_u32 s88, s88, s42\n" \
  "s_addc_u32 s89, s89, 0\n" \
  "s_lshl_b32 s43, s40, 12\n" \
  "s_add_u32 m0, s43, 0\n" \
  "s_nop 0\n" \
  "global_load_lds_dwordx4 v192, s[88:89]\n" \
  "s_add_u32 m0, s43, 15360\n" \
  "s_nop 0\n" \
  "global_load_lds_dwordx4 v192, s[88:89] offset:1024\n" \
  "s_add_u32 m0, s43, 30720\n" \
  "s_nop 0\n" \
  "global_load_lds_dwordx4 v192, s[88:89] offset:2048\n" \
  "s_add_u32 s88, s88, 24576\n" \
  "s_addc_u32 s89, s89, 0\n" \
  "s_add_u32 m0, s43, 1024\n" \
  "s_nop 0\n" \
  "global_load_lds_dwordx4 v192, s[88:89]\n" \
  "s_add_u32 m0, s43, 16384\n" \
  "s_nop 0\n" \
  "global_load_lds_dwordx4 v192, s[88:89] offset:1024\n" \
  "s_add_u32 m0, s43, 31744\n" \
  "s_nop 0\n" \
  "global_load_lds_dwordx4 v192, s[88:89] offset:2048\n" \
  "s_add_u32 s88, s88, 24576\n" \
  "s_addc_u32 s89, s89, 0\n" \
  "s_add_u32 m0, s43, 2048\n" \
  "s_nop 0\n" \
  "global_load_lds_dwordx4 v192, s[88:89]\n" \
  "s_add_u32 m0, s43, 17408\n" \
  "s_nop 0\n" \
  "global_load_lds_dwordx4 v192, s[88:89] offset:1024\n" \
  "s_add_u32 m0, s43, 32768\n" \
  "s_nop 0\n" \
  "global_load_lds_dwordx4 v192, s[88:89] offset:2048\n" \
  "s_add_u32 s88, s88, 24576\n" \
  "s_addc_u32 s89, s89, 0\n" \
  "s_add_u32 m0, s43, 3072\n" \
  "s_nop 0\n" \
  "global_load_lds_dwordx4 v192, s[88:89]\n" \
  "s_add_u32 m0, s43, 18432\n" \
  "s_nop 0\n" \
  "global_load_lds_dwordx4 v192, s[88:89] offset:1024\n" \
  "s_add_u32 m0, s43, 33792\n" \
  "s_nop 0\n" \
  "global_load_lds_dwordx4 v192, s[88:89] offset:2048\n" \
  "s_add_u32 s88, s88, 24576\n" \
  "s_addc_u32 s89, s89, 0\n" \
  "s_lshl_b32 s42, s40, 12\n" \
  "s_add_u32 s90, s90, s42\n" \
  "s_addc_u32 s91, s91, 0\n" \
  "s_mul_i32 s42, s40, 4160\n" \
  "s_add_u32 m0, s42, 133120\n" \
  "s_nop 0\n" \
  "global_load_lds_dwordx4 v249, s[90:91]\n" \
  "s_add_u32 m0, s42, 133136\n" \
  "s_nop 0\n" \
  "global_load_lds_dwordx4 v249, s[90:91] offset:1024\n" \
  "s_add_u32 m0, s42, 133152\n" \
  "s_nop 0\n" \
  "global_load_lds_dwordx4 v249, s[90:91] offset:2048\n" \
  "s_add_u32 m0, s42, 133168\n" \
  "s_nop 0\n" \
  "global_load_lds_dwordx4 v249, s[90:91] offset:3072\n" \
  "v_lshrrev_b32 v193, 4, v254\n" \
  "v_bfe_u32 v194, v254, 2, 2\n" \
  "v_lshl_add_u32 v193, v193, 3, v194\n" \
  "v_lshlrev_b32 v193, 5, v193\n" \
  "v_and_b32 v194, 3, v254\n" \
  "v_lshl_add_u32 v193, v194, 3, v193\n" \
  "s_waitcnt vmcnt(0)\n" \
  "s_barrier\n" \
  "ds_read_b64_tr_b16 v[0:1], v193\n" \
  "ds_read_b64_tr_b16 v[2:3], v193 offset:128\n" \
  "ds_read_b64_tr_b16 v[4:5], v193 offset:1024\n" \
  "ds_read_b64_tr_b16 v[6:7], v193 offset:1152\n" \
  "ds_read_b64_tr_b16 v[8:9], v193 offset:2048\n" \
  "ds_read_b64_tr_b16 v[10:11], v193 offset:2176\n" \
  "ds_read_b64_tr_b16 v[12:13], v193 offset:3072\n" \
  "ds_read_b64_tr_b16 v[14:15], v193 offset:3200\n" \
  "ds_read_b64_tr_b16 v[16:17], v193 offset:4096\n" \
  "ds_read_b64_tr_b16 v[18:19], v193 offset:4224\n" \
  "ds_read_b64_tr_b16 v[20:21], v193 offset:5120\n" \
  "ds_read_b64_tr_b16 v[22:23], v193 offset:5248\n" \
  "s_waitcnt lgkmcnt(4)\n" \
  "ds_read_b64_tr_b16 v[24:25], v193 offset:6144\n" \
  "ds_read_b64_tr_b16 v[26:27], v193 offset:6272\n" \
  "ds_read_b64_tr_b16 v[28:29], v193 offset:7168\n" \
  "ds_read_b64_tr_b16 v[30:31], v193 offset:7296\n" \
  "ds_read_b64_tr_b16 v[32:33], v193 offset:8192\n" \
  "ds_read_b64_tr_b16 v[34:35], v193 offset:8320\n" \
  "ds_read_b64_tr_b16 v[36:37], v193 offset:9216\n" \
  "ds_read_b64_tr_b16 v[38:39], v193 offset:9344\n" \
  "s_waitcnt lgkmcnt(4)\n" \
  "ds_read_b64_tr_b16 v[40:41], v193 offset:10240\n" \
  "ds_read_b64_tr_b16 v[42:43], v193 offset:10368\n" \
  "ds_read_b64_tr_b16 v[44:45], v193 offset:11264\n" \
  "ds_read_b64_tr_b16 v[46:47], v193 offset:11392\n" \
  "ds_read_b64_tr_b16 v[48:49], v193 offset:12288\n" \
  "ds_read_b64_tr_b16 v[50:51], v193 offset:12416\n" \
  "ds_read_b64_tr_b16 v[52:53], v193 offset:13312\n" \
  "ds_read_b64_tr_b16 v[54:55], v193 offset:13440\n" \
  "s_waitcnt lgkmcnt(4)\n" \
  "ds_read_b64_tr_b16 v[56:57], v193 offset:14336\n" \
  "ds_read_b64_tr_b16 v[58:59], v193 offset:14464\n" \
  "ds_read_b64_tr_b16 v[60:61], v193 offset:15360\n" \
  "ds_read_b64_tr_b16 v[62:63], v193 offset:15488\n" \
  "ds_read_b64_tr_b16 v[64:65], v193 offset:16384\n" \
  "ds_read_b64_tr_b16 v[66:67], v193 offset:16512\n" \
  "ds_read_b64_tr_b16 v[68:69], v193 offset:17408\n" \
  "ds_read_b64_tr_b16 v[70:71], v193 offset:17536\n" \
  "s_waitcnt lgkmcnt(4)\n" \
  "ds_read_b64_tr_b16 v[72:73], v193 offset:18432\n" \
  "ds_read_b64_tr_b16 v[74:75], v193 offset:18560\n" \
  "ds_read_b64_tr_b16 v[76:77], v193 offset:19456\n" \
  "ds_read_b64_tr_b16 v[78:79], v193 offset:19584\n" \
  "ds_read_b64_tr_b16 v[80:81], v193 offset:20480\n" \
  "ds_read_b64_tr_b16 v[82:83], v193 offset:20608\n" \
  "ds_read_b64_tr_b16 v[84:85], v193 offset:21504\n" \
  "ds_read_b64_tr_b16 v[86:87], v193 offset:21632\n" \
  "s_waitcnt lgkmcnt(4)\n" \
  "ds_read_b64_tr_b16 v[88:89], v193 offset:22528\n" \
  "ds_read_b64_tr_b16 v[90:91], v193 offset:22656\n" \
  "ds_read_b64_tr_b16 v[92:93], v193 offset:23552\n" \
  "ds_read_b64_tr_b16 v[94:95], v193 offset:23680\n" \
  "ds_read_b64_tr_b16 v[96:97], v193 offset:24576\n" \
  "ds_read_b64_tr_b16 v[98:99], v193 offset:24704\n" \
  "ds_read_b64_tr_b16 v[100:101], v193 offset:25600\n" \
  "ds_read_b64_tr_b16 v[102:103], v193 offset:25728\n" \
  "s_waitcnt lgkmcnt(4)\n" \
  "ds_read_b64_tr_b16 v[104:105], v193 offset:26624\n" \
  "ds_read_b64_tr_b16 v[106:107], v193 offset:26752\n" \
  "ds_read_b64_tr_b16 v[108:109], v193 offset:27648\n" \
  "ds_read_b64_tr_b16 v[110:111], v193 offset:27776\n" \
  "ds_read_b64_tr_b16 v[112:113], v193 offset:28672\n" \
  "ds_read_b64_tr_b16 v[114:115], v193 offset:28800\n" \
  "ds_read_b64_tr_b16 v[116:117], v193 offset:29696\n" \
  "ds_read_b64_tr_b16 v[118:119], v193 offset:29824\n" \
  "s_waitcnt lgkmcnt(4)\n" \
  "ds_read_b64_tr_b16 v[120:121], v193 offset:30720\n" \
  "ds_read_b64_tr_b16 v[122:123], v193 offset:30848\n" \
  "ds_read_b64_tr_b16 v[124:125], v193 offset:31744\n" \
  "ds_read_b64_tr_b16 v[126:127], v193 offset:31872\n" \
  "ds_read_b64_tr_b16 v[128:129], v193 offset:32768\n" \
  "ds_read_b64_tr_b16 v[130:131], v193 offset:32896\n" \
  "ds_read_b64_tr_b16 v[132:133], v193 offset:33792\n" \
  "ds_read_b64_tr_b16 v[134:135], v193 offset:33920\n" \
  "s_waitcnt lgkmcnt(4)\n" \
  "ds_read_b64_tr_b16 v[136:137], v193 offset:34816\n" \
  "ds_read_b64_tr_b16 v[138:139], v193 offset:34944\n" \
  "ds_read_b64_tr_b16 v[140:141], v193 offset:35840\n" \
  "ds_read_b64_tr_b16 v[142:143], v193 offset:35968\n" \
  "ds_read_b64_tr_b16 v[144:145], v193 offset:36864\n" \
  "ds_read_b64_tr_b16 v[146:147], v193 offset:36992\n" \
  "ds_read_b64_tr_b16 v[148:149], v193 offset:37888\n" \
  "ds_read_b64_tr_b16 v[150:151], v193 offset:38016\n" \
  "s_waitcnt lgkmcnt(4)\n" \
  "ds_read_b64_tr_b16 v[152:153], v193 offset:38912\n" \
  "ds_read_b64_tr_b16 v[154:155], v193 offset:39040\n" \
  "ds_read_b64_tr_b16 v[156:157], v193 offset:39936\n" \
  "ds_read_b64_tr_b16 v[158:159], v193 offset:40064\n" \
  "ds_read_b64_tr_b16 v[160:161], v193 offset:40960\n" \
  "ds_read_b64_tr_b16 v[162:163], v193 offset:41088\n" \
  "ds_read_b64_tr_b16 v[164:165], v193 offset:41984\n" \
  "ds_read_b64_tr_b16 v[166:167], v193 offset:42112\n" \
  "s_waitcnt lgkmcnt(4)\n" \
  "ds_read_b64_tr_b16 v[168:169], v193 offset:43008\n" \
  "ds_read_b64_tr_b16 v[170:171], v193 offset:43136\n" \
  "ds_read_b64_tr_b16 v[172:173], v193 offset:44032\n" \
  "ds_read_b64_tr_b16 v[174:175], v193 offset:44160\n" \
  "ds_read_b64_tr_b16 v[176:177], v193 offset:45056\n" \
  "ds_read_b64_tr_b16 v[178:179], v193 offset:45184\n" \
  "ds_read_b64_tr_b16 v[180:181], v193 offset:46080\n" \
  "ds_read_b64_tr_b16 v[182:183], v193 offset:46208\n" \
  "s_waitcnt lgkmcnt(4)\n" \
  "ds_read_b64_tr_b16 v[184:185], v193 offset:47104\n" \
  "ds_read_b64_tr_b16 v[186:187], v193 offset:47232\n" \
  "ds_read_b64_tr_b16 v[188:189], v193 offset:48128\n" \
  "ds_read_b64_tr_b16 v[190:191], v193 offset:48256\n" \
  "v_mov_b32 v242, 0x3f803f80\n" \
  "v_mov_b32 v224, 0\n" \
  "v_mov_b32 v228, 0\n" \
  "v_mov_b32 v243, 0x3f803f80\n" \
  "v_mov_b32 v225, 0\n" \
  "v_mov_b32 v229, 0\n" \
  "v_mov_b32 v244, 0x3f803f80\n" \
  "v_mov_b32 v226, 0\n" \
  "v_mov_b32 v230, 0\n" \
  "v_mov_b32 v245, 0x3f803f80\n" \
  "v_mov_b32 v227, 0\n" \
  "v_mov_b32 v231, 0\n" \
  "v_mov_b32 v238, 0\n" \
  "v_mov_b32 v239, 0\n" \
  "s_waitcnt lgkmcnt(0)\n" \
  "s_barrier\n" \
  "v_mfma_f32_16x16x32_bf16 v[232:235], v[0:3], v[242:245], 0\n" \
  "v_mfma_f32_16x16x32_bf16 v[208:211], v[242:245], v[64:67], 0\n" \
  "v_mfma_f32_16x16x32_bf16 v[212:215], v[242:245], v[128:131], 0\n" \
  "v_mfma_f32_16x16x32_bf16 v[232:235], v[4:7], v[242:245], v[232:235]\n" \
  "v_mfma_f32_16x16x32_bf16 v[208:211], v[242:245], v[68:71], v[208:211]\n" \
  "v_mfma_f32_16x16x32_bf16 v[212:215], v[242:245], v[132:135], v[212:215]\n" \
  "v_mfma_f32_16x16x32_bf16 v[232:235], v[8:11], v[242:245], v[232:235]\n" \
  "v_mfma_f32_16x16x32_bf16 v[208:211], v[242:245], v[72:75], v[208:211]\n" \
  "v_mfma_f32_16x16x32_bf16 v[212:215], v[242:245], v[136:139], v[212:215]\n" \
  "v_mfma_f32_16x16x32_bf16 v[232:235], v[12:15], v[242:245], v[232:235]\n" \
  "v_mfma_f32_16x16x32_bf16 v[208:211], v[242:245], v[76:79], v[208:211]\n" \
  "v_mfma_f32_16x16x32_bf16 v[212:215], v[242:245], v[140:143], v[212:215]\n" \
  "v_mfma_f32_16x16x32_bf16 v[232:235], v[16:19], v[242:245], v[232:235]\n" \
  "v_mfma_f32_16x16x32_bf16 v[208:211], v[242:245], v[80:83], v[208:211]\n" \
  "v_mfma_f32_16x16x32_bf16 v[212:215], v[242:245], v[144:147], v[212:215]\n" \
  "v_mfma_f32_16x16x32_bf16 v[232:235], v[20:23], v[242:245], v[232:235]\n" \
  "v_mfma_f32_16x16x32_bf16 v[208:211], v[242:245], v[84:87], v[208:211]\n" \
  "v_mfma_f32_16x16x32_bf16 v[212:215], v[242:245], v[148:151], v[212:215]\n" \
  "v_mfma_f32_16x16x32_bf16 v[232:235], v[24:27], v[242:245], v[232:235]\n" \
  "v_mfma_f32_16x16x32_bf16 v[208:211], v[242:245], v[88:91], v[208:211]\n" \
  "v_mfma_f32_16x16x32_bf16 v[212:215], v[242:245], v[152:155], v[212:215]\n" \
  "v_mfma_f32_16x16x32_bf16 v[232:235], v[28:31], v[242:245], v[232:235]\n" \
  "v_mfma_f32_16x16x32_bf16 v[208:211], v[242:245], v[92:95], v[208:211]\n" \
  "v_mfma_f32_16x16x32_bf16 v[212:215], v[242:245], v[156:159], v[212:215]\n" \
  "v_mfma_f32_16x16x32_bf16 v[232:235], v[32:35], v[242:245], v[232:235]\n" \
  "v_mfma_f32_16x16x32_bf16 v[208:211], v[242:245], v[96:99], v[208:211]\n" \
  "v_mfma_f32_16x16x32_bf16 v[212:215], v[242:245], v[160:163], v[212:215]\n" \
  "v_mfma_f32_16x16x32_bf16 v[232:235], v[36:39], v[242:245], v[232:235]\n" \
  "v_mfma_f32_16x16x32_bf16 v[208:211], v[242:245], v[100:103], v[208:211]\n" \
  "v_mfma_f32_16x16x32_bf16 v[212:215], v[242:245], v[164:167], v[212:215]\n" \
  "v_mfma_f32_16x16x32_bf16 v[232:235], v[40:43], v[242:245], v[232:235]\n" \
  "v_mfma_f32_16x16x32_bf16 v[208:211], v[242:245], v[104:107], v[208:211]\n" \
  "v_mfma_f32_16x16x32_bf16 v[212:215], v[242:245], v[168:171], v[212:215]\n" \
  "v_mfma_f32_16x16x32_bf16 v[232:235], v[44:47], v[242:245], v[232:235]\n" \
  "v_mfma_f32_16x16x32_bf16 v[208:211], v[242:245], v[108:111], v[208:211]\n" \
  "v_mfma_f32_16x16x32_bf16 v[212:215], v[242:245], v[172:175], v[212:215]\n" \
  "v_mfma_f32_16x16x32_bf16 v[232:235], v[48:51], v[242:245], v[232:235]\n" \
  "v_mfma_f32_16x16x32_bf16 v[208:211], v[242:245], v[112:115], v[208:211]\n" \
  "v_mfma_f32_16x16x32_bf16 v[212:215], v[242:245], v[176:179], v[212:215]\n" \
  "v_mfma_f32_16x16x32_bf16 v[232:235], v[52:55], v[242:245], v[232:235]\n" \
  "v_mfma_f32_16x16x32_bf16 v[208:211], v[242:245], v[116:119], v[208:211]\n" \
  "v_mfma_f32_16x16x32_bf16 v[212:215], v[242:245], v[180:183], v[212:215]\n" \
  "v_mfma_f32_16x16x32_bf16 v[232:235], v[56:59], v[242:245], v[232:235]\n" \
  "v_mfma_f32_16x16x32_bf16 v[208:211], v[242:245], v[120:123], v[208:211]\n" \
  "v_mfma_f32_16x16x32_bf16 v[212:215], v[242:245], v[184:187], v[212:215]\n" \
  "v_mfma_f32_16x16x32_bf16 v[232:235], v[60:63], v[242:245], v[232:235]\n" \
  "v_mfma_f32_16x16x32_bf16 v[208:211], v[242:245], v[124:127], v[208:211]\n" \
  "v_mfma_f32_16x16x32_bf16 v[212:215], v[242:245], v[188:191], v[212:215]\n" \
  "v_and_b32 v192, 15, v254\n" \
  "v_lshrrev_b32 v193, 4, v254\n" \
  "s_mul_i32 s5, s40, 33280\n" \
  "v_mul_u32_u24 v248, 1040, v192\n" \
  "v_lshl_add_u32 v248, v193, 8, v248\n" \
  "v_add_u32 v248, s5, v248\n" \
  "s_lshl_b32 s42, s40, 7\n" \
  "s_add_u32 s42, s42, 149760\n" \
  "v_lshlrev_b32 v251, 2, v192\n" \
  "v_add_u32 v251, s42, v251\n" \
  "v_lshlrev_b32 v252, 4, v193\n" \
  "v_add_u32 v252, s42, v252\n" \
  "v_lshlrev_b32 v193, 2, v193\n" \
  "v_sub_u32 v193, v192, v193\n" \
  "v_cmp_eq_u32 s[8:9], 0, v193\n" \
  "v_cmp_eq_u32 s[10:11], 1, v193\n" \
  "v_cmp_eq_u32 s[12:13], 2, v193\n" \
  "v_cmp_eq_u32 s[14:15], 3, v193\n" \
  "v_mov_b32 v236, v208\n" \
  "v_mov_b32 v237, v212\n" \
  "s_lshl_b32 s42, s40, 14\n" \
  "s_add_u32 s86, s86, s42\n" \
  "s_addc_u32 s87, s87, 0\n" \
  "s_add_u32 s92, s92, s42\n" \
  "s_addc_u32 s93, s93, 0\n" \
  "s_mov_b32 s21, 16640\n" \
  "s_mov_b32 s4, 0\n" \
  "s_mov_b32 s7, 1\n" \
  "v_lshlrev_b32 v203, 7, v254\n" \
  "Lkvs5_%=:\n" \
  "global_load_dword v253, v203, s[98:99] sc1\n" \
  "s_waitcnt vmcnt(0)\n" \
  "v_cmp_gt_u32 vcc, s7, v253\n" \
  "s_nop 0\n" \
  "s_cbranch_vccz Lkvs5b_%=\n" \
  "s_sleep 8\n" \
  "s_sub_u32 s41, s41, 1\n" \
  "s_cmp_gt_i32 s41, 0\n" \
  "s_cbranch_scc1 Lkvs5_%=\n" \
  "Lkvs5b_%=:\n" \
  "s_add_u32 m0, s5, 0\n" \
  "s_nop 0\n" \
  "global_load_lds_dwordx4 v249, s[86:87] sc1\n" \
  "s_add_u32 m0, s5, 16\n" \
  "s_nop 0\n" \
  "global_load_lds_dwordx4 v249, s[86:87] offset:1024 sc1\n" \
  "s_add_u32 m0, s5, 32\n" \
  "s_nop 0\n" \
  "global_load_lds_dwordx4 v249, s[86:87] offset:2048 sc1\n" \
  "s_add_u32 m0, s5, 48\n" \
  "s_nop 0\n" \
  "global_load_lds_dwordx4 v249, s[86:87] offset:3072 sc1\n" \
  "s_add_u32 s86, s86, 4096\n" \
  "s_addc_u32 s87, s87, 0\n" \
  "s_add_u32 m0, s5, 4160\n" \
  "s_nop 0\n" \
  "global_load_lds_dwordx4 v249, s[86:87] sc1\n" \
  "s_add_u32 m0, s5, 4176\n" \
  "s_nop 0\n" \
  "global_load_lds_dwordx4 v249, s[86:87] offset:1024 sc1\n" \
  "s_add_u32 m0, s5, 4192\n" \
  "s_nop 0\n" \
  "global_load_lds_dwordx4 v249, s[86:87] offset:2048 sc1\n" \
  "s_add_u32 m0, s5, 4208\n" \
  "s_nop 0\n" \
  "global_load_lds_dwordx4 v249, s[86:87] offset:3072 sc1\n" \
  "s_add_u32 s86, s86, 4096\n" \
  "s_addc_u32 s87, s87, 0\n" \
  "s_add_u32 m0, s5, 8320\n" \
  "s_nop 0\n" \
  "global_load_lds_dwordx4 v249, s[86:87] sc1\n" \
  "s_add_u32 m0, s5, 8336\n" \
  "s_nop 0\n" \
  "global_load_lds_dwordx4 v249, s[86:87] offset:1024 sc1\n" \
  "s_add_u32 m0, s5, 8352\n" \
  "s_nop 0\n" \
  "global_load_lds_dwordx4 v249, s[86:87] offset:2048 sc1\n" \
  "s_add_u32 m0, s5, 8368\n" \
  "s_nop 0\n" \
  "global_load_lds_dwordx4 v249, s[86:87] offset:3072 sc1\n" \
  "s_add_u32 s86, s86, 4096\n" \
  "s_addc_u32 s87, s87, 0\n" \
  "s_add_u32 m0, s5, 12480\n" \
  "s_nop 0\n" \
  "global_load_lds_dwordx4 v249, s[86:87] sc1\n" \
  "s_add_u32 m0, s5, 12496\n" \
  "s_nop 0\n" \
  "global_load_lds_dwordx4 v249, s[86:87] offset:1024 sc1\n" \
  "s_add_u32 m0, s5, 12512\n" \
  "s_nop 0\n" \
  "global_load_lds_dwordx4 v249, s[86:87] offset:2048 sc1\n" \
  "s_add_u32 m0, s5, 12528\n" \
  "s_nop 0\n" \
  "global_load_lds_dwordx4 v249, s[86:87] offset:3072 sc1\n" \
  "s_add_u32 s86, s86, 53248\n" \
  "s_addc_u32 s87, s87, 0\n" \
  "s_mul_i32 s42, s40, 66560\n" \
  "s_add_u32 s42, s42, 16640\n" \
  "s_sub_u32 s5, s42, s5\n" \
  "s_waitcnt vmcnt(0)\n" \
  "Lkva1_%=:\n" \
  "s_waitcnt vmcnt(1)\n" \
  "s_add_u32 s7, s4, 2\n" \
  "v_cmp_gt_u32 vcc, s7, v253\n" \
  "v_lshlrev_b32 v203, 7, v254\n" \
  "s_cmp_eq_u64 vcc, 0\n" \
  "s_cselect_b32 s6, 1, 0\n" \
  "s_cselect_b64 s[16:17], -1, 0\n" \
  "global_load_dword v253, v203, s[98:99] sc1\n" \
  "ds_read_b128 v[192:195], v248\n" \
  "ds_read_b128 v[196:199], v248 offset:16\n" \
  "s_waitcnt lgkmcnt(1)\n" \
  "v_mfma_f32_16x16x32_bf16 v[216:219], v[192:195], v[192:195], 0\n" \
  "ds_read_b128 v[200:203], v248 offset:32\n" \
  "v_mfma_f32_16x16x32_bf16 v[220:223], v[242:245], v[192:195], 0\n" \
  "v_mfma_f32_16x16x32_bf16 v[204:207], v[0:3], v[192:195], 0\n" \
  "s_mov_b64 exec, s[16:17]\n" \
  "s_add_u32 m0, s5, 0\n" \
  "s_nop 0\n" \
  "global_load_lds_dwordx4 v249, s[86:87] sc1\n" \
  "s_mov_b64 exec, -1\n" \
  "v_mfma_f32_16x16x32_bf16 v[208:211], v[192:195], v[64:67], 0\n" \
  "v_mfma_f32_16x16x32_bf16 v[212:215], v[192:195], v[128:131], 0\n" \
  "s_mov_b64 exec, s[16:17]\n" \
  "s_add_u32 m0, s5, 16\n" \
  "s_nop 0\n" \
  "global_load_lds_dwordx4 v249, s[86:87] offset:1024 sc1\n" \
  "s_mov_b64 exec, -1\n" \
  "s_waitcnt lgkmcnt(1)\n" \
  "v_mfma_f32_16x16x32_bf16 v[216:219], v[196:199], v[196:199], v[216:219]\n" \
  "ds_read_b128 v[192:195], v248 offset:48\n" \
  "v_mfma_f32_16x16x32_bf16 v[220:223], v[242:245], v[196:199], v[220:223]\n" \
  "v_mfma_f32_16x16x32_bf16 v[204:207], v[4:7], v[196:199], v[204:207]\n" \
  "s_mov_b64 exec, s[16:17]\n" \
  "s_add_u32 m0, s5, 32\n" \
  "s_nop 0\n" \
  "global_load_lds_dwordx4 v249, s[86:87] offset:2048 sc1\n" \
  "s_mov_b64 exec, -1\n" \
  "v_mfma_f32_16x16x32_bf16 v[208:211], v[196:199], v[68:71], v[208:211]\n" \
  "v_mfma_f32_16x16x32_bf16 v[212:215], v[196:199], v[132:135], v[212:215]\n" \
  "s_mov_b64 exec, s[16:17]\n" \
  "s_add_u32 m0, s5, 48\n" \
  "s_nop 0\n" \
  "global_load_lds_dwordx4 v249, s[86:87] offset:3072 sc1\n" \
  "s_add_u32 s86, s86, 4096\n" \
  "s_addc_u32 s87, s87, 0\n" \
  "s_mov_b64 exec, -1\n" \
  "s_waitcnt lgkmcnt(1)\n" \
  "v_mfma_f32_16x16x32_bf16 v[216:219], v[200:203], v[200:203], v[216:219]\n" \
  "ds_read_b128 v[196:199], v248 offset:64\n" \
  "v_mfma_f32_16x16x32_bf16 v[220:223], v[242:245], v[200:203], v[220:223]\n" \
  "v_mfma_f32_16x16x32_bf16 v[204:207], v[8:11], v[200:203], v[204:207]\n" \
  "s_mov_b64 exec, s[16:17]\n" \
  "s_add_u32 m0, s5, 4160\n" \
  "s_nop 0\n" \
  "global_load_lds_dwordx4 v249, s[86:87] sc1\n" \
  "s_mov_b64 exec, -1\n" \
  "v_mfma_f32_16x16x32_bf16 v[208:211], v[200:203], v[72:75], v[208:211]\n" \
  "v_mfma_f32_16x16x32_bf16 v[212:215], v[200:203], v[136:139], v[212:215]\n" \
  "s_mov_b64 exec, s[16:17]\n" \
  "s_add_u32 m0, s5, 4176\n" \
  "s_nop 0\n" \
  "global_load_lds_dwordx4 v249, s[86:87] offset:1024 sc1\n" \
  "s_mov_b64 exec, -1\n" \
  "s_waitcnt lgkmcnt(1)\n" \
  "v_mfma_f32_16x16x32_bf16 v[216:219], v[192:195], v[192:195], v[216:219]\n" \
  "ds_read_b128 v[200:203], v248 offset:80\n" \
  "v_mfma_f32_16x16x32_bf16 v[220:223], v[242:245], v[192:195], v[220:223]\n" \
  "v_mfma_f32_16x16x32_bf16 v[204:207], v[12:15], v[192:195], v[204:207]\n" \
  "s_mov_b64 exec, s[16:17]\n" \
  "s_add_u32 m0, s5, 4192\n" \
  "s_nop 0\n" \
  "global_load_lds_dwordx4 v249, s[86:87] offset:2048 sc1\n" \
  "s_mov_b64 exec, -1\n" \
  "v_mfma_f32_16x16x32_bf16 v[208:211], v[192:195], v[76:79], v[208:211]\n" \
  "v_mfma_f32_16x16x32_bf16 v[212:215], v[192:195], v[140:143], v[212:215]\n" \
  "s_mov_b64 exec, s[16:17]\n" \
  "s_add_u32 m0, s5, 4208\n" \
  "s_nop 0\n" \
  "global_load_lds_dwordx4 v249, s[86:87] offset:3072 sc1\n" \
  "s_add_u32 s86, s86, 4096\n" \
  "s_addc_u32 s87, s87, 0\n" \
  "s_mov_b64 exec, -1\n" \
  "s_waitcnt lgkmcnt(1)\n" \
  "v_mfma_f32_16x16x32_bf16 v[216:219], v[196:199], v[196:199], v[216:219]\n" \
  "ds_read_b128 v[192:195], v248 offset:96\n" \
  "v_mfma_f32_16x16x32_bf16 v[220:223], v[242:245], v[196:199], v[220:223]\n" \
  "v_mfma_f32_16x16x32_bf16 v[204:207], v[16:19], v[196:199], v[204:207]\n" \
  "s_mov_b64 exec, s[16:17]\n" \
  "s_add_u32 m0, s5, 8320\n" \
  "s_nop 0\n" \
  "global_load_lds_dwordx4 v249, s[86:87] sc1\n" \
  "s_mov_b64 exec, -1\n" \
  "v_mfma_f32_16x16x32_bf16 v[208:211], v[196:199], v[80:83], v[208:211]\n" \
  "v_mfma_f32_16x16x32_bf16 v[212:215], v[196:199], v[144:147], v[212:215]\n" \
  "s_mov_b64 exec, s[16:17]\n" \
  "s_add_u32 m0, s5, 8336\n" \
  "s_nop 0\n" \
  "global_load_lds_dwordx4 v249, s[86:87] offset:1024 sc1\n" \
  "s_mov_b64 exec, -1\n" \
  "s_waitcnt lgkmcnt(1)\n" \
  "v_mfma_f32_16x16x32_bf16 v[216:219], v[200:203], v[200:203], v[216:219]\n" \
  "ds_read_b128 v[196:199], v248 offset:112\n" \
  "v_mfma_f32_16x16x32_bf16 v[220:223], v[242:245], v[200:203], v[220:223]\n" \
  "v_mfma_f32_16x16x32_bf16 v[204:207], v[20:23], v[200:203], v[204:207]\n" \
  "s_mov_b64 exec, s[16:17]\n" \
  "s_add_u32 m0, s5, 8352\n" \
  "s_nop 0\n" \
  "global_load_lds_dwordx4 v249, s[86:87] offset:2048 sc1\n" \
  "s_mov_b64 exec, -1\n" \
  "v_mfma_f32_16x16x32_bf16 v[208:211], v[200:203], v[84:87], v[208:211]\n" \
  "v_mfma_f32_16x16x32_bf16 v[212:215], v[200:203], v[148:151], v[212:215]\n" \
  "s_mov_b64 exec, s[16:17]\n" \
  "s_add_u32 m0, s5, 8368\n" \
  "s_nop 0\n" \
  "global_load_lds_dwordx4 v249, s[86:87] offset:3072 sc1\n" \
  "s_add_u32 s86, s86, 4096\n" \
  "s_addc_u32 s87, s87, 0\n" \
  "s_mov_b64 exec, -1\n" \
  "s_waitcnt lgkmcnt(1)\n" \
  "v_mfma_f32_16x16x32_bf16 v[216:219], v[192:195], v[192:195], v[216:219]\n" \
  "ds_read_b128 v[200:203], v248 offset:128\n" \
  "v_mfma_f32_16x16x32_bf16 v[220:223], v[242:245], v[192:195], v[220:223]\n" \
  "v_mfma_f32_16x16x32_bf16 v[204:207], v[24:27], v[192:195], v[204:207]\n" \
  "s_mov_b64 exec, s[16:17]\n" \
  "s_add_u32 m0, s5, 12480\n" \
  "s_nop 0\n" \
  "global_load_lds_dwordx4 v249, s[86:87] sc1\n" \
  "s_mov_b64 exec, -1\n" \
  "v_mfma_f32_16x16x32_bf16 v[208:211], v[192:195], v[88:91], v[208:211]\n" \
  "v_mfma_f32_16x16x32_bf16 v[212:215], v[192:195], v[152:155], v[212:215]\n" \
  "s_mov_b64 exec, s[16:17]\n" \
  "s_add_u32 m0, s5, 12496\n" \
  "s_nop 0\n" \
  "global_load_lds_dwordx4 v249, s[86:87] offset:1024 sc1\n" \
  "s_mov_b64 exec, -1\n" \
  "s_waitcnt lgkmcnt(1)\n" \
  "v_mfma_f32_16x16x32_bf16 v[216:219], v[196:199], v[196:199], v[216:219]\n" \
  "ds_read_b128 v[192:195], v248 offset:144\n" \
  "v_mfma_f32_16x16x32_bf16 v[220:223], v[242:245], v[196:199], v[220:223]\n" \
  "v_mfma_f32_16x16x32_bf16 v[204:207], v[28:31], v[196:199], v[204:207]\n" \
  "s_mov_b64 exec, s[16:17]\n" \
  "s_add_u32 m0, s5, 12512\n" \
  "s_nop 0\n" \
  "global_load_lds_dwordx4 v249, s[86:87] offset:2048 sc1\n" \
  "s_mov_b64 exec, -1\n" \
  "v_mfma_f32_16x16x32_bf16 v[208:211], v[196:199], v[92:95], v[208:211]\n" \
  "v_mfma_f32_16x16x32_bf16 v[212:215], v[196:199], v[156:159], v[212:215]\n" \
  "s_mov_b64 exec, s[16:17]\n" \
  "s_add_u32 m0, s5, 12528\n" \
  "s_nop 0\n" \
  "global_load_lds_dwordx4 v249, s[86:87] offset:3072 sc1\n" \
  "s_add_u32 s86, s86, 53248\n" \
  "s_addc_u32 s87, s87, 0\n" \
  "s_mov_b64 exec, -1\n" \
  "s_waitcnt lgkmcnt(1)\n" \
  "v_mfma_f32_16x16x32_bf16 v[216:219], v[200:203], v[200:203], v[216:219]\n" \
  "ds_read_b128 v[196:199], v248 offset:160\n" \
  "v_mfma_f32_16x16x32_bf16 v[220:223], v[242:245], v[200:203], v[220:223]\n" \
  "v_mfma_f32_16x16x32_bf16 v[204:207], v[32:35], v[200:203], v[204:207]\n" \
  "v_mfma_f32_16x16x32_bf16 v[208:211], v[200:203], v[96:99], v[208:211]\n" \
  "v_mfma_f32_16x16x32_bf16 v[212:215], v[200:203], v[160:163], v[212:215]\n" \
  "s_waitcnt lgkmcnt(1)\n" \
  "v_mfma_f32_16x16x32_bf16 v[216:219], v[192:195], v[192:195], v[216:219]\n" \
  "ds_read_b128 v[200:203], v248 offset:176\n" \
  "v_mfma_f32_16x16x32_bf16 v[220:223], v[242:245], v[192:195], v[220:223]\n" \
  "v_mfma_f32_16x16x32_bf16 v[204:207], v[36:39], v[192:195], v[204:207]\n" \
  "v_mfma_f32_16x16x32_bf16 v[208:211], v[192:195], v[100:103], v[208:211]\n" \
  "v_mfma_f32_16x16x32_bf16 v[212:215], v[192:195], v[164:167], v[212:215]\n" \
  "s_waitcnt lgkmcnt(1)\n" \
  "v_mfma_f32_16x16x32_bf16 v[216:219], v[196:199], v[196:199], v[216:219]\n" \
  "ds_read_b128 v[192:195], v248 offset:192\n" \
  "v_mfma_f32_16x16x32_bf16 v[220:223], v[242:245], v[196:199], v[220:223]\n" \
  "v_mfma_f32_16x16x32_bf16 v[204:207], v[40:43], v[196:199], v[204:207]\n" \
  "v_mfma_f32_16x16x32_bf16 v[208:211], v[196:199], v[104:107], v[208:211]\n" \
  "v_mfma_f32_16x16x32_bf16 v[212:215], v[196:199], v[168:171], v[212:215]\n" \
  "s_waitcnt lgkmcnt(1)\n" \
  "v_mfma_f32_16x16x32_bf16 v[216:219], v[200:203], v[200:203], v[216:219]\n" \
  "ds_read_b128 v[196:199], v248 offset:208\n" \
  "v_mfma_f32_16x16x32_bf16 v[220:223], v[242:245], v[200:203], v[220:223]\n" \
  "v_mfma_f32_16x16x32_bf16 v[204:207], v[44:47], v[200:203], v[204:207]\n" \
  "v_mfma_f32_16x16x32_bf16 v[208:211], v[200:203], v[108:111], v[208:211]\n" \
  "v_mfma_f32_16x16x32_bf16 v[212:215], v[200:203], v[172:175], v[212:215]\n" \
  "s_waitcnt lgkmcnt(1)\n" \
  "v_mfma_f32_16x16x32_bf16 v[216:219], v[192:195], v[192:195], v[216:219]\n" \
  "ds_read_b128 v[200:203], v248 offset:224\n" \
  "v_mfma_f32_16x16x32_bf16 v[220:223], v[242:245], v[192:195], v[220:223]\n" \
  "v_mfma_f32_16x16x32_bf16 v[204:207], v[48:51], v[192:195], v[204:207]\n" \
  "v_mfma_f32_16x16x32_bf16 v[208:211], v[192:195], v[112:115], v[208:211]\n" \
  "v_mfma_f32_16x16x32_bf16 v[212:215], v[192:195], v[176:179], v[212:215]\n" \
  "s_waitcnt lgkmcnt(1)\n" \
  "v_mfma_f32_16x16x32_bf16 v[216:219], v[196:199], v[196:199], v[216:219]\n" \
  "ds_read_b128 v[192:195], v248 offset:240\n" \
  "v_mfma_f32_16x16x32_bf16 v[220:223], v[242:245], v[196:199], v[220:223]\n" \
  "v_mfma_f32_16x16x32_bf16 v[204:207], v[52:55], v[196:199], v[204:207]\n" \
  "v_mfma_f32_16x16x32_bf16 v[208:211], v[196:199], v[116:119], v[208:211]\n" \
  "v_mfma_f32_16x16x32_bf16 v[212:215], v[196:199], v[180:183], v[212:215]\n" \
  "s_waitcnt lgkmcnt(1)\n" \
  "v_mfma_f32_16x16x32_bf16 v[216:219], v[200:203], v[200:203], v[216:219]\n" \
  "v_mfma_f32_16x16x32_bf16 v[220:223], v[242:245], v[200:203], v[220:223]\n" \
  "v_mfma_f32_16x16x32_bf16 v[204:207], v[56:59], v[200:203], v[204:207]\n" \
  "v_mfma_f32_16x16x32_bf16 v[208:211], v[200:203], v[120:123], v[208:211]\n" \
  "v_mfma_f32_16x16x32_bf16 v[212:215], v[200:203], v[184:187], v[212:215]\n" \
  "s_waitcnt lgkmcnt(0)\n" \
  "v_mfma_f32_16x16x32_bf16 v[216:219], v[192:195], v[192:195], v[216:219]\n" \
  "v_mfma_f32_16x16x32_bf16 v[220:223], v[242:245], v[192:195], v[220:223]\n" \
  "v_mfma_f32_16x16x32_bf16 v[204:207], v[60:63], v[192:195], v[204:207]\n" \
  "v_mfma_f32_16x16x32_bf16 v[208:211], v[192:195], v[124:127], v[208:211]\n" \
  "v_mfma_f32_16x16x32_bf16 v[212:215], v[192:195], v[188:191], v[212:215]\n" \
  "v_mov_b32 v192, 0\n" \
  "v_cndmask_b32 v192, v192, v216, s[8:9]\n" \
  "v_cndmask_b32 v192, v192, v217, s[10:11]\n" \
  "v_cndmask_b32 v192, v192, v218, s[12:13]\n" \
  "v_cndmask_b32 v192, v192, v219, s[14:15]\n" \
  "v_mov_b32 v196, v192\n" \
  "s_nop 1\n" \
  "v_permlane16_swap_b32 v192, v196\n" \
  "v_add_f32 v192, v192, v196\n" \
  "v_mov_b32 v196, v192\n" \
  "s_nop 1\n" \
  "v_permlane32_swap_b32 v192, v196\n" \
  "v_add_f32 v192, v192, v196\n" \
  "v_mul_f32 v193, 0x3b000000, v220\n" \
  "v_mul_f32 v192, 0x3b000000, v192\n" \
  "v_fma_f32 v192, -v193, v193, v192\n" \
  "v_max_f32 v192, 0, v192\n" \
  "v_add_f32 v192, 0x3727c5ac, v192\n" \
  "v_rsq_f32 v194, v192\n" \
  "s_nop 0\n" \
  "v_mul_f32_e64 v195, -v193, v194\n" \
  "ds_write_b32 v251, v194\n" \
  "ds_write_b32 v251, v195 offset:64\n" \
  "ds_read_b128 v[196:199], v252\n" \
  "ds_read_b128 v[200:203], v252 offset:64\n" \
  "v_mul_f32 v204, v204, v194\n" \
  "v_fmac_f32 v204, v195, v232\n" \
  "v_mul_f32 v192, 0x3fb8aa3b, v204\n" \
  "v_min_f32 v192, 0, v192\n" \
  "v_exp_f32 v192, v192\n" \
  "v_max_f32 v204, 0, v204\n" \
  "v_add_f32 v204, 0x358637bd, v204\n" \
  "v_add_f32 v204, v204, v192\n" \
  "v_mul_f32 v205, v205, v194\n" \
  "v_fmac_f32 v205, v195, v233\n" \
  "v_mul_f32 v192, 0x3fb8aa3b, v205\n" \
  "v_min_f32 v192, 0, v192\n" \
  "v_exp_f32 v192, v192\n" \
  "v_max_f32 v205, 0, v205\n" \
  "v_add_f32 v205, 0x358637bd, v205\n" \
  "v_add_f32 v205, v205, v192\n" \
  "v_mul_f32 v206, v206, v194\n" \
  "v_fmac_f32 v206, v195, v234\n" \
  "v_mul_f32 v192, 0x3fb8aa3b, v206\n" \
  "v_min_f32 v192, 0, v192\n" \
  "v_exp_f32 v192, v192\n" \
  "v_max_f32 v206, 0, v206\n" \
  "v_add_f32 v206, 0x358637bd, v206\n" \
  "v_add_f32 v206, v206, v192\n" \
  "v_mul_f32 v207, v207, v194\n" \
  "v_fmac_f32 v207, v195, v235\n" \
  "v_mul_f32 v192, 0x3fb8aa3b, v207\n" \
  "v_min_f32 v192, 0, v192\n" \
  "v_exp_f32 v192, v192\n" \
  "v_max_f32 v207, 0, v207\n" \
  "v_add_f32 v207, 0x358637bd, v207\n" \
  "v_add_f32 v207, v207, v192\n" \
  "v_cvt_pk_bf16_f32 v216, v204, v205\n" \
  "v_cvt_pk_bf16_f32 v217, v206, v207\n" \
  "global_store_dwordx2 v249, v[216:217], s[92:93] sc1\n" \
  "s_add_u32 s92, s92, 65536\n" \
  "s_addc_u32 s93, s93, 0\n" \
  "s_waitcnt lgkmcnt(0)\n" \
  "v_mul_f32 v208, v208, v196\n" \
  "v_fmac_f32 v208, v200, v236\n" \
  "v_mul_f32 v192, 0x3fb8aa3b, v208\n" \
  "v_min_f32 v192, 0, v192\n" \
  "v_exp_f32 v192, v192\n" \
  "v_max_f32 v208, 0, v208\n" \
  "v_add_f32 v208, 0x358637bd, v208\n" \
  "v_add_f32 v208, v208, v192\n" \
  "v_mul_f32 v212, v212, v196\n" \
  "v_fmac_f32 v212, v200, v237\n" \
  "v_mul_f32 v209, v209, v197\n" \
  "v_fmac_f32 v209, v201, v236\n" \
  "v_mul_f32 v192, 0x3fb8aa3b, v209\n" \
  "v_min_f32 v192, 0, v192\n" \
  "v_exp_f32 v192, v192\n" \
  "v_max_f32 v209, 0, v209\n" \
  "v_add_f32 v209, 0x358637bd, v209\n" \
  "v_add_f32 v209, v209, v192\n" \
  "v_mul_f32 v213, v213, v197\n" \
  "v_fmac_f32 v213, v201, v237\n" \
  "v_mul_f32 v210, v210, v198\n" \
  "v_fmac_f32 v210, v202, v236\n" \
  "v_mul_f32 v192, 0x3fb8aa3b, v210\n" \
  "v_min_f32 v192, 0, v192\n" \
  "v_exp_f32 v192, v192\n" \
  "v_max_f32 v210, 0, v210\n" \
  "v_add_f32 v210, 0x358637bd, v210\n" \
  "v_add_f32 v210, v210, v192\n" \
  "v_mul_f32 v214, v214, v198\n" \
  "v_fmac_f32 v214, v202, v237\n" \
  "v_mul_f32 v211, v211, v199\n" \
  "v_fmac_f32 v211, v203, v236\n" \
  "v_mul_f32 v192, 0x3fb8aa3b, v211\n" \
  "v_min_f32 v192, 0, v192\n" \
  "v_exp_f32 v192, v192\n" \
  "v_max_f32 v211, 0, v211\n" \
  "v_add_f32 v211, 0x358637bd, v211\n" \
  "v_add_f32 v211, v211, v192\n" \
  "v_mul_f32 v215, v215, v199\n" \
  "v_fmac_f32 v215, v203, v237\n" \
  "v_cvt_pk_bf16_f32 v240, v208, v209\n" \
  "v_cvt_pk_bf16_f32 v241, v210, v211\n" \
  "v_cvt_pk_bf16_f32 v246, v212, v213\n" \
  "v_cvt_pk_bf16_f32 v247, v214, v215\n" \
  "s_nop 1\n" \
  "v_mfma_f32_16x16x32_bf16 v[224:227], v[238:241], v[244:247], v[224:227]\n" \
  "v_mfma_f32_16x16x32_bf16 v[228:231], v[238:241], v[242:245], v[228:231]\n" \
  "s_add_u32 s4, s4, 1\n" \
  "s_mul_i32 s43, s40, 33280\n" \
  "s_cmp_eq_u32 s5, s43\n" \
  "s_cselect_b32 s43, 0xffffbf00, s21\n" \
  "v_add_u32 v248, s43, v248\n" \
  "s_cmp_eq_u32 s4, 32\n" \
  "s_cbranch_scc1 Lkva1e_%=\n" \
  "s_cmp_eq_u32 s6, 1\n" \
  "s_cbranch_scc1 Lkva1f_%=\n" \
  "s_sub_u32 s86, s86, 65536\n" \
  "s_subb_u32 s87, s87, 0\n" \
  "v_lshlrev_b32 v203, 7, v254\n" \
  "Lkvs6_%=:\n" \
  "global_load_dword v253, v203, s[98:99] sc1\n" \
  "s_waitcnt vmcnt(0)\n" \
  "v_cmp_gt_u32 vcc, s7, v253\n" \
  "s_nop 0\n" \
  "s_cbranch_vccz Lkvs6b_%=\n" \
  "s_sleep 2\n" \
  "s_sub_u32 s41, s41, 1\n" \
  "s_cmp_gt_i32 s41, 0\n" \
  "s_cbranch_scc1 Lkvs6_%=\n" \
  "Lkvs6b_%=:\n" \
  "s_add_u32 m0, s5, 0\n" \
  "s_nop 0\n" \
  "global_load_lds_dwordx4 v249, s[86:87] sc1\n" \
  "s_add_u32 m0, s5, 16\n" \
  "s_nop 0\n" \
  "global_load_lds_dwordx4 v249, s[86:87] offset:1024 sc1\n" \
  "s_add_u32 m0, s5, 32\n" \
  "s_nop 0\n" \
  "global_load_lds_dwordx4 v249, s[86:87] offset:2048 sc1\n" \
  "s_add_u32 m0, s5, 48\n" \
  "s_nop 0\n" \
  "global_load_lds_dwordx4 v249, s[86:87] offset:3072 sc1\n" \
  "s_add_u32 s86, s86, 4096\n" \
  "s_addc_u32 s87, s87, 0\n" \
  "s_add_u32 m0, s5, 4160\n" \
  "s_nop 0\n" \
  "global_load_lds_dwordx4 v249, s[86:87] sc1\n" \
  "s_add_u32 m0, s5, 4176\n" \
  "s_nop 0\n" \
  "global_load_lds_dwordx4 v249, s[86:87] offset:1024 sc1\n" \
  "s_add_u32 m0, s5, 4192\n" \
  "s_nop 0\n" \
  "global_load_lds_dwordx4 v249, s[86:87] offset:2048 sc1\n" \
  "s_add_u32 m0, s5, 4208\n" \
  "s_nop 0\n" \
  "global_load_lds_dwordx4 v249, s[86:87] offset:3072 sc1\n" \
  "s_add_u32 s86, s86, 4096\n" \
  "s_addc_u32 s87, s87, 0\n" \
  "s_add_u32 m0, s5, 8320\n" \
  "s_nop 0\n" \
  "global_load_lds_dwordx4 v249, s[86:87] sc1\n" \
  "s_add_u32 m0, s5, 8336\n" \
  "s_nop 0\n" \
  "global_load_lds_dwordx4 v249, s[86:87] offset:1024 sc1\n" \
  "s_add_u32 m0, s5, 8352\n" \
  "s_nop 0\n" \
  "global_load_lds_dwordx4 v249, s[86:87] offset:2048 sc1\n" \
  "s_add_u32 m0, s5, 8368\n" \
  "s_nop 0\n" \
  "global_load_lds_dwordx4 v249, s[86:87] offset:3072 sc1\n" \
  "s_add_u32 s86, s86, 4096\n" \
  "s_addc_u32 s87, s87, 0\n" \
  "s_add_u32 m0, s5, 12480\n" \
  "s_nop 0\n" \
  "global_load_lds_dwordx4 v249, s[86:87] sc1\n" \
  "s_add_u32 m0, s5, 12496\n" \
  "s_nop 0\n" \
  "global_load_lds_dwordx4 v249, s[86:87] offset:1024 sc1\n" \
  "s_add_u32 m0, s5, 12512\n" \
  "s_nop 0\n" \
  "global_load_lds_dwordx4 v249, s[86:87] offset:2048 sc1\n" \
  "s_add_u32 m0, s5, 12528\n" \
  "s_nop 0\n" \
  "global_load_lds_dwordx4 v249, s[86:87] offset:3072 sc1\n" \
  "s_add_u32 s86, s86, 53248\n" \
  "s_addc_u32 s87, s87, 0\n" \
  "s_waitcnt vmcnt(0)\n" \
  "Lkva1f_%=:\n" \
  "s_mul_i32 s42, s40, 66560\n" \
  "s_add_u32 s42, s42, 16640\n" \
  "s_sub_u32 s5, s42, s5\n" \
  "s_branch Lkva1_%=\n" \
  "Lkva1e_%=:\n" \
  "s_barrier\n" \
  "s_mul_i32 s42, s40, 1088\n" \
  "v_add_u32 v192, s42, v249\n" \
  "ds_write_b128 v192, v[224:227]\n" \
  "v_lshrrev_b32 v193, 4, v254\n" \
  "v_lshlrev_b32 v194, 4, v193\n" \
  "v_add_u32 v192, s42, v194\n" \
  "ds_write_b128 v192, v[228:231] offset:1024\n" \
  "v_bfe_u32 v195, v254, 2, 2\n" \
  "v_lshlrev_b32 v192, 8, v195\n" \
  "v_lshl_add_u32 v192, v193, 6, v192\n" \
  "v_and_b32 v196, 3, v254\n" \
  "v_lshl_add_u32 v192, v196, 2, v192\n" \
  "v_lshl_add_u32 v195, v193, 2, v195\n" \
  "v_mul_u32_u24 v195, 1040, v195\n" \
  "v_lshl_add_u32 v195, v196, 4, v195\n" \
  "s_lshl_b32 s42, s40, 8\n" \
  "s_add_u32 s42, s42, 133120\n" \
  "v_add_u32 v195, s42, v195\n" \
  "s_waitcnt lgkmcnt(0)\n" \
  "s_barrier\n" \
  "ds_read_b32 v0, v192\n" \
  "ds_read_b32 v1, v192 offset:16\n" \
  "ds_read_b32 v2, v192 offset:32\n" \
  "ds_read_b32 v3, v192 offset:48\n" \
  "ds_read_b32 v4, v192 offset:1088\n" \
  "ds_read_b32 v5, v192 offset:1104\n" \
  "ds_read_b32 v6, v192 offset:1120\n" \
  "ds_read_b32 v7, v192 offset:1136\n" \
  "s_waitcnt lgkmcnt(0)\n" \
  "ds_read_b32 v8, v192 offset:2176\n" \
  "ds_read_b32 v9, v192 offset:2192\n" \
  "ds_read_b32 v10, v192 offset:2208\n" \
  "ds_read_b32 v11, v192 offset:2224\n" \
  "ds_read_b32 v12, v192 offset:3264\n" \
  "ds_read_b32 v13, v192 offset:3280\n" \
  "ds_read_b32 v14, v192 offset:3296\n" \
  "ds_read_b32 v15, v192 offset:3312\n" \
  "ds_read_b128 v[16:19], v194 offset:1024\n" \
  "ds_read_b128 v[20:23], v194 offset:2112\n" \
  "ds_read_b128 v[24:27], v194 offset:3200\n" \
  "ds_read_b128 v[28:31], v194 offset:4288\n" \
  "s_waitcnt lgkmcnt(0)\n" \
  "ds_read_b64_tr_b16 v[32:33], v195\n" \
  "ds_read_b64_tr_b16 v[34:35], v195 offset:8\n" \
  "ds_read_b64_tr_b16 v[36:37], v195 offset:64\n" \
  "ds_read_b64_tr_b16 v[38:39], v195 offset:72\n" \
  "ds_read_b64_tr_b16 v[40:41], v195 offset:128\n" \
  "ds_read_b64_tr_b16 v[42:43], v195 offset:136\n" \
  "ds_read_b64_tr_b16 v[44:45], v195 offset:192\n" \
  "ds_read_b64_tr_b16 v[46:47], v195 offset:200\n" \
  "ds_read_b64_tr_b16 v[48:49], v195\n" \
  "v_add_f32 v0, v0, v4\n" \
  "v_add_f32 v8, v8, v12\n" \
  "v_add_f32 v0, v0, v8\n" \
  "v_add_f32 v1, v1, v5\n" \
  "v_add_f32 v9, v9, v13\n" \
  "v_add_f32 v1, v1, v9\n" \
  "v_add_f32 v2, v2, v6\n" \
  "v_add_f32 v10, v10, v14\n" \
  "v_add_f32 v2, v2, v10\n" \
  "v_add_f32 v3, v3, v7\n" \
  "v_add_f32 v11, v11, v15\n" \
  "v_add_f32 v3, v3, v11\n" \
  "v_add_f32 v16, v16, v20\n" \
  "v_add_f32 v24, v24, v28\n" \
  "v_add_f32 v16, v16, v24\n" \
  "v_add_f32 v17, v17, v21\n" \
  "v_add_f32 v25, v25, v29\n" \
  "v_add_f32 v17, v17, v25\n" \
  "v_add_f32 v18, v18, v22\n" \
  "v_add_f32 v26, v26, v30\n" \
  "v_add_f32 v18, v18, v26\n" \
  "v_add_f32 v19, v19, v23\n" \
  "v_add_f32 v27, v27, v31\n" \
  "v_add_f32 v19, v19, v27\n" \
  "v_cvt_pk_bf16_f32 v4, v0, v1\n" \
  "v_cvt_pk_bf16_f32 v5, v2, v3\n" \
  "v_mov_b32 v6, 0\n" \
  "v_mov_b32 v7, 0\n" \
  "v_cvt_pk_bf16_f32 v8, v16, v17\n" \
  "v_cvt_pk_bf16_f32 v9, v18, v19\n" \
  "v_xor_b32 v196, s36, v254\n" \
  "v_and_b32 v196, 1, v196\n" \
  "v_cmp_eq_u32 vcc, 0, v196\n" \
  "s_nop 1\n" \
  "v_cndmask_b32 v8, 0, v8, vcc\n" \
  "v_cndmask_b32 v9, 0, v9, vcc\n" \
  "s_waitcnt lgkmcnt(0)\n" \
  "v_mfma_f32_16x16x32_bf16 v[64:67], v[4:7], v[32:35], 0\n" \
  "v_mfma_f32_16x16x32_bf16 v[68:71], v[4:7], v[34:37], 0\n" \
  "v_mfma_f32_16x16x32_bf16 v[72:75], v[4:7], v[36:39], 0\n" \
  "v_mfma_f32_16x16x32_bf16 v[76:79], v[4:7], v[38:41], 0\n" \
  "v_mfma_f32_16x16x32_bf16 v[80:83], v[4:7], v[40:43], 0\n" \
  "v_mfma_f32_16x16x32_bf16 v[84:87], v[4:7], v[42:45], 0\n" \
  "v_mfma_f32_16x16x32_bf16 v[88:91], v[4:7], v[44:47], 0\n" \
  "v_mfma_f32_16x16x32_bf16 v[92:95], v[4:7], v[46:49], 0\n" \
  "s_lshl_b32 s42, s40, 13\n" \
  "s_add_u32 s94, s94, s42\n" \
  "s_addc_u32 s95, s95, 0\n" \
  "v_cvt_pk_bf16_f32 v96, v64, v65\n" \
  "v_cvt_pk_bf16_f32 v97, v66, v67\n" \
  "global_store_dwordx2 v249, v[96:97], s[94:95] sc1\n" \
  "v_cvt_pk_bf16_f32 v98, v68, v69\n" \
  "v_cvt_pk_bf16_f32 v99, v70, v71\n" \
  "global_store_dwordx2 v249, v[98:99], s[94:95] offset:1024 sc1\n" \
  "v_cvt_pk_bf16_f32 v100, v72, v73\n" \
  "v_cvt_pk_bf16_f32 v101, v74, v75\n" \
  "global_store_dwordx2 v249, v[100:101], s[94:95] offset:2048 sc1\n" \
  "v_cvt_pk_bf16_f32 v102, v76, v77\n" \
  "v_cvt_pk_bf16_f32 v103, v78, v79\n" \
  "global_store_dwordx2 v249, v[102:103], s[94:95] offset:3072 sc1\n" \
  "s_add_u32 s94, s94, 4096\n" \
  "s_addc_u32 s95, s95, 0\n" \
  "v_cvt_pk_bf16_f32 v104, v80, v81\n" \
  "v_cvt_pk_bf16_f32 v105, v82, v83\n" \
  "global_store_dwordx2 v249, v[104:105], s[94:95] sc1\n" \
  "v_cvt_pk_bf16_f32 v106, v84, v85\n" \
  "v_cvt_pk_bf16_f32 v107, v86, v87\n" \
  "global_store_dwordx2 v249, v[106:107], s[94:95] offset:1024 sc1\n" \
  "v_cvt_pk_bf16_f32 v108, v88, v89\n" \
  "v_cvt_pk_bf16_f32 v109, v90, v91\n" \
  "global_store_dwordx2 v249, v[108:109], s[94:95] offset:2048 sc1\n" \
  "v_cvt_pk_bf16_f32 v110, v92, v93\n" \
  "v_cvt_pk_bf16_f32 v111, v94, v95\n" \
  "global_store_dwordx2 v249, v[110:111], s[94:95] offset:3072 sc1\n" \
  "s_cmp_eq_u32 s40, 0\n" \
  "s_cselect_b64 exec, -1, 0\n" \
  "global_store_dwordx2 v249, v[8:9], s[96:97] sc1\n" \
  "s_mov_b64 exec, -1\n" \
  "s_waitcnt vmcnt(0)\n" \
  "s_barrier\n" \
  "v_mov_b32 v192, 1\n" \
  "s_mov_b64 exec, s[60:61]\n" \
  "global_store_dword v255, v192, s[34:35] sc1\n" \
  "s_mov_b64 exec, -1\n" \
  "s_add_u32 s34, s34, 4096\n" \
  "s_addc_u32 s35, s35, 0\n" \
  "v_mov_b32 v192, 1\n" \
  "s_mov_b64 exec, s[60:61]\n" \
  "global_store_dword v255, v192, s[34:35] sc1\n" \
  "s_mov_b64 exec, -1\n" \
  "s_mov_b32 s69, 1\n" \
  "s_branch Lkvlayer_%=\n" \
  "Lkvend_%=:\n" \
  "s_endpgm\n"
#define QA_ASM \
  "s_mov_b32 s40, %[w]\n" \
  "v_mbcnt_lo_u32_b32 v254, -1, 0\n" \
  "v_mbcnt_hi_u32_b32 v254, -1, v254\n" \
  "v_lshlrev_b32 v255, 2, v254\n" \
  "v_add_u32 v255, 162816, v255\n" \
  "ds_read_b32 v255, v255\n" \
  "s_waitcnt lgkmcnt(0)\n" \
  "v_readlane_b32 s70, v255, 0\n" \
  "v_readlane_b32 s71, v255, 1\n" \
  "v_readlane_b32 s72, v255, 2\n" \
  "v_readlane_b32 s73, v255, 3\n" \
  "v_readlane_b32 s74, v255, 4\n" \
  "v_readlane_b32 s75, v255, 5\n" \
  "v_readlane_b32 s76, v255, 6\n" \
  "v_readlane_b32 s77, v255, 7\n" \
  "v_readlane_b32 s78, v255, 8\n" \
  "v_readlane_b32 s79, v255, 9\n" \
  "v_readlane_b32 s80, v255, 10\n" \
  "v_readlane_b32 s81, v255, 11\n" \
  "v_readlane_b32 s82, v255, 12\n" \
  "v_readlane_b32 s83, v255, 13\n" \
  "v_readlane_b32 s84, v255, 14\n" \
  "v_readlane_b32 s85, v255, 15\n" \
  "v_readlane_b32 s86, v255, 16\n" \
  "v_readlane_b32 s87, v255, 17\n" \
  "v_readlane_b32 s88, v255, 18\n" \
  "v_readlane_b32 s89, v255, 19\n" \
  "v_readlane_b32 s90, v255, 20\n" \
  "v_readlane_b32 s91, v255, 21\n" \
  "v_readlane_b32 s92, v255, 22\n" \
  "v_readlane_b32 s93, v255, 23\n" \
  "v_readlane_b32 s94, v255, 24\n" \
  "v_readlane_b32 s95, v255, 25\n" \
  "v_readlane_b32 s96, v255, 26\n" \
  "v_readlane_b32 s97, v255, 27\n" \
  "v_readlane_b32 s98, v255, 28\n" \
  "v_readlane_b32 s99, v255, 29\n" \
  "v_readlane_b32 s34, v255, 32\n" \
  "v_readlane_b32 s35, v255, 33\n" \
  "v_readlane_b32 s36, v255, 34\n" \
  "v_readlane_b32 s37, v255, 35\n" \
  "v_readlane_b32 s38, v255, 36\n" \
  "v_readlane_b32 s39, v255, 37\n" \
  "s_mov_b32 s69, 0\n" \
  "s_nop 7\n" \
  "s_nop 1\n" \
  "s_mov_b64 s[4:5], s[70:71]\n" \
  "s_mov_b64 s[6:7], s[72:73]\n" \
  "s_mov_b64 s[8:9], s[74:75]\n" \
  "s_mov_b64 s[10:11], s[76:77]\n" \
  "s_mov_b64 s[12:13], s[78:79]\n" \
  "s_mov_b64 s[14:15], s[80:81]\n" \
  "s_mov_b64 s[16:17], s[82:83]\n" \
  "s_mov_b64 s[18:19], s[84:85]\n" \
  "s_mov_b64 s[20:21], s[86:87]\n" \
  "s_mov_b64 s[22:23], s[88:89]\n" \
  "s_mov_b64 s[24:25], s[90:91]\n" \
  "s_mov_b64 s[26:27], s[92:93]\n" \
  "s_mov_b64 s[28:29], s[94:95]\n" \
  "s_mov_b64 s[30:31], s[96:97]\n" \
  "v_mov_b32 v255, 0\n" \
  "s_mov_b32 s41, 400000\n" \
  "s_cmp_eq_u32 s40, 0\n" \
  "s_cselect_b64 s[60:61], 1, 0\n" \
  "s_nop 7\n" \
  "s_nop 1\n" \
  "s_barrier\n" \
  "v_and_b32 v240, 15, v254\n" \
  "v_lshrrev_b32 v241, 4, v254\n" \
  "v_lshlrev_b32 v214, 4, v254\n" \
  "v_mov_b32 v225, 1\n" \
  "v_lshlrev_b32 v212, 4, v254\n" \
  "v_lshlrev_b32 v213, 3, v240\n" \
  "s_lshl_b32 s42, s40, 6\n" \
  "s_add_u32 s48, s4, s42\n" \
  "s_addc_u32 s49, s5, 0\n" \
  "s_lshl_b32 s44, s40, 14\n" \
  "s_add_u32 s44, s44, 65536\n" \
  "v_lshrrev_b32 v243, 2, v254\n" \
  "v_and_b32 v242, 3, v254\n" \
  "v_lshlrev_b32 v242, 4, v242\n" \
  "v_mul_u32_u24 v243, 8192, v243\n" \
  "v_add_u32 v242, v242, v243\n" \
  "v_lshrrev_b32 v243, 4, v254\n" \
  "v_lshlrev_b32 v243, 3, v243\n" \
  "v_bfe_u32 v244, v254, 2, 2\n" \
  "v_add_u32 v243, v243, v244\n" \
  "v_mul_u32_u24 v243, 64, v243\n" \
  "v_and_b32 v244, 3, v254\n" \
  "v_lshl_add_u32 v243, v244, 4, v243\n" \
  "v_add_u32 v243, s44, v243\n" \
  "s_add_u32 m0, s44, 0\n" \
  "s_nop 0\n" \
  "global_load_lds_dwordx4 v242, s[48:49]\n" \
  "s_add_u32 s48, s48, 131072\n" \
  "s_addc_u32 s49, s49, 0\n" \
  "s_add_u32 m0, s44, 1024\n" \
  "s_nop 0\n" \
  "global_load_lds_dwordx4 v242, s[48:49]\n" \
  "s_add_u32 s48, s48, 131072\n" \
  "s_addc_u32 s49, s49, 0\n" \
  "s_add_u32 m0, s44, 2048\n" \
  "s_nop 0\n" \
  "global_load_lds_dwordx4 v242, s[48:49]\n" \
  "s_add_u32 s48, s48, 131072\n" \
  "s_addc_u32 s49, s49, 0\n" \
  "s_add_u32 m0, s44, 3072\n" \
  "s_nop 0\n" \
  "global_load_lds_dwordx4 v242, s[48:49]\n" \
  "s_add_u32 s48, s48, 131072\n" \
  "s_addc_u32 s49, s49, 0\n" \
  "s_add_u32 m0, s44, 4096\n" \
  "s_nop 0\n" \
  "global_load_lds_dwordx4 v242, s[48:49]\n" \
  "s_add_u32 s48, s48, 131072\n" \
  "s_addc_u32 s49, s49, 0\n" \
  "s_add_u32 m0, s44, 5120\n" \
  "s_nop 0\n" \
  "global_load_lds_dwordx4 v242, s[48:49]\n" \
  "s_add_u32 s48, s48, 131072\n" \
  "s_addc_u32 s49, s49, 0\n" \
  "s_add_u32 m0, s44, 6144\n" \
  "s_nop 0\n" \
  "global_load_lds_dwordx4 v242, s[48:49]\n" \
  "s_add_u32 s48, s48, 131072\n" \
  "s_addc_u32 s49, s49, 0\n" \
  "s_add_u32 m0, s44, 7168\n" \
  "s_nop 0\n" \
  "global_load_lds_dwordx4 v242, s[48:49]\n" \
  "s_add_u32 s48, s48, 131072\n" \
  "s_addc_u32 s49, s49, 0\n" \
  "s_add_u32 m0, s44, 8192\n" \
  "s_nop 0\n" \
  "global_load_lds_dwordx4 v242, s[48:49]\n" \
  "s_add_u32 s48, s48, 131072\n" \
  "s_addc_u32 s49, s49, 0\n" \
  "s_add_u32 m0, s44, 9216\n" \
  "s_nop 0\n" \
  "global_load_lds_dwordx4 v242, s[48:49]\n" \
  "s_add_u32 s48, s48, 131072\n" \
  "s_addc_u32 s49, s49, 0\n" \
  "s_add_u32 m0, s44, 10240\n" \
  "s_nop 0\n" \
  "global_load_lds_dwordx4 v242, s[48:49]\n" \
  "s_add_u32 s48, s48, 131072\n" \
  "s_addc_u32 s49, s49, 0\n" \
  "s_add_u32 m0, s44, 11264\n" \
  "s_nop 0\n" \
  "global_load_lds_dwordx4 v242, s[48:49]\n" \
  "s_add_u32 s48, s48, 131072\n" \
  "s_addc_u32 s49, s49, 0\n" \
  "s_waitcnt vmcnt(8)\n" \
  "ds_read_b64_tr_b16 v[0:1], v243\n" \
  "ds_read_b64_tr_b16 v[2:3], v243 offset:256\n" \
  "ds_read_b64_tr_b16 v[4:5], v243 offset:8\n" \
  "ds_read_b64_tr_b16 v[6:7], v243 offset:264\n" \
  "ds_read_b64_tr_b16 v[8:9], v243 offset:2048\n" \
  "ds_read_b64_tr_b16 v[10:11], v243 offset:2304\n" \
  "ds_read_b64_tr_b16 v[12:13], v243 offset:2056\n" \
  "ds_read_b64_tr_b16 v[14:15], v243 offset:2312\n" \
  "s_add_u32 m0, s44, 12288\n" \
  "s_nop 0\n" \
  "global_load_lds_dwordx4 v242, s[48:49]\n" \
  "s_add_u32 s48, s48, 131072\n" \
  "s_addc_u32 s49, s49, 0\n" \
  "s_add_u32 m0, s44, 13312\n" \
  "s_nop 0\n" \
  "global_load_lds_dwordx4 v242, s[48:49]\n" \
  "s_add_u32 s48, s48, 131072\n" \
  "s_addc_u32 s49, s49, 0\n" \
  "s_add_u32 m0, s44, 14336\n" \
  "s_nop 0\n" \
  "global_load_lds_dwordx4 v242, s[48:49]\n" \
  "s_add_u32 s48, s48, 131072\n" \
  "s_addc_u32 s49, s49, 0\n" \
  "s_add_u32 m0, s44, 15360\n" \
  "s_nop 0\n" \
  "global_load_lds_dwordx4 v242, s[48:49]\n" \
  "s_add_u32 s48, s48, 131072\n" \
  "s_addc_u32 s49, s49, 0\n" \
  "s_waitcnt lgkmcnt(0)\n" \
  "s_waitcnt vmcnt(8)\n" \
  "ds_read_b64_tr_b16 v[16:17], v243 offset:4096\n" \
  "ds_read_b64_tr_b16 v[18:19], v243 offset:4352\n" \
  "ds_read_b64_tr_b16 v[20:21], v243 offset:4104\n" \
  "ds_read_b64_tr_b16 v[22:23], v243 offset:4360\n" \
  "ds_read_b64_tr_b16 v[24:25], v243 offset:6144\n" \
  "ds_read_b64_tr_b16 v[26:27], v243 offset:6400\n" \
  "ds_read_b64_tr_b16 v[28:29], v243 offset:6152\n" \
  "ds_read_b64_tr_b16 v[30:31], v243 offset:6408\n" \
  "s_add_u32 m0, s44, 0\n" \
  "s_nop 0\n" \
  "global_load_lds_dwordx4 v242, s[48:49]\n" \
  "s_add_u32 s48, s48, 131072\n" \
  "s_addc_u32 s49, s49, 0\n" \
  "s_add_u32 m0, s44, 1024\n" \
  "s_nop 0\n" \
  "global_load_lds_dwordx4 v242, s[48:49]\n" \
  "s_add_u32 s48, s48, 131072\n" \
  "s_addc_u32 s49, s49, 0\n" \
  "s_add_u32 m0, s44, 2048\n" \
  "s_nop 0\n" \
  "global_load_lds_dwordx4 v242, s[48:49]\n" \
  "s_add_u32 s48, s48, 131072\n" \
  "s_addc_u32 s49, s49, 0\n" \
  "s_add_u32 m0, s44, 3072\n" \
  "s_nop 0\n" \
  "global_load_lds_dwordx4 v242, s[48:49]\n" \
  "s_add_u32 s48, s48, 131072\n" \
  "s_addc_u32 s49, s49, 0\n" \
  "s_waitcnt lgkmcnt(0)\n" \
  "s_waitcnt vmcnt(8)\n" \
  "ds_read_b64_tr_b16 v[32:33], v243 offset:8192\n" \
  "ds_read_b64_tr_b16 v[34:35], v243 offset:8448\n" \
  "ds_read_b64_tr_b16 v[36:37], v243 offset:8200\n" \
  "ds_read_b64_tr_b16 v[38:39], v243 offset:8456\n" \
  "ds_read_b64_tr_b16 v[40:41], v243 offset:10240\n" \
  "ds_read_b64_tr_b16 v[42:43], v243 offset:10496\n" \
  "ds_read_b64_tr_b16 v[44:45], v243 offset:10248\n" \
  "ds_read_b64_tr_b16 v[46:47], v243 offset:10504\n" \
  "s_add_u32 m0, s44, 4096\n" \
  "s_nop 0\n" \
  "global_load_lds_dwordx4 v242, s[48:49]\n" \
  "s_add_u32 s48, s48, 131072\n" \
  "s_addc_u32 s49, s49, 0\n" \
  "s_add_u32 m0, s44, 5120\n" \
  "s_nop 0\n" \
  "global_load_lds_dwordx4 v242, s[48:49]\n" \
  "s_add_u32 s48, s48, 131072\n" \
  "s_addc_u32 s49, s49, 0\n" \
  "s_add_u32 m0, s44, 6144\n" \
  "s_nop 0\n" \
  "global_load_lds_dwordx4 v242, s[48:49]\n" \
  "s_add_u32 s48, s48, 131072\n" \
  "s_addc_u32 s49, s49, 0\n" \
  "s_add_u32 m0, s44, 7168\n" \
  "s_nop 0\n" \
  "global_load_lds_dwordx4 v242, s[48:49]\n" \
  "s_add_u32 s48, s48, 131072\n" \
  "s_addc_u32 s49, s49, 0\n" \
  "s_waitcnt lgkmcnt(0)\n" \
  "s_waitcnt vmcnt(8)\n" \
  "ds_read_b64_tr_b16 v[48:49], v243 offset:12288\n" \
  "ds_read_b64_tr_b16 v[50:51], v243 offset:12544\n" \
  "ds_read_b64_tr_b16 v[52:53], v243 offset:12296\n" \
  "ds_read_b64_tr_b16 v[54:55], v243 offset:12552\n" \
  "ds_read_b64_tr_b16 v[56:57], v243 offset:14336\n" \
  "ds_read_b64_tr_b16 v[58:59], v243 offset:14592\n" \
  "ds_read_b64_tr_b16 v[60:61], v243 offset:14344\n" \
  "ds_read_b64_tr_b16 v[62:63], v243 offset:14600\n" \
  "s_add_u32 m0, s44, 8192\n" \
  "s_nop 0\n" \
  "global_load_lds_dwordx4 v242, s[48:49]\n" \
  "s_add_u32 s48, s48, 131072\n" \
  "s_addc_u32 s49, s49, 0\n" \
  "s_add_u32 m0, s44, 9216\n" \
  "s_nop 0\n" \
  "global_load_lds_dwordx4 v242, s[48:49]\n" \
  "s_add_u32 s48, s48, 131072\n" \
  "s_addc_u32 s49, s49, 0\n" \
  "s_add_u32 m0, s44, 10240\n" \
  "s_nop 0\n" \
  "global_load_lds_dwordx4 v242, s[48:49]\n" \
  "s_add_u32 s48, s48, 131072\n" \
  "s_addc_u32 s49, s49, 0\n" \
  "s_add_u32 m0, s44, 11264\n" \
  "s_nop 0\n" \
  "global_load_lds_dwordx4 v242, s[48:49]\n" \
  "s_add_u32 s48, s48, 131072\n" \
  "s_addc_u32 s49, s49, 0\n" \
  "s_waitcnt lgkmcnt(0)\n" \
  "s_waitcnt vmcnt(8)\n" \
  "ds_read_b64_tr_b16 v[64:65], v243\n" \
  "ds_read_b64_tr_b16 v[66:67], v243 offset:256\n" \
  "ds_read_b64_tr_b16 v[68:69], v243 offset:8\n" \
  "ds_read_b64_tr_b16 v[70:71], v243 offset:264\n" \
  "ds_read_b64_tr_b16 v[72:73], v243 offset:2048\n" \
  "ds_read_b64_tr_b16 v[74:75], v243 offset:2304\n" \
  "ds_read_b64_tr_b16 v[76:77], v243 offset:2056\n" \
  "ds_read_b64_tr_b16 v[78:79], v243 offset:2312\n" \
  "s_add_u32 m0, s44, 12288\n" \
  "s_nop 0\n" \
  "global_load_lds_dwordx4 v242, s[48:49]\n" \
  "s_add_u32 s48, s48, 131072\n" \
  "s_addc_u32 s49, s49, 0\n" \
  "s_add_u32 m0, s44, 13312\n" \
  "s_nop 0\n" \
  "global_load_lds_dwordx4 v242, s[48:49]\n" \
  "s_add_u32 s48, s48, 131072\n" \
  "s_addc_u32 s49, s49, 0\n" \
  "s_add_u32 m0, s44, 14336\n" \
  "s_nop 0\n" \
  "global_load_lds_dwordx4 v242, s[48:49]\n" \
  "s_add_u32 s48, s48, 131072\n" \
  "s_addc_u32 s49, s49, 0\n" \
  "s_add_u32 m0, s44, 15360\n" \
  "s_nop 0\n" \
  "global_load_lds_dwordx4 v242, s[48:49]\n" \
  "s_add_u32 s48, s48, 131072\n" \
  "s_addc_u32 s49, s49, 0\n" \
  "s_waitcnt lgkmcnt(0)\n" \
  "s_waitcnt vmcnt(8)\n" \
  "ds_read_b64_tr_b16 v[80:81], v243 offset:4096\n" \
  "ds_read_b64_tr_b16 v[82:83], v243 offset:4352\n" \
  "ds_read_b64_tr_b16 v[84:85], v243 offset:4104\n" \
  "ds_read_b64_tr_b16 v[86:87], v243 offset:4360\n" \
  "ds_read_b64_tr_b16 v[88:89], v243 offset:6144\n" \
  "ds_read_b64_tr_b16 v[90:91], v243 offset:6400\n" \
  "ds_read_b64_tr_b16 v[92:93], v243 offset:6152\n" \
  "ds_read_b64_tr_b16 v[94:95], v243 offset:6408\n" \
  "s_waitcnt lgkmcnt(0)\n" \
  "s_waitcnt vmcnt(4)\n" \
  "ds_read_b64_tr_b16 v[96:97], v243 offset:8192\n" \
  "ds_read_b64_tr_b16 v[98:99], v243 offset:8448\n" \
  "ds_read_b64_tr_b16 v[100:101], v243 offset:8200\n" \
  "ds_read_b64_tr_b16 v[102:103], v243 offset:8456\n" \
  "ds_read_b64_tr_b16 v[104:105], v243 offset:10240\n" \
  "ds_read_b64_tr_b16 v[106:107], v243 offset:10496\n" \
  "ds_read_b64_tr_b16 v[108:109], v243 offset:10248\n" \
  "ds_read_b64_tr_b16 v[110:111], v243 offset:10504\n" \
  "s_waitcnt lgkmcnt(0)\n" \
  "s_waitcnt vmcnt(0)\n" \
  "ds_read_b64_tr_b16 v[112:113], v243 offset:12288\n" \
  "ds_read_b64_tr_b16 v[114:115], v243 offset:12544\n" \
  "ds_read_b64_tr_b16 v[116:117], v243 offset:12296\n" \
  "ds_read_b64_tr_b16 v[118:119], v243 offset:12552\n" \
  "ds_read_b64_tr_b16 v[120:121], v243 offset:14336\n" \
  "ds_read_b64_tr_b16 v[122:123], v243 offset:14592\n" \
  "ds_read_b64_tr_b16 v[124:125], v243 offset:14344\n" \
  "ds_read_b64_tr_b16 v[126:127], v243 offset:14600\n" \
  "s_waitcnt lgkmcnt(0)\n" \
  "s_barrier\n" \
  "s_cmp_lt_u32 s40, 2\n" \
  "s_cselect_b64 s[48:49], s[6:7], s[8:9]\n" \
  "s_and_b32 s43, s40, 1\n" \
  "s_lshl_b32 s43, s43, 7\n" \
  "s_add_u32 s48, s48, s43\n" \
  "s_addc_u32 s49, s49, 0\n" \
  "s_mul_i32 s44, s40, 24576\n" \
  "s_add_u32 s44, s44, 65536\n" \
  "v_lshrrev_b32 v243, 3, v254\n" \
  "v_and_b32 v242, 7, v254\n" \
  "v_bfe_u32 v244, v243, 1, 1\n" \
  "v_lshlrev_b32 v244, 2, v244\n" \
  "v_xor_b32 v242, v242, v244\n" \
  "v_lshlrev_b32 v242, 4, v242\n" \
  "v_mul_u32_u24 v243, 8192, v243\n" \
  "v_add_u32 v242, v242, v243\n" \
  "v_lshrrev_b32 v243, 4, v254\n" \
  "v_lshlrev_b32 v243, 3, v243\n" \
  "v_bfe_u32 v244, v254, 2, 2\n" \
  "v_add_u32 v243, v243, v244\n" \
  "v_mul_u32_u24 v243, 128, v243\n" \
  "v_and_b32 v244, 3, v254\n" \
  "v_lshl_add_u32 v243, v244, 4, v243\n" \
  "v_bfe_u32 v244, v254, 3, 1\n" \
  "v_lshl_add_u32 v243, v244, 6, v243\n" \
  "v_add_u32 v243, s44, v243\n" \
  "v_xor_b32 v244, 64, v243\n" \
  "s_add_u32 m0, s44, 0\n" \
  "s_nop 0\n" \
  "global_load_lds_dwordx4 v242, s[48:49]\n" \
  "s_add_u32 s48, s48, 65536\n" \
  "s_addc_u32 s49, s49, 0\n" \
  "s_add_u32 m0, s44, 1024\n" \
  "s_nop 0\n" \
  "global_load_lds_dwordx4 v242, s[48:49]\n" \
  "s_add_u32 s48, s48, 65536\n" \
  "s_addc_u32 s49, s49, 0\n" \
  "s_add_u32 m0, s44, 2048\n" \
  "s_nop 0\n" \
  "global_load_lds_dwordx4 v242, s[48:49]\n" \
  "s_add_u32 s48, s48, 65536\n" \
  "s_addc_u32 s49, s49, 0\n" \
  "s_add_u32 m0, s44, 3072\n" \
  "s_nop 0\n" \
  "global_load_lds_dwordx4 v242, s[48:49]\n" \
  "s_add_u32 s48, s48, 65536\n" \
  "s_addc_u32 s49, s49, 0\n" \
  "s_add_u32 m0, s44, 4096\n" \
  "s_nop 0\n" \
  "global_load_lds_dwordx4 v242, s[48:49]\n" \
  "s_add_u32 s48, s48, 65536\n" \
  "s_addc_u32 s49, s49, 0\n" \
  "s_add_u32 m0, s44, 5120\n" \
  "s_nop 0\n" \
  "global_load_lds_dwordx4 v242, s[48:49]\n" \
  "s_add_u32 s48, s48, 65536\n" \
  "s_addc_u32 s49, s49, 0\n" \
  "s_add_u32 m0, s44, 6144\n" \
  "s_nop 0\n" \
  "global_load_lds_dwordx4 v242, s[48:49]\n" \
  "s_add_u32 s48, s48, 65536\n" \
  "s_addc_u32 s49, s49, 0\n" \
  "s_add_u32 m0, s44, 7168\n" \
  "s_nop 0\n" \
  "global_load_lds_dwordx4 v242, s[48:49]\n" \
  "s_add_u32 s48, s48, 65536\n" \
  "s_addc_u32 s49, s49, 0\n" \
  "s_add_u32 m0, s44, 8192\n" \
  "s_nop 0\n" \
  "global_load_lds_dwordx4 v242, s[48:49]\n" \
  "s_add_u32 s48, s48, 65536\n" \
  "s_addc_u32 s49, s49, 0\n" \
  "s_add_u32 m0, s44, 9216\n" \
  "s_nop 0\n" \
  "global_load_lds_dwordx4 v242, s[48:49]\n" \
  "s_add_u32 s48, s48, 65536\n" \
  "s_addc_u32 s49, s49, 0\n" \
  "s_add_u32 m0, s44, 10240\n" \
  "s_nop 0\n" \
  "global_load_lds_dwordx4 v242, s[48:49]\n" \
  "s_add_u32 s48, s48, 65536\n" \
  "s_addc_u32 s49, s49, 0\n" \
  "s_add_u32 m0, s44, 11264\n" \
  "s_nop 0\n" \
  "global_load_lds_dwordx4 v242, s[48:49]\n" \
  "s_add_u32 s48, s48, 65536\n" \
  "s_addc_u32 s49, s49, 0\n" \
  "s_add_u32 m0, s44, 12288\n" \
  "s_nop 0\n" \
  "global_load_lds_dwordx4 v242, s[48:49]\n" \
  "s_add_u32 s48, s48, 65536\n" \
  "s_addc_u32 s49, s49, 0\n" \
  "s_add_u32 m0, s44, 13312\n" \
  "s_nop 0\n" \
  "global_load_lds_dwordx4 v242, s[48:49]\n" \
  "s_add_u32 s48, s48, 65536\n" \
  "s_addc_u32 s49, s49, 0\n" \
  "s_add_u32 m0, s44, 14336\n" \
  "s_nop 0\n" \
  "global_load_lds_dwordx4 v242, s[48:49]\n" \
  "s_add_u32 s48, s48, 65536\n" \
  "s_addc_u32 s49, s49, 0\n" \
  "s_add_u32 m0, s44, 15360\n" \
  "s_nop 0\n" \
  "global_load_lds_dwordx4 v242, s[48:49]\n" \
  "s_add_u32 s48, s48, 65536\n" \
  "s_addc_u32 s49, s49, 0\n" \
  "s_waitcnt vmcnt(8)\n" \
  "ds_read_b64_tr_b16 a[0:1], v243\n" \
  "ds_read_b64_tr_b16 a[2:3], v243 offset:512\n" \
  "ds_read_b64_tr_b16 a[4:5], v243 offset:8\n" \
  "ds_read_b64_tr_b16 a[6:7], v243 offset:520\n" \
  "ds_read_b64_tr_b16 a[8:9], v244\n" \
  "ds_read_b64_tr_b16 a[10:11], v244 offset:512\n" \
  "ds_read_b64_tr_b16 a[12:13], v244 offset:8\n" \
  "ds_read_b64_tr_b16 a[14:15], v244 offset:520\n" \
  "ds_read_b64_tr_b16 a[16:17], v243 offset:4096\n" \
  "ds_read_b64_tr_b16 a[18:19], v243 offset:4608\n" \
  "ds_read_b64_tr_b16 a[20:21], v243 offset:4104\n" \
  "ds_read_b64_tr_b16 a[22:23], v243 offset:4616\n" \
  "s_waitcnt lgkmcnt(4)\n" \
  "ds_read_b64_tr_b16 a[24:25], v244 offset:4096\n" \
  "ds_read_b64_tr_b16 a[26:27], v244 offset:4608\n" \
  "ds_read_b64_tr_b16 a[28:29], v244 offset:4104\n" \
  "ds_read_b64_tr_b16 a[30:31], v244 offset:4616\n" \
  "s_add_u32 m0, s44, 16384\n" \
  "s_nop 0\n" \
  "global_load_lds_dwordx4 v242, s[48:49]\n" \
  "s_add_u32 s48, s48, 65536\n" \
  "s_addc_u32 s49, s49, 0\n" \
  "s_add_u32 m0, s44, 17408\n" \
  "s_nop 0\n" \
  "global_load_lds_dwordx4 v242, s[48:49]\n" \
  "s_add_u32 s48, s48, 65536\n" \
  "s_addc_u32 s49, s49, 0\n" \
  "s_add_u32 m0, s44, 18432\n" \
  "s_nop 0\n" \
  "global_load_lds_dwordx4 v242, s[48:49]\n" \
  "s_add_u32 s48, s48, 65536\n" \
  "s_addc_u32 s49, s49, 0\n" \
  "s_add_u32 m0, s44, 19456\n" \
  "s_nop 0\n" \
  "global_load_lds_dwordx4 v242, s[48:49]\n" \
  "s_add_u32 s48, s48, 65536\n" \
  "s_addc_u32 s49, s49, 0\n" \
  "s_add_u32 m0, s44, 20480\n" \
  "s_nop 0\n" \
  "global_load_lds_dwordx4 v242, s[48:49]\n" \
  "s_add_u32 s48, s48, 65536\n" \
  "s_addc_u32 s49, s49, 0\n" \
  "s_add_u32 m0, s44, 21504\n" \
  "s_nop 0\n" \
  "global_load_lds_dwordx4 v242, s[48:49]\n" \
  "s_add_u32 s48, s48, 65536\n" \
  "s_addc_u32 s49, s49, 0\n" \
  "s_add_u32 m0, s44, 22528\n" \
  "s_nop 0\n" \
  "global_load_lds_dwordx4 v242, s[48:49]\n" \
  "s_add_u32 s48, s48, 65536\n" \
  "s_addc_u32 s49, s49, 0\n" \
  "s_add_u32 m0, s44, 23552\n" \
  "s_nop 0\n" \
  "global_load_lds_dwordx4 v242, s[48:49]\n" \
  "s_add_u32 s48, s48, 65536\n" \
  "s_addc_u32 s49, s49, 0\n" \
  "s_waitcnt lgkmcnt(0)\n" \
  "s_waitcnt vmcnt(8)\n" \
  "ds_read_b64_tr_b16 a[32:33], v243 offset:8192\n" \
  "ds_read_b64_tr_b16 a[34:35], v243 offset:8704\n" \
  "ds_read_b64_tr_b16 a[36:37], v243 offset:8200\n" \
  "ds_read_b64_tr_b16 a[38:39], v243 offset:8712\n" \
  "ds_read_b64_tr_b16 a[40:41], v244 offset:8192\n" \
  "ds_read_b64_tr_b16 a[42:43], v244 offset:8704\n" \
  "ds_read_b64_tr_b16 a[44:45], v244 offset:8200\n" \
  "ds_read_b64_tr_b16 a[46:47], v244 offset:8712\n" \
  "ds_read_b64_tr_b16 a[48:49], v243 offset:12288\n" \
  "ds_read_b64_tr_b16 a[50:51], v243 offset:12800\n" \
  "ds_read_b64_tr_b16 a[52:53], v243 offset:12296\n" \
  "ds_read_b64_tr_b16 a[54:55], v243 offset:12808\n" \
  "s_waitcnt lgkmcnt(4)\n" \
  "ds_read_b64_tr_b16 a[56:57], v244 offset:12288\n" \
  "ds_read_b64_tr_b16 a[58:59], v244 offset:12800\n" \
  "ds_read_b64_tr_b16 a[60:61], v244 offset:12296\n" \
  "ds_read_b64_tr_b16 a[62:63], v244 offset:12808\n" \
  "s_add_u32 m0, s44, 0\n" \
  "s_nop 0\n" \
  "global_load_lds_dwordx4 v242, s[48:49]\n" \
  "s_add_u32 s48, s48, 65536\n" \
  "s_addc_u32 s49, s49, 0\n" \
  "s_add_u32 m0, s44, 1024\n" \
  "s_nop 0\n" \
  "global_load_lds_dwordx4 v242, s[48:49]\n" \
  "s_add_u32 s48, s48, 65536\n" \
  "s_addc_u32 s49, s49, 0\n" \
  "s_add_u32 m0, s44, 2048\n" \
  "s_nop 0\n" \
  "global_load_lds_dwordx4 v242, s[48:49]\n" \
  "s_add_u32 s48, s48, 65536\n" \
  "s_addc_u32 s49, s49, 0\n" \
  "s_add_u32 m0, s44, 3072\n" \
  "s_nop 0\n" \
  "global_load_lds_dwordx4 v242, s[48:49]\n" \
  "s_add_u32 s48, s48, 65536\n" \
  "s_addc_u32 s49, s49, 0\n" \
  "s_add_u32 m0, s44, 4096\n" \
  "s_nop 0\n" \
  "global_load_lds_dwordx4 v242, s[48:49]\n" \
  "s_add_u32 s48, s48, 65536\n" \
  "s_addc_u32 s49, s49, 0\n" \
  "s_add_u32 m0, s44, 5120\n" \
  "s_nop 0\n" \
  "global_load_lds_dwordx4 v242, s[48:49]\n" \
  "s_add_u32 s48, s48, 65536\n" \
  "s_addc_u32 s49, s49, 0\n" \
  "s_add_u32 m0, s44, 6144\n" \
  "s_nop 0\n" \
  "global_load_lds_dwordx4 v242, s[48:49]\n" \
  "s_add_u32 s48, s48, 65536\n" \
  "s_addc_u32 s49, s49, 0\n" \
  "s_add_u32 m0, s44, 7168\n" \
  "s_nop 0\n" \
  "global_load_lds_dwordx4 v242, s[48:49]\n" \
  "s_add_u32 s48, s48, 65536\n" \
  "s_addc_u32 s49, s49, 0\n" \
  "s_waitcnt lgkmcnt(0)\n" \
  "s_waitcnt vmcnt(8)\n" \
  "ds_read_b64_tr_b16 a[64:65], v243 offset:16384\n" \
  "ds_read_b64_tr_b16 a[66:67], v243 offset:16896\n" \
  "ds_read_b64_tr_b16 a[68:69], v243 offset:16392\n" \
  "ds_read_b64_tr_b16 a[70:71], v243 offset:16904\n" \
  "ds_read_b64_tr_b16 a[72:73], v244 offset:16384\n" \
  "ds_read_b64_tr_b16 a[74:75], v244 offset:16896\n" \
  "ds_read_b64_tr_b16 a[76:77], v244 offset:16392\n" \
  "ds_read_b64_tr_b16 a[78:79], v244 offset:16904\n" \
  "ds_read_b64_tr_b16 a[80:81], v243 offset:20480\n" \
  "ds_read_b64_tr_b16 a[82:83], v243 offset:20992\n" \
  "ds_read_b64_tr_b16 a[84:85], v243 offset:20488\n" \
  "ds_read_b64_tr_b16 a[86:87], v243 offset:21000\n" \
  "s_waitcnt lgkmcnt(4)\n" \
  "ds_read_b64_tr_b16 a[88:89], v244 offset:20480\n" \
  "ds_read_b64_tr_b16 a[90:91], v244 offset:20992\n" \
  "ds_read_b64_tr_b16 a[92:93], v244 offset:20488\n" \
  "ds_read_b64_tr_b16 a[94:95], v244 offset:21000\n" \
  "s_add_u32 m0, s44, 8192\n" \
  "s_nop 0\n" \
  "global_load_lds_dwordx4 v242, s[48:49]\n" \
  "s_add_u32 s48, s48, 65536\n" \
  "s_addc_u32 s49, s49, 0\n" \
  "s_add_u32 m0, s44, 9216\n" \
  "s_nop 0\n" \
  "global_load_lds_dwordx4 v242, s[48:49]\n" \
  "s_add_u32 s48, s48, 65536\n" \
  "s_addc_u32 s49, s49, 0\n" \
  "s_add_u32 m0, s44, 10240\n" \
  "s_nop 0\n" \
  "global_load_lds_dwordx4 v242, s[48:49]\n" \
  "s_add_u32 s48, s48, 65536\n" \
  "s_addc_u32 s49, s49, 0\n" \
  "s_add_u32 m0, s44, 11264\n" \
  "s_nop 0\n" \
  "global_load_lds_dwordx4 v242, s[48:49]\n" \
  "s_add_u32 s48, s48, 65536\n" \
  "s_addc_u32 s49, s49, 0\n" \
  "s_add_u32 m0, s44, 12288\n" \
  "s_nop 0\n" \
  "global_load_lds_dwordx4 v242, s[48:49]\n" \
  "s_add_u32 s48, s48, 65536\n" \
  "s_addc_u32 s49, s49, 0\n" \
  "s_add_u32 m0, s44, 13312\n" \
  "s_nop 0\n" \
  "global_load_lds_dwordx4 v242, s[48:49]\n" \
  "s_add_u32 s48, s48, 65536\n" \
  "s_addc_u32 s49, s49, 0\n" \
  "s_add_u32 m0, s44, 14336\n" \
  "s_nop 0\n" \
  "global_load_lds_dwordx4 v242, s[48:49]\n" \
  "s_add_u32 s48, s48, 65536\n" \
  "s_addc_u32 s49, s49, 0\n" \
  "s_add_u32 m0, s44, 15360\n" \
  "s_nop 0\n" \
  "global_load_lds_dwordx4 v242, s[48:49]\n" \
  "s_add_u32 s48, s48, 65536\n" \
  "s_addc_u32 s49, s49, 0\n" \
  "s_waitcnt lgkmcnt(0)\n" \
  "s_waitcnt vmcnt(8)\n" \
  "ds_read_b64_tr_b16 a[96:97], v243\n" \
  "ds_read_b64_tr_b16 a[98:99], v243 offset:512\n" \
  "ds_read_b64_tr_b16 a[100:101], v243 offset:8\n" \
  "ds_read_b64_tr_b16 a[102:103], v243 offset:520\n" \
  "ds_read_b64_tr_b16 a[104:105], v244\n" \
  "ds_read_b64_tr_b16 a[106:107], v244 offset:512\n" \
  "ds_read_b64_tr_b16 a[108:109], v244 offset:8\n" \
  "ds_read_b64_tr_b16 a[110:111], v244 offset:520\n" \
  "ds_read_b64_tr_b16 a[112:113], v243 offset:4096\n" \
  "ds_read_b64_tr_b16 a[114:115], v243 offset:4608\n" \
  "ds_read_b64_tr_b16 a[116:117], v243 offset:4104\n" \
  "ds_read_b64_tr_b16 a[118:119], v243 offset:4616\n" \
  "s_waitcnt lgkmcnt(4)\n" \
  "ds_read_b64_tr_b16 a[120:121], v244 offset:4096\n" \
  "ds_read_b64_tr_b16 a[122:123], v244 offset:4608\n" \
  "ds_read_b64_tr_b16 a[124:125], v244 offset:4104\n" \
  "ds_read_b64_tr_b16 a[126:127], v244 offset:4616\n" \
  "s_add_u32 m0, s44, 16384\n" \
  "s_nop 0\n" \
  "global_load_lds_dwordx4 v242, s[48:49]\n" \
  "s_add_u32 s48, s48, 65536\n" \
  "s_addc_u32 s49, s49, 0\n" \
  "s_add_u32 m0, s44, 17408\n" \
  "s_nop 0\n" \
  "global_load_lds_dwordx4 v242, s[48:49]\n" \
  "s_add_u32 s48, s48, 65536\n" \
  "s_addc_u32 s49, s49, 0\n" \
  "s_add_u32 m0, s44, 18432\n" \
  "s_nop 0\n" \
  "global_load_lds_dwordx4 v242, s[48:49]\n" \
  "s_add_u32 s48, s48, 65536\n" \
  "s_addc_u32 s49, s49, 0\n" \
  "s_add_u32 m0, s44, 19456\n" \
  "s_nop 0\n" \
  "global_load_lds_dwordx4 v242, s[48:49]\n" \
  "s_add_u32 s48, s48, 65536\n" \
  "s_addc_u32 s49, s49, 0\n" \
  "s_add_u32 m0, s44, 20480\n" \
  "s_nop 0\n" \
  "global_load_lds_dwordx4 v242, s[48:49]\n" \
  "s_add_u32 s48, s48, 65536\n" \
  "s_addc_u32 s49, s49, 0\n" \
  "s_add_u32 m0, s44, 21504\n" \
  "s_nop 0\n" \
  "global_load_lds_dwordx4 v242, s[48:49]\n" \
  "s_add_u32 s48, s48, 65536\n" \
  "s_addc_u32 s49, s49, 0\n" \
  "s_add_u32 m0, s44, 22528\n" \
  "s_nop 0\n" \
  "global_load_lds_dwordx4 v242, s[48:49]\n" \
  "s_add_u32 s48, s48, 65536\n" \
  "s_addc_u32 s49, s49, 0\n" \
  "s_add_u32 m0, s44, 23552\n" \
  "s_nop 0\n" \
  "global_load_lds_dwordx4 v242, s[48:49]\n" \
  "s_add_u32 s48, s48, 65536\n" \
  "s_addc_u32 s49, s49, 0\n" \
  "s_waitcnt lgkmcnt(0)\n" \
  "s_waitcnt vmcnt(8)\n" \
  "ds_read_b64_tr_b16 a[128:129], v243 offset:8192\n" \
  "ds_read_b64_tr_b16 a[130:131], v243 offset:8704\n" \
  "ds_read_b64_tr_b16 a[132:133], v243 offset:8200\n" \
  "ds_read_b64_tr_b16 a[134:135], v243 offset:8712\n" \
  "ds_read_b64_tr_b16 a[136:137], v244 offset:8192\n" \
  "ds_read_b64_tr_b16 a[138:139], v244 offset:8704\n" \
  "ds_read_b64_tr_b16 a[140:141], v244 offset:8200\n" \
  "ds_read_b64_tr_b16 a[142:143], v244 offset:8712\n" \
  "ds_read_b64_tr_b16 a[144:145], v243 offset:12288\n" \
  "ds_read_b64_tr_b16 a[146:147], v243 offset:12800\n" \
  "ds_read_b64_tr_b16 a[148:149], v243 offset:12296\n" \
  "ds_read_b64_tr_b16 a[150:151], v243 offset:12808\n" \
  "s_waitcnt lgkmcnt(4)\n" \
  "ds_read_b64_tr_b16 a[152:153], v244 offset:12288\n" \
  "ds_read_b64_tr_b16 a[154:155], v244 offset:12800\n" \
  "ds_read_b64_tr_b16 a[156:157], v244 offset:12296\n" \
  "ds_read_b64_tr_b16 a[158:159], v244 offset:12808\n" \
  "s_add_u32 m0, s44, 0\n" \
  "s_nop 0\n" \
  "global_load_lds_dwordx4 v242, s[48:49]\n" \
  "s_add_u32 s48, s48, 65536\n" \
  "s_addc_u32 s49, s49, 0\n" \
  "s_add_u32 m0, s44, 1024\n" \
  "s_nop 0\n" \
  "global_load_lds_dwordx4 v242, s[48:49]\n" \
  "s_add_u32 s48, s48, 65536\n" \
  "s_addc_u32 s49, s49, 0\n" \
  "s_add_u32 m0, s44, 2048\n" \
  "s_nop 0\n" \
  "global_load_lds_dwordx4 v242, s[48:49]\n" \
  "s_add_u32 s48, s48, 65536\n" \
  "s_addc_u32 s49, s49, 0\n" \
  "s_add_u32 m0, s44, 3072\n" \
  "s_nop 0\n" \
  "global_load_lds_dwordx4 v242, s[48:49]\n" \
  "s_add_u32 s48, s48, 65536\n" \
  "s_addc_u32 s49, s49, 0\n" \
  "s_add_u32 m0, s44, 4096\n" \
  "s_nop 0\n" \
  "global_load_lds_dwordx4 v242, s[48:49]\n" \
  "s_add_u32 s48, s48, 65536\n" \
  "s_addc_u32 s49, s49, 0\n" \
  "s_add_u32 m0, s44, 5120\n" \
  "s_nop 0\n" \
  "global_load_lds_dwordx4 v242, s[48:49]\n" \
  "s_add_u32 s48, s48, 65536\n" \
  "s_addc_u32 s49, s49, 0\n" \
  "s_add_u32 m0, s44, 6144\n" \
  "s_nop 0\n" \
  "global_load_lds_dwordx4 v242, s[48:49]\n" \
  "s_add_u32 s48, s48, 65536\n" \
  "s_addc_u32 s49, s49, 0\n" \
  "s_add_u32 m0, s44, 7168\n" \
  "s_nop 0\n" \
  "global_load_lds_dwordx4 v242, s[48:49]\n" \
  "s_add_u32 s48, s48, 65536\n" \
  "s_addc_u32 s49, s49, 0\n" \
  "s_waitcnt lgkmcnt(0)\n" \
  "s_waitcnt vmcnt(8)\n" \
  "ds_read_b64_tr_b16 a[160:161], v243 offset:16384\n" \
  "ds_read_b64_tr_b16 a[162:163], v243 offset:16896\n" \
  "ds_read_b64_tr_b16 a[164:165], v243 offset:16392\n" \
  "ds_read_b64_tr_b16 a[166:167], v243 offset:16904\n" \
  "ds_read_b64_tr_b16 a[168:169], v244 offset:16384\n" \
  "ds_read_b64_tr_b16 a[170:171], v244 offset:16896\n" \
  "ds_read_b64_tr_b16 a[172:173], v244 offset:16392\n" \
  "ds_read_b64_tr_b16 a[174:175], v244 offset:16904\n" \
  "ds_read_b64_tr_b16 a[176:177], v243 offset:20480\n" \
  "ds_read_b64_tr_b16 a[178:179], v243 offset:20992\n" \
  "ds_read_b64_tr_b16 a[180:181], v243 offset:20488\n" \
  "ds_read_b64_tr_b16 a[182:183], v243 offset:21000\n" \
  "s_waitcnt lgkmcnt(4)\n" \
  "ds_read_b64_tr_b16 a[184:185], v244 offset:20480\n" \
  "ds_read_b64_tr_b16 a[186:187], v244 offset:20992\n" \
  "ds_read_b64_tr_b16 a[188:189], v244 offset:20488\n" \
  "ds_read_b64_tr_b16 a[190:191], v244 offset:21000\n" \
  "s_add_u32 m0, s44, 8192\n" \
  "s_nop 0\n" \
  "global_load_lds_dwordx4 v242, s[48:49]\n" \
  "s_add_u32 s48, s48, 65536\n" \
  "s_addc_u32 s49, s49, 0\n" \
  "s_add_u32 m0, s44, 9216\n" \
  "s_nop 0\n" \
  "global_load_lds_dwordx4 v242, s[48:49]\n" \
  "s_add_u32 s48, s48, 65536\n" \
  "s_addc_u32 s49, s49, 0\n" \
  "s_add_u32 m0, s44, 10240\n" \
  "s_nop 0\n" \
  "global_load_lds_dwordx4 v242, s[48:49]\n" \
  "s_add_u32 s48, s48, 65536\n" \
  "s_addc_u32 s49, s49, 0\n" \
  "s_add_u32 m0, s44, 11264\n" \
  "s_nop 0\n" \
  "global_load_lds_dwordx4 v242, s[48:49]\n" \
  "s_add_u32 s48, s48, 65536\n" \
  "s_addc_u32 s49, s49, 0\n" \
  "s_add_u32 m0, s44, 12288\n" \
  "s_nop 0\n" \
  "global_load_lds_dwordx4 v242, s[48:49]\n" \
  "s_add_u32 s48, s48, 65536\n" \
  "s_addc_u32 s49, s49, 0\n" \
  "s_add_u32 m0, s44, 13312\n" \
  "s_nop 0\n" \
  "global_load_lds_dwordx4 v242, s[48:49]\n" \
  "s_add_u32 s48, s48, 65536\n" \
  "s_addc_u32 s49, s49, 0\n" \
  "s_add_u32 m0, s44, 14336\n" \
  "s_nop 0\n" \
  "global_load_lds_dwordx4 v242, s[48:49]\n" \
  "s_add_u32 s48, s48, 65536\n" \
  "s_addc_u32 s49, s49, 0\n" \
  "s_add_u32 m0, s44, 15360\n" \
  "s_nop 0\n" \
  "global_load_lds_dwordx4 v242, s[48:49]\n" \
  "s_add_u32 s48, s48, 65536\n" \
  "s_addc_u32 s49, s49, 0\n" \
  "s_waitcnt lgkmcnt(0)\n" \
  "s_waitcnt vmcnt(8)\n" \
  "ds_read_b64_tr_b16 a[192:193], v243\n" \
  "ds_read_b64_tr_b16 a[194:195], v243 offset:512\n" \
  "ds_read_b64_tr_b16 a[196:197], v243 offset:8\n" \
  "ds_read_b64_tr_b16 a[198:199], v243 offset:520\n" \
  "ds_read_b64_tr_b16 a[200:201], v244\n" \
  "ds_read_b64_tr_b16 a[202:203], v244 offset:512\n" \
  "ds_read_b64_tr_b16 a[204:205], v244 offset:8\n" \
  "ds_read_b64_tr_b16 a[206:207], v244 offset:520\n" \
  "ds_read_b64_tr_b16 a[208:209], v243 offset:4096\n" \
  "ds_read_b64_tr_b16 a[210:211], v243 offset:4608\n" \
  "ds_read_b64_tr_b16 a[212:213], v243 offset:4104\n" \
  "ds_read_b64_tr_b16 a[214:215], v243 offset:4616\n" \
  "s_waitcnt lgkmcnt(4)\n" \
  "ds_read_b64_tr_b16 a[216:217], v244 offset:4096\n" \
  "ds_read_b64_tr_b16 a[218:219], v244 offset:4608\n" \
  "ds_read_b64_tr_b16 a[220:221], v244 offset:4104\n" \
  "ds_read_b64_tr_b16 a[222:223], v244 offset:4616\n" \
  "s_waitcnt lgkmcnt(0)\n" \
  "s_waitcnt vmcnt(0)\n" \
  "ds_read_b64_tr_b16 a[224:225], v243 offset:8192\n" \
  "ds_read_b64_tr_b16 a[226:227], v243 offset:8704\n" \
  "ds_read_b64_tr_b16 a[228:229], v243 offset:8200\n" \
  "ds_read_b64_tr_b16 a[230:231], v243 offset:8712\n" \
  "ds_read_b64_tr_b16 a[232:233], v244 offset:8192\n" \
  "ds_read_b64_tr_b16 a[234:235], v244 offset:8704\n" \
  "ds_read_b64_tr_b16 a[236:237], v244 offset:8200\n" \
  "ds_read_b64_tr_b16 a[238:239], v244 offset:8712\n" \
  "ds_read_b64_tr_b16 a[240:241], v243 offset:12288\n" \
  "ds_read_b64_tr_b16 a[242:243], v243 offset:12800\n" \
  "ds_read_b64_tr_b16 a[244:245], v243 offset:12296\n" \
  "ds_read_b64_tr_b16 a[246:247], v243 offset:12808\n" \
  "s_waitcnt lgkmcnt(4)\n" \
  "ds_read_b64_tr_b16 a[248:249], v244 offset:12288\n" \
  "ds_read_b64_tr_b16 a[250:251], v244 offset:12800\n" \
  "ds_read_b64_tr_b16 a[252:253], v244 offset:12296\n" \
  "ds_read_b64_tr_b16 a[254:255], v244 offset:12808\n" \
  "s_waitcnt lgkmcnt(0)\n" \
  "v_mov_b32 v160, 0x3f803f80\n" \
  "v_mov_b32 v161, 0x3f803f80\n" \
  "v_mov_b32 v162, 0x3f803f80\n" \
  "v_mov_b32 v163, 0x3f803f80\n" \
  "s_nop 1\n" \
  "v_mfma_f32_16x16x32_bf16 v[184:187], v[0:3], v[160:163], 0\n" \
  "v_mfma_f32_16x16x32_bf16 v[188:191], v[4:7], v[160:163], 0\n" \
  "v_mfma_f32_16x16x32_bf16 v[184:187], v[8:11], v[160:163], v[184:187]\n" \
  "v_mfma_f32_16x16x32_bf16 v[188:191], v[12:15], v[160:163], v[188:191]\n" \
  "v_mfma_f32_16x16x32_bf16 v[184:187], v[16:19], v[160:163], v[184:187]\n" \
  "v_mfma_f32_16x16x32_bf16 v[188:191], v[20:23], v[160:163], v[188:191]\n" \
  "v_mfma_f32_16x16x32_bf16 v[184:187], v[24:27], v[160:163], v[184:187]\n" \
  "v_mfma_f32_16x16x32_bf16 v[188:191], v[28:31], v[160:163], v[188:191]\n" \
  "v_mfma_f32_16x16x32_bf16 v[184:187], v[32:35], v[160:163], v[184:187]\n" \
  "v_mfma_f32_16x16x32_bf16 v[188:191], v[36:39], v[160:163], v[188:191]\n" \
  "v_mfma_f32_16x16x32_bf16 v[184:187], v[40:43], v[160:163], v[184:187]\n" \
  "v_mfma_f32_16x16x32_bf16 v[188:191], v[44:47], v[160:163], v[188:191]\n" \
  "v_mfma_f32_16x16x32_bf16 v[184:187], v[48:51], v[160:163], v[184:187]\n" \
  "v_mfma_f32_16x16x32_bf16 v[188:191], v[52:55], v[160:163], v[188:191]\n" \
  "v_mfma_f32_16x16x32_bf16 v[184:187], v[56:59], v[160:163], v[184:187]\n" \
  "v_mfma_f32_16x16x32_bf16 v[188:191], v[60:63], v[160:163], v[188:191]\n" \
  "v_mfma_f32_16x16x32_bf16 v[184:187], v[64:67], v[160:163], v[184:187]\n" \
  "v_mfma_f32_16x16x32_bf16 v[188:191], v[68:71], v[160:163], v[188:191]\n" \
  "v_mfma_f32_16x16x32_bf16 v[184:187], v[72:75], v[160:163], v[184:187]\n" \
  "v_mfma_f32_16x16x32_bf16 v[188:191], v[76:79], v[160:163], v[188:191]\n" \
  "v_mfma_f32_16x16x32_bf16 v[184:187], v[80:83], v[160:163], v[184:187]\n" \
  "v_mfma_f32_16x16x32_bf16 v[188:191], v[84:87], v[160:163], v[188:191]\n" \
  "v_mfma_f32_16x16x32_bf16 v[184:187], v[88:91], v[160:163], v[184:187]\n" \
  "v_mfma_f32_16x16x32_bf16 v[188:191], v[92:95], v[160:163], v[188:191]\n" \
  "v_mfma_f32_16x16x32_bf16 v[184:187], v[96:99], v[160:163], v[184:187]\n" \
  "v_mfma_f32_16x16x32_bf16 v[188:191], v[100:103], v[160:163], v[188:191]\n" \
  "v_mfma_f32_16x16x32_bf16 v[184:187], v[104:107], v[160:163], v[184:187]\n" \
  "v_mfma_f32_16x16x32_bf16 v[188:191], v[108:111], v[160:163], v[188:191]\n" \
  "v_mfma_f32_16x16x32_bf16 v[184:187], v[112:115], v[160:163], v[184:187]\n" \
  "v_mfma_f32_16x16x32_bf16 v[188:191], v[116:119], v[160:163], v[188:191]\n" \
  "v_mfma_f32_16x16x32_bf16 v[184:187], v[120:123], v[160:163], v[184:187]\n" \
  "v_mfma_f32_16x16x32_bf16 v[188:191], v[124:127], v[160:163], v[188:191]\n" \
  "v_and_b32 v226, 15, v254\n" \
  "v_lshrrev_b32 v208, 4, v254\n" \
  "v_xor_b32 v208, v208, v226\n" \
  "v_lshlrev_b32 v208, 4, v208\n" \
  "v_lshl_add_u32 v208, v226, 10, v208\n" \
  "v_xor_b32 v209, 64, v208\n" \
  "v_xor_b32 v210, 128, v208\n" \
  "v_xor_b32 v211, 192, v208\n" \
  "s_lshl_b32 s42, s40, 10\n" \
  "s_add_u32 s42, s42, 114688\n" \
  "v_add_u32 v215, s42, v214\n" \
  "v_bfe_u32 v227, v254, 5, 1\n" \
  "v_lshlrev_b32 v227, 7, v227\n" \
  "v_xor_b32 v217, v227, v214\n" \
  "v_and_b32 v226, 7, v254\n" \
  "v_lshlrev_b32 v218, 7, v241\n" \
  "v_lshl_add_u32 v218, v226, 4, v218\n" \
  "s_lshr_b32 s42, s40, 1\n" \
  "s_lshl_b32 s42, s42, 12\n" \
  "s_and_b32 s43, s40, 1\n" \
  "s_lshl_b32 s43, s43, 15\n" \
  "s_or_b32 s42, s42, s43\n" \
  "v_add_u32 v218, s42, v218\n" \
  "v_lshlrev_b32 v219, 10, v226\n" \
  "v_lshl_add_u32 v219, v241, 4, v219\n" \
  "s_lshl_b32 s42, s40, 8\n" \
  "v_add_u32 v219, s42, v219\n" \
  "v_bfe_u32 v227, v254, 3, 1\n" \
  "v_lshlrev_b32 v220, 12, v227\n" \
  "v_lshl_add_u32 v220, v226, 4, v220\n" \
  "v_lshl_add_u32 v220, v241, 7, v220\n" \
  "s_lshl_b32 s42, s40, 10\n" \
  "s_add_u32 s42, s42, 131072\n" \
  "v_add_u32 v220, s42, v220\n" \
  "v_lshlrev_b32 v221, 4, v240\n" \
  "v_lshl_add_u32 v221, v241, 9, v221\n" \
  "s_lshr_b32 s42, s40, 1\n" \
  "s_lshl_b32 s43, s42, 14\n" \
  "s_add_u32 s43, s43, 65536\n" \
  "v_add_u32 v221, s43, v221\n" \
  "v_lshlrev_b32 v222, 4, v226\n" \
  "v_lshl_add_u32 v222, v241, 7, v222\n" \
  "s_lshl_b32 s44, s42, 12\n" \
  "s_add_u32 s44, s44, 131072\n" \
  "v_add_u32 v222, s44, v222\n" \
  "v_bfe_u32 v236, v254, 2, 2\n" \
  "v_bfe_u32 v237, v254, 1, 1\n" \
  "v_and_b32 v238, 1, v254\n" \
  "v_lshlrev_b32 v238, 3, v238\n" \
  "v_lshl_add_u32 v223, v241, 2, v236\n" \
  "v_lshlrev_b32 v227, 3, v237\n" \
  "v_xor_b32 v223, v223, v227\n" \
  "v_lshl_add_u32 v223, v223, 4, v238\n" \
  "v_lshl_add_u32 v223, v237, 9, v223\n" \
  "s_add_u32 s43, s43, 8192\n" \
  "v_add_u32 v223, s43, v223\n" \
  "v_and_b32 v224, 1, v241\n" \
  "v_lshl_add_u32 v224, v224, 2, v236\n" \
  "v_lshl_add_u32 v224, v224, 4, v238\n" \
  "v_lshl_add_u32 v224, v237, 7, v224\n" \
  "s_add_u32 s44, s44, 2048\n" \
  "v_add_u32 v224, s44, v224\n" \
  "s_and_b32 s43, s40, 1\n" \
  "s_lshl_b32 s43, s43, 4\n" \
  "v_add_u32 v228, s43, v240\n" \
  "v_lshlrev_b32 v227, 2, v241\n" \
  "v_sub_u32 v228, v228, v227\n" \
  "s_mov_b32 s54, 0x3e0293ee\n" \
  "s_cmp_lt_u32 s40, 2\n" \
  "s_cselect_b64 vcc, -1, 0\n" \
  "v_cndmask_b32 v216, v217, v214, vcc\n" \
  "Lqalayer_%=:\n" \
  "s_mov_b64 s[4:5], s[70:71]\n" \
  "s_mov_b64 s[6:7], s[72:73]\n" \
  "s_mov_b64 s[8:9], s[74:75]\n" \
  "s_mov_b64 s[10:11], s[76:77]\n" \
  "s_mov_b64 s[12:13], s[78:79]\n" \
  "s_mov_b64 s[14:15], s[80:81]\n" \
  "s_mov_b64 s[16:17], s[82:83]\n" \
  "s_mov_b64 s[18:19], s[84:85]\n" \
  "s_mov_b64 s[20:21], s[86:87]\n" \
  "s_mov_b64 s[22:23], s[88:89]\n" \
  "s_mov_b64 s[24:25], s[90:91]\n" \
  "s_mov_b64 s[26:27], s[92:93]\n" \
  "s_mov_b64 s[28:29], s[94:95]\n" \
  "s_mov_b64 s[30:31], s[96:97]\n" \
  "s_lshl_b32 s42, s69, 16\n" \
  "s_add_u32 s16, s16, s42\n" \
  "s_addc_u32 s17, s17, 0\n" \
  "s_add_u32 s18, s18, s42\n" \
  "s_addc_u32 s19, s19, 0\n" \
  "s_cmp_eq_u32 s69, 1\n" \
  "s_cselect_b64 s[24:25], s[34:35], s[24:25]\n" \
  "s_cselect_b64 s[26:27], s[36:37], s[26:27]\n" \
  "s_cselect_b32 s42, 252, 0\n" \
  "s_add_u32 s22, s22, s42\n" \
  "s_addc_u32 s23, s23, 0\n" \
  "v_mov_b32 v160, 0x3f803f80\n" \
  "v_mov_b32 v161, 0x3f803f80\n" \
  "v_mov_b32 v162, 0x3f803f80\n" \
  "v_mov_b32 v163, 0x3f803f80\n" \
  "s_nop 1\n" \
  "v_mfma_f32_16x16x32_bf16 v[192:195], a[0:3], v[160:163], 0\n" \
  "v_mfma_f32_16x16x32_bf16 v[196:199], a[4:7], v[160:163], 0\n" \
  "v_mfma_f32_16x16x32_bf16 v[200:203], a[8:11], v[160:163], 0\n" \
  "v_mfma_f32_16x16x32_bf16 v[204:207], a[12:15], v[160:163], 0\n" \
  "v_mfma_f32_16x16x32_bf16 v[192:195], a[16:19], v[160:163], v[192:195]\n" \
  "v_mfma_f32_16x16x32_bf16 v[196:199], a[20:23], v[160:163], v[196:199]\n" \
  "v_mfma_f32_16x16x32_bf16 v[200:203], a[24:27], v[160:163], v[200:203]\n" \
  "v_mfma_f32_16x16x32_bf16 v[204:207], a[28:31], v[160:163], v[204:207]\n" \
  "v_mfma_f32_16x16x32_bf16 v[192:195], a[32:35], v[160:163], v[192:195]\n" \
  "v_mfma_f32_16x16x32_bf16 v[196:199], a[36:39], v[160:163], v[196:199]\n" \
  "v_mfma_f32_16x16x32_bf16 v[200:203], a[40:43], v[160:163], v[200:203]\n" \
  "v_mfma_f32_16x16x32_bf16 v[204:207], a[44:47], v[160:163], v[204:207]\n" \
  "v_mfma_f32_16x16x32_bf16 v[192:195], a[48:51], v[160:163], v[192:195]\n" \
  "v_mfma_f32_16x16x32_bf16 v[196:199], a[52:55], v[160:163], v[196:199]\n" \
  "v_mfma_f32_16x16x32_bf16 v[200:203], a[56:59], v[160:163], v[200:203]\n" \
  "v_mfma_f32_16x16x32_bf16 v[204:207], a[60:63], v[160:163], v[204:207]\n" \
  "v_mfma_f32_16x16x32_bf16 v[192:195], a[64:67], v[160:163], v[192:195]\n" \
  "v_mfma_f32_16x16x32_bf16 v[196:199], a[68:71], v[160:163], v[196:199]\n" \
  "v_mfma_f32_16x16x32_bf16 v[200:203], a[72:75], v[160:163], v[200:203]\n" \
  "v_mfma_f32_16x16x32_bf16 v[204:207], a[76:79], v[160:163], v[204:207]\n" \
  "v_mfma_f32_16x16x32_bf16 v[192:195], a[80:83], v[160:163], v[192:195]\n" \
  "v_mfma_f32_16x16x32_bf16 v[196:199], a[84:87], v[160:163], v[196:199]\n" \
  "v_mfma_f32_16x16x32_bf16 v[200:203], a[88:91], v[160:163], v[200:203]\n" \
  "v_mfma_f32_16x16x32_bf16 v[204:207], a[92:95], v[160:163], v[204:207]\n" \
  "v_mfma_f32_16x16x32_bf16 v[192:195], a[96:99], v[160:163], v[192:195]\n" \
  "v_mfma_f32_16x16x32_bf16 v[196:199], a[100:103], v[160:163], v[196:199]\n" \
  "v_mfma_f32_16x16x32_bf16 v[200:203], a[104:107], v[160:163], v[200:203]\n" \
  "v_mfma_f32_16x16x32_bf16 v[204:207], a[108:111], v[160:163], v[204:207]\n" \
  "v_mfma_f32_16x16x32_bf16 v[192:195], a[112:115], v[160:163], v[192:195]\n" \
  "v_mfma_f32_16x16x32_bf16 v[196:199], a[116:119], v[160:163], v[196:199]\n" \
  "v_mfma_f32_16x16x32_bf16 v[200:203], a[120:123], v[160:163], v[200:203]\n" \
  "v_mfma_f32_16x16x32_bf16 v[204:207], a[124:127], v[160:163], v[204:207]\n" \
  "v_mfma_f32_16x16x32_bf16 v[192:195], a[128:131], v[160:163], v[192:195]\n" \
  "v_mfma_f32_16x16x32_bf16 v[196:199], a[132:135], v[160:163], v[196:199]\n" \
  "v_mfma_f32_16x16x32_bf16 v[200:203], a[136:139], v[160:163], v[200:203]\n" \
  "v_mfma_f32_16x16x32_bf16 v[204:207], a[140:143], v[160:163], v[204:207]\n" \
  "v_mfma_f32_16x16x32_bf16 v[192:195], a[144:147], v[160:163], v[192:195]\n" \
  "v_mfma_f32_16x16x32_bf16 v[196:199], a[148:151], v[160:163], v[196:199]\n" \
  "v_mfma_f32_16x16x32_bf16 v[200:203], a[152:155], v[160:163], v[200:203]\n" \
  "v_mfma_f32_16x16x32_bf16 v[204:207], a[156:159], v[160:163], v[204:207]\n" \
  "v_mfma_f32_16x16x32_bf16 v[192:195], a[160:163], v[160:163], v[192:195]\n" \
  "v_mfma_f32_16x16x32_bf16 v[196:199], a[164:167], v[160:163], v[196:199]\n" \
  "v_mfma_f32_16x16x32_bf16 v[200:203], a[168:171], v[160:163], v[200:203]\n" \
  "v_mfma_f32_16x16x32_bf16 v[204:207], a[172:175], v[160:163], v[204:207]\n" \
  "v_mfma_f32_16x16x32_bf16 v[192:195], a[176:179], v[160:163], v[192:195]\n" \
  "v_mfma_f32_16x16x32_bf16 v[196:199], a[180:183], v[160:163], v[196:199]\n" \
  "v_mfma_f32_16x16x32_bf16 v[200:203], a[184:187], v[160:163], v[200:203]\n" \
  "v_mfma_f32_16x16x32_bf16 v[204:207], a[188:191], v[160:163], v[204:207]\n" \
  "v_mfma_f32_16x16x32_bf16 v[192:195], a[192:195], v[160:163], v[192:195]\n" \
  "v_mfma_f32_16x16x32_bf16 v[196:199], a[196:199], v[160:163], v[196:199]\n" \
  "v_mfma_f32_16x16x32_bf16 v[200:203], a[200:203], v[160:163], v[200:203]\n" \
  "v_mfma_f32_16x16x32_bf16 v[204:207], a[204:207], v[160:163], v[204:207]\n" \
  "v_mfma_f32_16x16x32_bf16 v[192:195], a[208:211], v[160:163], v[192:195]\n" \
  "v_mfma_f32_16x16x32_bf16 v[196:199], a[212:215], v[160:163], v[196:199]\n" \
  "v_mfma_f32_16x16x32_bf16 v[200:203], a[216:219], v[160:163], v[200:203]\n" \
  "v_mfma_f32_16x16x32_bf16 v[204:207], a[220:223], v[160:163], v[204:207]\n" \
  "v_mfma_f32_16x16x32_bf16 v[192:195], a[224:227], v[160:163], v[192:195]\n" \
  "v_mfma_f32_16x16x32_bf16 v[196:199], a[228:231], v[160:163], v[196:199]\n" \
  "v_mfma_f32_16x16x32_bf16 v[200:203], a[232:235], v[160:163], v[200:203]\n" \
  "v_mfma_f32_16x16x32_bf16 v[204:207], a[236:239], v[160:163], v[204:207]\n" \
  "v_mfma_f32_16x16x32_bf16 v[192:195], a[240:243], v[160:163], v[192:195]\n" \
  "v_mfma_f32_16x16x32_bf16 v[196:199], a[244:247], v[160:163], v[196:199]\n" \
  "v_mfma_f32_16x16x32_bf16 v[200:203], a[248:251], v[160:163], v[200:203]\n" \
  "v_mfma_f32_16x16x32_bf16 v[204:207], a[252:255], v[160:163], v[204:207]\n" \
  "s_add_u32 s52, s30, 1\n" \
  "s_lshl_b32 s42, s69, 16\n" \
  "s_add_u32 s58, s38, s42\n" \
  "s_addc_u32 s59, s39, 0\n" \
  "Lqah_%=:\n" \
  "v_and_b32 v251, 3, v254\n" \
  "v_lshlrev_b32 v251, 7, v251\n" \
  "s_and_b32 s42, s52, 7\n" \
  "s_lshl_b32 s42, s42, 9\n" \
  "v_or_b32 v251, s42, v251\n" \
  "s_lshr_b32 s53, s52, 3\n" \
  "s_add_u32 s53, s53, 1\n" \
  "s_cmp_eq_u32 s40, 0\n" \
  "s_cbranch_scc0 Lqaw1_%=\n" \
  "Lqas2_%=:\n" \
  "global_load_dword v226, v251, s[58:59] sc1\n" \
  "s_waitcnt vmcnt(0)\n" \
  "v_cmp_gt_u32 vcc, s53, v226\n" \
  "s_nop 0\n" \
  "s_cbranch_vccz Lqas2b_%=\n" \
  "s_sleep 2\n" \
  "s_sub_u32 s41, s41, 1\n" \
  "s_cmp_gt_i32 s41, 0\n" \
  "s_cbranch_scc1 Lqas2_%=\n" \
  "Lqas2b_%=:\n" \
  "Lqaw1_%=:\n" \
  "s_barrier\n" \
  "s_lshl_b32 s42, s52, 15\n" \
  "s_lshl_b32 s43, s40, 13\n" \
  "s_add_u32 s42, s42, s43\n" \
  "s_add_u32 s4, s10, s42\n" \
  "s_addc_u32 s5, s11, 0\n" \
  "s_lshl_b32 s42, s52, 10\n" \
  "s_add_u32 s6, s98, s42\n" \
  "s_addc_u32 s7, s99, 0\n" \
  "s_lshl_b32 s42, s52, 14\n" \
  "s_add_u32 s8, s14, s42\n" \
  "s_addc_u32 s9, s15, 0\n" \
  "s_lshl_b32 s42, s52, 8\n" \
  "s_add_u32 s56, s12, s42\n" \
  "s_addc_u32 s57, s13, 0\n" \
  "s_and_b32 s44, s40, 1\n" \
  "s_lshl_b32 s44, s44, 7\n" \
  "v_xor_b32 v251, s44, v212\n" \
  "s_add_u32 m0, s43, 0\n" \
  "v_xor_b32 v227, 0, v251\n" \
  "global_load_lds_dwordx4 v227, s[4:5] sc1\n" \
  "v_xor_b32 v227, 16, v251\n" \
  "global_load_lds_dwordx4 v227, s[4:5] offset:1024 sc1\n" \
  "v_xor_b32 v227, 32, v251\n" \
  "global_load_lds_dwordx4 v227, s[4:5] offset:2048 sc1\n" \
  "v_xor_b32 v227, 48, v251\n" \
  "global_load_lds_dwordx4 v227, s[4:5] offset:3072 sc1\n" \
  "s_add_u32 s4, s4, 4096\n" \
  "s_addc_u32 s5, s5, 0\n" \
  "s_add_u32 m0, s43, 4096\n" \
  "v_xor_b32 v227, 64, v251\n" \
  "global_load_lds_dwordx4 v227, s[4:5] sc1\n" \
  "v_xor_b32 v227, 80, v251\n" \
  "global_load_lds_dwordx4 v227, s[4:5] offset:1024 sc1\n" \
  "v_xor_b32 v227, 96, v251\n" \
  "global_load_lds_dwordx4 v227, s[4:5] offset:2048 sc1\n" \
  "v_xor_b32 v227, 112, v251\n" \
  "global_load_lds_dwordx4 v227, s[4:5] offset:3072 sc1\n" \
  "v_lshlrev_b32 v251, 3, v254\n" \
  "global_load_dwordx2 v[242:243], v251, s[6:7] sc1\n" \
  "global_load_dwordx2 v[244:245], v251, s[6:7] offset:512 sc1\n" \
  "v_and_b32 v226, 15, v254\n" \
  "v_lshrrev_b32 v227, 4, v254\n" \
  "v_lshlrev_b32 v250, 4, v226\n" \
  "v_lshl_add_u32 v250, v227, 9, v250\n" \
  "s_lshl_b32 s42, s40, 12\n" \
  "v_add_u32 v250, s42, v250\n" \
  "s_waitcnt vmcnt(0)\n" \
  "s_barrier\n" \
  "ds_read_b128 v[160:163], v208\n" \
  "ds_read_b128 v[164:167], v209\n" \
  "ds_read_b128 v[168:171], v210\n" \
  "ds_read_b128 v[172:175], v211\n" \
  "s_waitcnt lgkmcnt(3)\n" \
  "v_mfma_f32_16x16x32_bf16 v[128:131], a[0:3], v[160:163], 0\n" \
  "ds_read_b128 v[176:179], v208 offset:256\n" \
  "v_mfma_f32_16x16x32_bf16 v[132:135], a[4:7], v[160:163], 0\n" \
  "v_mfma_f32_16x16x32_bf16 v[136:139], a[8:11], v[160:163], 0\n" \
  "v_mov_b32 v226, v242\n" \
  "v_mfma_f32_16x16x32_bf16 v[140:143], a[12:15], v[160:163], 0\n" \
  "v_permlane16_swap_b32 v242, v226\n" \
  "s_waitcnt lgkmcnt(3)\n" \
  "v_mfma_f32_16x16x32_bf16 v[128:131], a[16:19], v[164:167], v[128:131]\n" \
  "ds_read_b128 v[180:183], v209 offset:256\n" \
  "v_add_f32 v242, v242, v226\n" \
  "v_mfma_f32_16x16x32_bf16 v[132:135], a[20:23], v[164:167], v[132:135]\n" \
  "v_mov_b32 v226, v242\n" \
  "v_mfma_f32_16x16x32_bf16 v[136:139], a[24:27], v[164:167], v[136:139]\n" \
  "v_permlane32_swap_b32 v242, v226\n" \
  "v_mfma_f32_16x16x32_bf16 v[140:143], a[28:31], v[164:167], v[140:143]\n" \
  "v_add_f32 v242, v242, v226\n" \
  "s_waitcnt lgkmcnt(3)\n" \
  "v_mfma_f32_16x16x32_bf16 v[128:131], a[32:35], v[168:171], v[128:131]\n" \
  "ds_read_b128 v[160:163], v210 offset:256\n" \
  "v_mov_b32 v226, v243\n" \
  "v_mfma_f32_16x16x32_bf16 v[132:135], a[36:39], v[168:171], v[132:135]\n" \
  "v_permlane16_swap_b32 v243, v226\n" \
  "v_mfma_f32_16x16x32_bf16 v[136:139], a[40:43], v[168:171], v[136:139]\n" \
  "v_add_f32 v243, v243, v226\n" \
  "v_mfma_f32_16x16x32_bf16 v[140:143], a[44:47], v[168:171], v[140:143]\n" \
  "v_mov_b32 v226, v243\n" \
  "s_waitcnt lgkmcnt(3)\n" \
  "v_mfma_f32_16x16x32_bf16 v[128:131], a[48:51], v[172:175], v[128:131]\n" \
  "ds_read_b128 v[164:167], v211 offset:256\n" \
  "v_permlane32_swap_b32 v243, v226\n" \
  "v_mfma_f32_16x16x32_bf16 v[132:135], a[52:55], v[172:175], v[132:135]\n" \
  "v_add_f32 v243, v243, v226\n" \
  "v_mfma_f32_16x16x32_bf16 v[136:139], a[56:59], v[172:175], v[136:139]\n" \
  "v_mul_f32 v247, 0x3b000000, v242\n" \
  "v_mfma_f32_16x16x32_bf16 v[140:143], a[60:63], v[172:175], v[140:143]\n" \
  "v_mul_f32 v243, 0x3b000000, v243\n" \
  "s_waitcnt lgkmcnt(3)\n" \
  "v_mfma_f32_16x16x32_bf16 v[128:131], a[64:67], v[176:179], v[128:131]\n" \
  "ds_read_b128 v[168:171], v208 offset:512\n" \
  "v_fma_f32 v243, -v247, v247, v243\n" \
  "v_mfma_f32_16x16x32_bf16 v[132:135], a[68:71], v[176:179], v[132:135]\n" \
  "v_max_f32 v243, 0, v243\n" \
  "v_mfma_f32_16x16x32_bf16 v[136:139], a[72:75], v[176:179], v[136:139]\n" \
  "v_add_f32 v243, 0x3727c5ac, v243\n" \
  "v_mfma_f32_16x16x32_bf16 v[140:143], a[76:79], v[176:179], v[140:143]\n" \
  "v_rsq_f32 v246, v243\n" \
  "s_waitcnt lgkmcnt(3)\n" \
  "v_mfma_f32_16x16x32_bf16 v[128:131], a[80:83], v[180:183], v[128:131]\n" \
  "ds_read_b128 v[172:175], v209 offset:512\n" \
  "v_mul_f32_e64 v247, -v247, v246\n" \
  "v_mfma_f32_16x16x32_bf16 v[132:135], a[84:87], v[180:183], v[132:135]\n" \
  "s_cmp_eq_u32 s40, 0\n" \
  "s_cselect_b64 exec, 0xffff, 0\n" \
  "global_store_dwordx2 v251, v[246:247], s[56:57] sc1\n" \
  "s_mov_b64 exec, -1\n" \
  "v_mfma_f32_16x16x32_bf16 v[136:139], a[88:91], v[180:183], v[136:139]\n" \
  "v_mov_b32 v226, v244\n" \
  "v_mfma_f32_16x16x32_bf16 v[140:143], a[92:95], v[180:183], v[140:143]\n" \
  "v_permlane16_swap_b32 v244, v226\n" \
  "s_waitcnt lgkmcnt(3)\n" \
  "v_mfma_f32_16x16x32_bf16 v[128:131], a[96:99], v[160:163], v[128:131]\n" \
  "ds_read_b128 v[176:179], v210 offset:512\n" \
  "v_add_f32 v244, v244, v226\n" \
  "v_mfma_f32_16x16x32_bf16 v[132:135], a[100:103], v[160:163], v[132:135]\n" \
  "v_mov_b32 v226, v244\n" \
  "v_mfma_f32_16x16x32_bf16 v[136:139], a[104:107], v[160:163], v[136:139]\n" \
  "v_permlane32_swap_b32 v244, v226\n" \
  "v_mfma_f32_16x16x32_bf16 v[140:143], a[108:111], v[160:163], v[140:143]\n" \
  "v_add_f32 v244, v244, v226\n" \
  "s_waitcnt lgkmcnt(3)\n" \
  "v_mfma_f32_16x16x32_bf16 v[128:131], a[112:115], v[164:167], v[128:131]\n" \
  "ds_read_b128 v[180:183], v211 offset:512\n" \
  "v_mov_b32 v226, v245\n" \
  "v_mfma_f32_16x16x32_bf16 v[132:135], a[116:119], v[164:167], v[132:135]\n" \
  "v_permlane16_swap_b32 v245, v226\n" \
  "v_mfma_f32_16x16x32_bf16 v[136:139], a[120:123], v[164:167], v[136:139]\n" \
  "v_add_f32 v245, v245, v226\n" \
  "v_mfma_f32_16x16x32_bf16 v[140:143], a[124:127], v[164:167], v[140:143]\n" \
  "v_mov_b32 v226, v245\n" \
  "s_waitcnt lgkmcnt(3)\n" \
  "v_mfma_f32_16x16x32_bf16 v[128:131], a[128:131], v[168:171], v[128:131]\n" \
  "ds_read_b128 v[160:163], v208 offset:768\n" \
  "v_permlane32_swap_b32 v245, v226\n" \
  "v_mfma_f32_16x16x32_bf16 v[132:135], a[132:135], v[168:171], v[132:135]\n" \
  "v_add_f32 v245, v245, v226\n" \
  "v_mfma_f32_16x16x32_bf16 v[136:139], a[136:139], v[168:171], v[136:139]\n" \
  "v_mul_f32 v249, 0x3b000000, v244\n" \
  "v_mfma_f32_16x16x32_bf16 v[140:143], a[140:143], v[168:171], v[140:143]\n" \
  "v_mul_f32 v245, 0x3b000000, v245\n" \
  "s_waitcnt lgkmcnt(3)\n" \
  "v_mfma_f32_16x16x32_bf16 v[128:131], a[144:147], v[172:175], v[128:131]\n" \
  "ds_read_b128 v[164:167], v209 offset:768\n" \
  "v_fma_f32 v245, -v249, v249, v245\n" \
  "v_mfma_f32_16x16x32_bf16 v[132:135], a[148:151], v[172:175], v[132:135]\n" \
  "v_max_f32 v245, 0, v245\n" \
  "v_mfma_f32_16x16x32_bf16 v[136:139], a[152:155], v[172:175], v[136:139]\n" \
  "v_add_f32 v245, 0x3727c5ac, v245\n" \
  "v_mfma_f32_16x16x32_bf16 v[140:143], a[156:159], v[172:175], v[140:143]\n" \
  "v_rsq_f32 v248, v245\n" \
  "s_waitcnt lgkmcnt(3)\n" \
  "v_mfma_f32_16x16x32_bf16 v[128:131], a[160:163], v[176:179], v[128:131]\n" \
  "ds_read_b128 v[168:171], v210 offset:768\n" \
  "v_mul_f32_e64 v249, -v249, v248\n" \
  "v_mfma_f32_16x16x32_bf16 v[132:135], a[164:167], v[176:179], v[132:135]\n" \
  "s_cmp_eq_u32 s40, 0\n" \
  "s_cselect_b64 exec, 0xffff, 0\n" \
  "global_store_dwordx2 v251, v[248:249], s[56:57] offset:128 sc1\n" \
  "s_mov_b64 exec, -1\n" \
  "v_mfma_f32_16x16x32_bf16 v[136:139], a[168:171], v[176:179], v[136:139]\n" \
  "v_mfma_f32_16x16x32_bf16 v[140:143], a[172:175], v[176:179], v[140:143]\n" \
  "s_waitcnt lgkmcnt(3)\n" \
  "v_mfma_f32_16x16x32_bf16 v[128:131], a[176:179], v[180:183], v[128:131]\n" \
  "ds_read_b128 v[172:175], v211 offset:768\n" \
  "v_mfma_f32_16x16x32_bf16 v[132:135], a[180:183], v[180:183], v[132:135]\n" \
  "v_mfma_f32_16x16x32_bf16 v[136:139], a[184:187], v[180:183], v[136:139]\n" \
  "v_mfma_f32_16x16x32_bf16 v[140:143], a[188:191], v[180:183], v[140:143]\n" \
  "s_waitcnt lgkmcnt(3)\n" \
  "v_mfma_f32_16x16x32_bf16 v[128:131], a[192:195], v[160:163], v[128:131]\n" \
  "ds_read_b128 v[176:179], v208 offset:16384\n" \
  "v_mfma_f32_16x16x32_bf16 v[132:135], a[196:199], v[160:163], v[132:135]\n" \
  "v_mfma_f32_16x16x32_bf16 v[136:139], a[200:203], v[160:163], v[136:139]\n" \
  "v_mfma_f32_16x16x32_bf16 v[140:143], a[204:207], v[160:163], v[140:143]\n" \
  "s_waitcnt lgkmcnt(3)\n" \
  "v_mfma_f32_16x16x32_bf16 v[128:131], a[208:211], v[164:167], v[128:131]\n" \
  "ds_read_b128 v[180:183], v209 offset:16384\n" \
  "v_mfma_f32_16x16x32_bf16 v[132:135], a[212:215], v[164:167], v[132:135]\n" \
  "v_mfma_f32_16x16x32_bf16 v[136:139], a[216:219], v[164:167], v[136:139]\n" \
  "v_mfma_f32_16x16x32_bf16 v[140:143], a[220:223], v[164:167], v[140:143]\n" \
  "s_waitcnt lgkmcnt(3)\n" \
  "v_mfma_f32_16x16x32_bf16 v[128:131], a[224:227], v[168:171], v[128:131]\n" \
  "ds_read_b128 v[160:163], v210 offset:16384\n" \
  "v_mfma_f32_16x16x32_bf16 v[132:135], a[228:231], v[168:171], v[132:135]\n" \
  "v_mfma_f32_16x16x32_bf16 v[136:139], a[232:235], v[168:171], v[136:139]\n" \
  "v_mfma_f32_16x16x32_bf16 v[140:143], a[236:239], v[168:171], v[140:143]\n" \
  "s_waitcnt lgkmcnt(3)\n" \
  "v_mfma_f32_16x16x32_bf16 v[128:131], a[240:243], v[172:175], v[128:131]\n" \
  "ds_read_b128 v[164:167], v211 offset:16384\n" \
  "v_mfma_f32_16x16x32_bf16 v[132:135], a[244:247], v[172:175], v[132:135]\n" \
  "v_mfma_f32_16x16x32_bf16 v[136:139], a[248:251], v[172:175], v[136:139]\n" \
  "v_mfma_f32_16x16x32_bf16 v[140:143], a[252:255], v[172:175], v[140:143]\n" \
  "s_waitcnt lgkmcnt(3)\n" \
  "v_mfma_f32_16x16x32_bf16 v[144:147], a[0:3], v[176:179], 0\n" \
  "ds_read_b128 v[168:171], v208 offset:16640\n" \
  "v_mfma_f32_16x16x32_bf16 v[148:151], a[4:7], v[176:179], 0\n" \
  "v_mfma_f32_16x16x32_bf16 v[152:155], a[8:11], v[176:179], 0\n" \
  "v_mul_f32 v128, v128, v246\n" \
  "v_mfma_f32_16x16x32_bf16 v[156:159], a[12:15], v[176:179], 0\n" \
  "v_fmac_f32 v128, v247, v192\n" \
  "s_waitcnt lgkmcnt(3)\n" \
  "v_mfma_f32_16x16x32_bf16 v[144:147], a[16:19], v[180:183], v[144:147]\n" \
  "ds_read_b128 v[172:175], v209 offset:16640\n" \
  "v_mul_f32 v129, v129, v246\n" \
  "v_mfma_f32_16x16x32_bf16 v[148:151], a[20:23], v[180:183], v[148:151]\n" \
  "v_fmac_f32 v129, v247, v193\n" \
  "v_mfma_f32_16x16x32_bf16 v[152:155], a[24:27], v[180:183], v[152:155]\n" \
  "v_mul_f32 v130, v130, v246\n" \
  "v_mfma_f32_16x16x32_bf16 v[156:159], a[28:31], v[180:183], v[156:159]\n" \
  "v_fmac_f32 v130, v247, v194\n" \
  "s_waitcnt lgkmcnt(3)\n" \
  "v_mfma_f32_16x16x32_bf16 v[144:147], a[32:35], v[160:163], v[144:147]\n" \
  "ds_read_b128 v[176:179], v210 offset:16640\n" \
  "v_mul_f32 v131, v131, v246\n" \
  "v_mfma_f32_16x16x32_bf16 v[148:151], a[36:39], v[160:163], v[148:151]\n" \
  "v_fmac_f32 v131, v247, v195\n" \
  "v_mfma_f32_16x16x32_bf16 v[152:155], a[40:43], v[160:163], v[152:155]\n" \
  "v_cvt_pk_bf16_f32 v236, v128, v129\n" \
  "v_cvt_pk_bf16_f32 v237, v130, v131\n" \
  "v_mfma_f32_16x16x32_bf16 v[156:159], a[44:47], v[160:163], v[156:159]\n" \
  "v_mul_f32 v132, v132, v246\n" \
  "s_waitcnt lgkmcnt(3)\n" \
  "v_mfma_f32_16x16x32_bf16 v[144:147], a[48:51], v[164:167], v[144:147]\n" \
  "ds_read_b128 v[180:183], v211 offset:16640\n" \
  "v_fmac_f32 v132, v247, v196\n" \
  "v_mfma_f32_16x16x32_bf16 v[148:151], a[52:55], v[164:167], v[148:151]\n" \
  "v_mul_f32 v133, v133, v246\n" \
  "v_mfma_f32_16x16x32_bf16 v[152:155], a[56:59], v[164:167], v[152:155]\n" \
  "v_fmac_f32 v133, v247, v197\n" \
  "v_mfma_f32_16x16x32_bf16 v[156:159], a[60:63], v[164:167], v[156:159]\n" \
  "v_mul_f32 v134, v134, v246\n" \
  "s_waitcnt lgkmcnt(3)\n" \
  "v_mfma_f32_16x16x32_bf16 v[144:147], a[64:67], v[168:171], v[144:147]\n" \
  "ds_read_b128 v[160:163], v208 offset:16896\n" \
  "v_fmac_f32 v134, v247, v198\n" \
  "v_mfma_f32_16x16x32_bf16 v[148:151], a[68:71], v[168:171], v[148:151]\n" \
  "v_mul_f32 v135, v135, v246\n" \
  "v_mfma_f32_16x16x32_bf16 v[152:155], a[72:75], v[168:171], v[152:155]\n" \
  "v_fmac_f32 v135, v247, v199\n" \
  "v_mfma_f32_16x16x32_bf16 v[156:159], a[76:79], v[168:171], v[156:159]\n" \
  "v_cvt_pk_bf16_f32 v238, v132, v133\n" \
  "v_cvt_pk_bf16_f32 v239, v134, v135\n" \
  "s_waitcnt lgkmcnt(3)\n" \
  "v_mfma_f32_16x16x32_bf16 v[144:147], a[80:83], v[172:175], v[144:147]\n" \
  "ds_read_b128 v[164:167], v209 offset:16896\n" \
  "global_store_dwordx4 v250, v[236:239], s[8:9] sc1\n" \
  "v_mfma_f32_16x16x32_bf16 v[148:151], a[84:87], v[172:175], v[148:151]\n" \
  "v_mul_f32 v136, v136, v246\n" \
  "v_mfma_f32_16x16x32_bf16 v[152:155], a[88:91], v[172:175], v[152:155]\n" \
  "v_fmac_f32 v136, v247, v200\n" \
  "v_mfma_f32_16x16x32_bf16 v[156:159], a[92:95], v[172:175], v[156:159]\n" \
  "v_mul_f32 v137, v137, v246\n" \
  "s_waitcnt lgkmcnt(3)\n" \
  "v_mfma_f32_16x16x32_bf16 v[144:147], a[96:99], v[176:179], v[144:147]\n" \
  "ds_read_b128 v[168:171], v210 offset:16896\n" \
  "v_fmac_f32 v137, v247, v201\n" \
  "v_mfma_f32_16x16x32_bf16 v[148:151], a[100:103], v[176:179], v[148:151]\n" \
  "v_mul_f32 v138, v138, v246\n" \
  "v_mfma_f32_16x16x32_bf16 v[152:155], a[104:107], v[176:179], v[152:155]\n" \
  "v_fmac_f32 v138, v247, v202\n" \
  "v_mfma_f32_16x16x32_bf16 v[156:159], a[108:111], v[176:179], v[156:159]\n" \
  "v_mul_f32 v139, v139, v246\n" \
  "s_waitcnt lgkmcnt(3)\n" \
  "v_mfma_f32_16x16x32_bf16 v[144:147], a[112:115], v[180:183], v[144:147]\n" \
  "ds_read_b128 v[172:175], v211 offset:16896\n" \
  "v_fmac_f32 v139, v247, v203\n" \
  "v_mfma_f32_16x16x32_bf16 v[148:151], a[116:119], v[180:183], v[148:151]\n" \
  "v_cvt_pk_bf16_f32 v232, v136, v137\n" \
  "v_cvt_pk_bf16_f32 v233, v138, v139\n" \
  "v_mfma_f32_16x16x32_bf16 v[152:155], a[120:123], v[180:183], v[152:155]\n" \
  "v_mul_f32 v140, v140, v246\n" \
  "v_mfma_f32_16x16x32_bf16 v[156:159], a[124:127], v[180:183], v[156:159]\n" \
  "v_fmac_f32 v140, v247, v204\n" \
  "s_waitcnt lgkmcnt(3)\n" \
  "v_mfma_f32_16x16x32_bf16 v[144:147], a[128:131], v[160:163], v[144:147]\n" \
  "ds_read_b128 v[176:179], v208 offset:17152\n" \
  "v_mul_f32 v141, v141, v246\n" \
  "v_mfma_f32_16x16x32_bf16 v[148:151], a[132:135], v[160:163], v[148:151]\n" \
  "v_fmac_f32 v141, v247, v205\n" \
  "v_mfma_f32_16x16x32_bf16 v[152:155], a[136:139], v[160:163], v[152:155]\n" \
  "v_mul_f32 v142, v142, v246\n" \
  "v_mfma_f32_16x16x32_bf16 v[156:159], a[140:143], v[160:163], v[156:159]\n" \
  "v_fmac_f32 v142, v247, v206\n" \
  "s_waitcnt lgkmcnt(3)\n" \
  "v_mfma_f32_16x16x32_bf16 v[144:147], a[144:147], v[164:167], v[144:147]\n" \
  "ds_read_b128 v[180:183], v209 offset:17152\n" \
  "v_mul_f32 v143, v143, v246\n" \
  "v_mfma_f32_16x16x32_bf16 v[148:151], a[148:151], v[164:167], v[148:151]\n" \
  "v_fmac_f32 v143, v247, v207\n" \
  "v_mfma_f32_16x16x32_bf16 v[152:155], a[152:155], v[164:167], v[152:155]\n" \
  "v_cvt_pk_bf16_f32 v234, v140, v141\n" \
  "v_cvt_pk_bf16_f32 v235, v142, v143\n" \
  "v_mfma_f32_16x16x32_bf16 v[156:159], a[156:159], v[164:167], v[156:159]\n" \
  "global_store_dwordx4 v250, v[232:235], s[8:9] offset:2048 sc1\n" \
  "s_waitcnt lgkmcnt(3)\n" \
  "v_mfma_f32_16x16x32_bf16 v[144:147], a[160:163], v[168:171], v[144:147]\n" \
  "ds_read_b128 v[160:163], v210 offset:17152\n" \
  "v_mfma_f32_16x16x32_bf16 v[148:151], a[164:167], v[168:171], v[148:151]\n" \
  "v_mfma_f32_16x16x32_bf16 v[152:155], a[168:171], v[168:171], v[152:155]\n" \
  "v_mfma_f32_16x16x32_bf16 v[156:159], a[172:175], v[168:171], v[156:159]\n" \
  "s_waitcnt lgkmcnt(3)\n" \
  "v_mfma_f32_16x16x32_bf16 v[144:147], a[176:179], v[172:175], v[144:147]\n" \
  "ds_read_b128 v[164:167], v211 offset:17152\n" \
  "v_mfma_f32_16x16x32_bf16 v[148:151], a[180:183], v[172:175], v[148:151]\n" \
  "v_mfma_f32_16x16x32_bf16 v[152:155], a[184:187], v[172:175], v[152:155]\n" \
  "v_mfma_f32_16x16x32_bf16 v[156:159], a[188:191], v[172:175], v[156:159]\n" \
  "s_waitcnt lgkmcnt(3)\n" \
  "v_mfma_f32_16x16x32_bf16 v[144:147], a[192:195], v[176:179], v[144:147]\n" \
  "v_mfma_f32_16x16x32_bf16 v[148:151], a[196:199], v[176:179], v[148:151]\n" \
  "v_mfma_f32_16x16x32_bf16 v[152:155], a[200:203], v[176:179], v[152:155]\n" \
  "v_mfma_f32_16x16x32_bf16 v[156:159], a[204:207], v[176:179], v[156:159]\n" \
  "s_waitcnt lgkmcnt(2)\n" \
  "v_mfma_f32_16x16x32_bf16 v[144:147], a[208:211], v[180:183], v[144:147]\n" \
  "v_mfma_f32_16x16x32_bf16 v[148:151], a[212:215], v[180:183], v[148:151]\n" \
  "v_mfma_f32_16x16x32_bf16 v[152:155], a[216:219], v[180:183], v[152:155]\n" \
  "v_mfma_f32_16x16x32_bf16 v[156:159], a[220:223], v[180:183], v[156:159]\n" \
  "s_waitcnt lgkmcnt(1)\n" \
  "v_mfma_f32_16x16x32_bf16 v[144:147], a[224:227], v[160:163], v[144:147]\n" \
  "v_mfma_f32_16x16x32_bf16 v[148:151], a[228:231], v[160:163], v[148:151]\n" \
  "v_mfma_f32_16x16x32_bf16 v[152:155], a[232:235], v[160:163], v[152:155]\n" \
  "v_mfma_f32_16x16x32_bf16 v[156:159], a[236:239], v[160:163], v[156:159]\n" \
  "s_waitcnt lgkmcnt(0)\n" \
  "v_mfma_f32_16x16x32_bf16 v[144:147], a[240:243], v[164:167], v[144:147]\n" \
  "v_mfma_f32_16x16x32_bf16 v[148:151], a[244:247], v[164:167], v[148:151]\n" \
  "v_mfma_f32_16x16x32_bf16 v[152:155], a[248:251], v[164:167], v[152:155]\n" \
  "v_mfma_f32_16x16x32_bf16 v[156:159], a[252:255], v[164:167], v[156:159]\n" \
  "v_mul_f32 v144, v144, v248\n" \
  "v_fmac_f32 v144, v249, v192\n" \
  "v_mul_f32 v145, v145, v248\n" \
  "v_fmac_f32 v145, v249, v193\n" \
  "v_mul_f32 v146, v146, v248\n" \
  "v_fmac_f32 v146, v249, v194\n" \
  "v_mul_f32 v147, v147, v248\n" \
  "v_fmac_f32 v147, v249, v195\n" \
  "v_cvt_pk_bf16_f32 v236, v144, v145\n" \
  "v_cvt_pk_bf16_f32 v237, v146, v147\n" \
  "v_mul_f32 v148, v148, v248\n" \
  "v_fmac_f32 v148, v249, v196\n" \
  "v_mul_f32 v149, v149, v248\n" \
  "v_fmac_f32 v149, v249, v197\n" \
  "v_mul_f32 v150, v150, v248\n" \
  "v_fmac_f32 v150, v249, v198\n" \
  "v_mul_f32 v151, v151, v248\n" \
  "v_fmac_f32 v151, v249, v199\n" \
  "v_cvt_pk_bf16_f32 v238, v148, v149\n" \
  "v_cvt_pk_bf16_f32 v239, v150, v151\n" \
  "global_store_dwordx4 v250, v[236:239], s[8:9] offset:256 sc1\n" \
  "v_mul_f32 v152, v152, v248\n" \
  "v_fmac_f32 v152, v249, v200\n" \
  "v_mul_f32 v153, v153, v248\n" \
  "v_fmac_f32 v153, v249, v201\n" \
  "v_mul_f32 v154, v154, v248\n" \
  "v_fmac_f32 v154, v249, v202\n" \
  "v_mul_f32 v155, v155, v248\n" \
  "v_fmac_f32 v155, v249, v203\n" \
  "v_cvt_pk_bf16_f32 v232, v152, v153\n" \
  "v_cvt_pk_bf16_f32 v233, v154, v155\n" \
  "v_mul_f32 v156, v156, v248\n" \
  "v_fmac_f32 v156, v249, v204\n" \
  "v_mul_f32 v157, v157, v248\n" \
  "v_fmac_f32 v157, v249, v205\n" \
  "v_mul_f32 v158, v158, v248\n" \
  "v_fmac_f32 v158, v249, v206\n" \
  "v_mul_f32 v159, v159, v248\n" \
  "v_fmac_f32 v159, v249, v207\n" \
  "v_cvt_pk_bf16_f32 v234, v156, v157\n" \
  "v_cvt_pk_bf16_f32 v235, v158, v159\n" \
  "global_store_dwordx4 v250, v[232:235], s[8:9] offset:2304 sc1\n" \
  "s_waitcnt vmcnt(0)\n" \
  "s_barrier\n" \
  "s_add_u32 s42, s52, 1\n" \
  "s_add_u32 s48, s18, 28672\n" \
  "s_addc_u32 s49, s19, 0\n" \
  "v_mov_b32 v226, s42\n" \
  "s_mov_b64 exec, s[60:61]\n" \
  "global_store_dword v255, v226, s[48:49] sc1\n" \
  "s_mov_b64 exec, -1\n" \
  "s_add_u32 s52, s52, 3\n" \
  "s_cmp_lt_u32 s52, 12\n" \
  "s_cbranch_scc1 Lqah_%=\n" \
  "s_lshl_b32 s42, s30, 13\n" \
  "s_sub_u32 s42, 32768, s42\n" \
  "s_add_u32 s48, s18, s42\n" \
  "s_addc_u32 s49, s19, 0\n" \
  "s_sub_u32 s53, 12, s30\n" \
  "s_cmp_eq_u32 s40, 0\n" \
  "s_cbranch_scc0 Lqaw3_%=\n" \
  "Lqas4_%=:\n" \
  "global_load_dword v226, v255, s[48:49] sc1\n" \
  "s_waitcnt vmcnt(0)\n" \
  "v_cmp_gt_u32 vcc, s53, v226\n" \
  "s_nop 0\n" \
  "s_cbranch_vccz Lqas4b_%=\n" \
  "s_sleep 4\n" \
  "s_sub_u32 s41, s41, 1\n" \
  "s_cmp_gt_i32 s41, 0\n" \
  "s_cbranch_scc1 Lqas4_%=\n" \
  "Lqas4b_%=:\n" \
  "Lqaw3_%=:\n" \
  "s_barrier\n" \
  "s_lshl_b32 s42, s40, 14\n" \
  "s_add_u32 s10, s10, s42\n" \
  "s_addc_u32 s11, s11, 0\n" \
  "s_lshl_b32 s42, s30, 16\n" \
  "s_add_u32 s10, s10, s42\n" \
  "s_addc_u32 s11, s11, 0\n" \
  "s_lshl_b32 s42, s30, 9\n" \
  "s_add_u32 s12, s12, s42\n" \
  "s_addc_u32 s13, s13, 0\n" \
  "s_lshl_b32 s42, s30, 15\n" \
  "s_add_u32 s14, s14, s42\n" \
  "s_addc_u32 s15, s15, 0\n" \
  "s_lshl_b32 s42, s30, 19\n" \
  "s_add_u32 s20, s20, s42\n" \
  "s_addc_u32 s21, s21, 0\n" \
  "s_lshr_b32 s42, s40, 1\n" \
  "s_lshl_b32 s42, s42, 18\n" \
  "s_and_b32 s43, s40, 1\n" \
  "s_lshl_b32 s43, s43, 12\n" \
  "s_add_u32 s42, s42, s43\n" \
  "s_add_u32 s20, s20, s42\n" \
  "s_addc_u32 s21, s21, 0\n" \
  "s_mov_b32 s52, s30\n" \
  "s_lshl_b32 s42, s40, 12\n" \
  "s_add_u32 s14, s14, s42\n" \
  "s_addc_u32 s15, s15, 0\n" \
  "s_add_u32 s53, s52, 1\n" \
  "s_lshl_b32 s53, s53, 1\n" \
  "s_cmp_eq_u32 s40, 0\n" \
  "s_cbranch_scc0 Lqaw5_%=\n" \
  "Lqas6_%=:\n" \
  "global_load_dword v226, v255, s[16:17] sc1\n" \
  "s_waitcnt vmcnt(0)\n" \
  "v_cmp_gt_u32 vcc, s53, v226\n" \
  "s_nop 0\n" \
  "s_cbranch_vccz Lqas6b_%=\n" \
  "s_sleep 8\n" \
  "s_sub_u32 s41, s41, 1\n" \
  "s_cmp_gt_i32 s41, 0\n" \
  "s_cbranch_scc1 Lqas6_%=\n" \
  "Lqas6b_%=:\n" \
  "Lqaw5_%=:\n" \
  "s_barrier\n" \
  "s_lshl_b32 s47, s40, 14\n" \
  "s_add_u32 m0, s47, 0\n" \
  "v_xor_b32 v227, 0, v212\n" \
  "global_load_lds_dwordx4 v227, s[10:11] sc1\n" \
  "v_xor_b32 v227, 16, v212\n" \
  "global_load_lds_dwordx4 v227, s[10:11] offset:1024 sc1\n" \
  "v_xor_b32 v227, 32, v212\n" \
  "global_load_lds_dwordx4 v227, s[10:11] offset:2048 sc1\n" \
  "v_xor_b32 v227, 48, v212\n" \
  "global_load_lds_dwordx4 v227, s[10:11] offset:3072 sc1\n" \
  "s_add_u32 s10, s10, 4096\n" \
  "s_addc_u32 s11, s11, 0\n" \
  "s_lshl_b32 s47, s40, 14\n" \
  "s_add_u32 m0, s47, 4096\n" \
  "v_xor_b32 v227, 64, v212\n" \
  "global_load_lds_dwordx4 v227, s[10:11] sc1\n" \
  "v_xor_b32 v227, 80, v212\n" \
  "global_load_lds_dwordx4 v227, s[10:11] offset:1024 sc1\n" \
  "v_xor_b32 v227, 96, v212\n" \
  "global_load_lds_dwordx4 v227, s[10:11] offset:2048 sc1\n" \
  "v_xor_b32 v227, 112, v212\n" \
  "global_load_lds_dwordx4 v227, s[10:11] offset:3072 sc1\n" \
  "s_add_u32 s10, s10, 4096\n" \
  "s_addc_u32 s11, s11, 0\n" \
  "s_lshl_b32 s47, s40, 14\n" \
  "s_add_u32 m0, s47, 8192\n" \
  "v_xor_b32 v227, 128, v212\n" \
  "global_load_lds_dwordx4 v227, s[10:11] sc1\n" \
  "v_xor_b32 v227, 144, v212\n" \
  "global_load_lds_dwordx4 v227, s[10:11] offset:1024 sc1\n" \
  "v_xor_b32 v227, 160, v212\n" \
  "global_load_lds_dwordx4 v227, s[10:11] offset:2048 sc1\n" \
  "v_xor_b32 v227, 176, v212\n" \
  "global_load_lds_dwordx4 v227, s[10:11] offset:3072 sc1\n" \
  "s_add_u32 s10, s10, 4096\n" \
  "s_addc_u32 s11, s11, 0\n" \
  "s_lshl_b32 s47, s40, 14\n" \
  "s_add_u32 m0, s47, 12288\n" \
  "v_xor_b32 v227, 192, v212\n" \
  "global_load_lds_dwordx4 v227, s[10:11] sc1\n" \
  "v_xor_b32 v227, 208, v212\n" \
  "global_load_lds_dwordx4 v227, s[10:11] offset:1024 sc1\n" \
  "v_xor_b32 v227, 224, v212\n" \
  "global_load_lds_dwordx4 v227, s[10:11] offset:2048 sc1\n" \
  "v_xor_b32 v227, 240, v212\n" \
  "global_load_lds_dwordx4 v227, s[10:11] offset:3072 sc1\n" \
  "s_add_u32 s10, s10, 118784\n" \
  "s_addc_u32 s11, s11, 0\n" \
  "global_load_dwordx2 v[192:193], v213, s[12:13] sc1\n" \
  "global_load_dwordx2 v[194:195], v213, s[12:13] offset:128 sc1\n" \
  "global_load_dwordx2 v[196:197], v213, s[12:13] offset:256 sc1\n" \
  "global_load_dwordx2 v[198:199], v213, s[12:13] offset:384 sc1\n" \
  "s_add_u32 s12, s12, 1024\n" \
  "s_addc_u32 s13, s13, 0\n" \
  "s_lshl_b32 s55, s52, 1\n" \
  "s_max_u32 s42, s55, 1\n" \
  "s_lshl_b32 s42, s42, 2\n" \
  "s_add_u32 s48, s22, s42\n" \
  "s_addc_u32 s49, s23, 0\n" \
  "global_load_dword v250, v255, s[16:17] sc1\n" \
  "global_load_dword v251, v255, s[48:49] sc1\n" \
  "s_waitcnt vmcnt(0)\n" \
  "s_barrier\n" \
  "Lqatop_%=:\n" \
  "ds_read_b128 v[160:163], v208\n" \
  "ds_read_b128 v[164:167], v209\n" \
  "ds_read_b128 v[168:171], v210\n" \
  "ds_read_b128 v[172:175], v211\n" \
  "s_waitcnt lgkmcnt(3)\n" \
  "v_mfma_f32_16x16x32_bf16 v[128:131], v[0:3], v[160:163], 0\n" \
  "ds_read_b128 v[176:179], v208 offset:256\n" \
  "v_mfma_f32_16x16x32_bf16 v[132:135], v[4:7], v[160:163], 0\n" \
  "s_waitcnt lgkmcnt(3)\n" \
  "v_mfma_f32_16x16x32_bf16 v[128:131], v[8:11], v[164:167], v[128:131]\n" \
  "ds_read_b128 v[180:183], v209 offset:256\n" \
  "v_mfma_f32_16x16x32_bf16 v[132:135], v[12:15], v[164:167], v[132:135]\n" \
  "s_waitcnt lgkmcnt(3)\n" \
  "v_mfma_f32_16x16x32_bf16 v[128:131], v[16:19], v[168:171], v[128:131]\n" \
  "ds_read_b128 v[160:163], v210 offset:256\n" \
  "v_mfma_f32_16x16x32_bf16 v[132:135], v[20:23], v[168:171], v[132:135]\n" \
  "s_waitcnt lgkmcnt(3)\n" \
  "v_mfma_f32_16x16x32_bf16 v[128:131], v[24:27], v[172:175], v[128:131]\n" \
  "ds_read_b128 v[164:167], v211 offset:256\n" \
  "v_mfma_f32_16x16x32_bf16 v[132:135], v[28:31], v[172:175], v[132:135]\n" \
  "s_waitcnt lgkmcnt(3)\n" \
  "v_mfma_f32_16x16x32_bf16 v[128:131], v[32:35], v[176:179], v[128:131]\n" \
  "ds_read_b128 v[168:171], v208 offset:512\n" \
  "v_mfma_f32_16x16x32_bf16 v[132:135], v[36:39], v[176:179], v[132:135]\n" \
  "s_waitcnt lgkmcnt(3)\n" \
  "v_mfma_f32_16x16x32_bf16 v[128:131], v[40:43], v[180:183], v[128:131]\n" \
  "ds_read_b128 v[172:175], v209 offset:512\n" \
  "v_mfma_f32_16x16x32_bf16 v[132:135], v[44:47], v[180:183], v[132:135]\n" \
  "s_waitcnt lgkmcnt(3)\n" \
  "v_mfma_f32_16x16x32_bf16 v[128:131], v[48:51], v[160:163], v[128:131]\n" \
  "ds_read_b128 v[176:179], v210 offset:512\n" \
  "v_mfma_f32_16x16x32_bf16 v[132:135], v[52:55], v[160:163], v[132:135]\n" \
  "s_waitcnt lgkmcnt(3)\n" \
  "v_mfma_f32_16x16x32_bf16 v[128:131], v[56:59], v[164:167], v[128:131]\n" \
  "ds_read_b128 v[180:183], v211 offset:512\n" \
  "v_mfma_f32_16x16x32_bf16 v[132:135], v[60:63], v[164:167], v[132:135]\n" \
  "s_waitcnt lgkmcnt(3)\n" \
  "v_mfma_f32_16x16x32_bf16 v[128:131], v[64:67], v[168:171], v[128:131]\n" \
  "ds_read_b128 v[160:163], v208 offset:768\n" \
  "v_mfma_f32_16x16x32_bf16 v[132:135], v[68:71], v[168:171], v[132:135]\n" \
  "s_waitcnt lgkmcnt(3)\n" \
  "v_mfma_f32_16x16x32_bf16 v[128:131], v[72:75], v[172:175], v[128:131]\n" \
  "ds_read_b128 v[164:167], v209 offset:768\n" \
  "v_mfma_f32_16x16x32_bf16 v[132:135], v[76:79], v[172:175], v[132:135]\n" \
  "s_waitcnt lgkmcnt(3)\n" \
  "v_mfma_f32_16x16x32_bf16 v[128:131], v[80:83], v[176:179], v[128:131]\n" \
  "ds_read_b128 v[168:171], v210 offset:768\n" \
  "v_mfma_f32_16x16x32_bf16 v[132:135], v[84:87], v[176:179], v[132:135]\n" \
  "s_waitcnt lgkmcnt(3)\n" \
  "v_mfma_f32_16x16x32_bf16 v[128:131], v[88:91], v[180:183], v[128:131]\n" \
  "ds_read_b128 v[172:175], v211 offset:768\n" \
  "v_mfma_f32_16x16x32_bf16 v[132:135], v[92:95], v[180:183], v[132:135]\n" \
  "s_waitcnt lgkmcnt(3)\n" \
  "v_mfma_f32_16x16x32_bf16 v[128:131], v[96:99], v[160:163], v[128:131]\n" \
  "ds_read_b128 v[176:179], v208 offset:16384\n" \
  "v_mfma_f32_16x16x32_bf16 v[132:135], v[100:103], v[160:163], v[132:135]\n" \
  "s_waitcnt lgkmcnt(3)\n" \
  "v_mfma_f32_16x16x32_bf16 v[128:131], v[104:107], v[164:167], v[128:131]\n" \
  "ds_read_b128 v[180:183], v209 offset:16384\n" \
  "v_mfma_f32_16x16x32_bf16 v[132:135], v[108:111], v[164:167], v[132:135]\n" \
  "s_waitcnt lgkmcnt(3)\n" \
  "v_mfma_f32_16x16x32_bf16 v[128:131], v[112:115], v[168:171], v[128:131]\n" \
  "ds_read_b128 v[160:163], v210 offset:16384\n" \
  "v_mfma_f32_16x16x32_bf16 v[132:135], v[116:119], v[168:171], v[132:135]\n" \
  "s_waitcnt lgkmcnt(3)\n" \
  "v_mfma_f32_16x16x32_bf16 v[128:131], v[120:123], v[172:175], v[128:131]\n" \
  "ds_read_b128 v[164:167], v211 offset:16384\n" \
  "v_mfma_f32_16x16x32_bf16 v[132:135], v[124:127], v[172:175], v[132:135]\n" \
  "s_waitcnt vmcnt(0)\n" \
  "s_add_u32 s53, s52, 3\n" \
  "s_min_u32 s53, s53, 32\n" \
  "s_lshl_b32 s53, s53, 1\n" \
  "v_cmp_gt_u32 vcc, s53, v250\n" \
  "s_nop 0\n" \
  "s_cbranch_vccz Lqakvok_%=\n" \
  "Lqas7_%=:\n" \
  "global_load_dword v226, v255, s[16:17] sc1\n" \
  "s_waitcnt vmcnt(0)\n" \
  "v_cmp_gt_u32 vcc, s53, v226\n" \
  "s_nop 0\n" \
  "s_cbranch_vccz Lqas7b_%=\n" \
  "s_sleep 2\n" \
  "s_sub_u32 s41, s41, 1\n" \
  "s_cmp_gt_i32 s41, 0\n" \
  "s_cbranch_scc1 Lqas7_%=\n" \
  "Lqas7b_%=:\n" \
  "Lqakvok_%=:\n" \
  "s_memrealtime s[50:51]\n" \
  "v_readfirstlane_b32 s58, v251\n" \
  "s_nop 0\n" \
  "s_cmp_eq_u32 s58, -1\n" \
  "s_cbranch_scc1 Lqalate_%=\n" \
  "s_waitcnt lgkmcnt(0)\n" \
  "s_sub_u32 s42, s50, s58\n" \
  "s_cmp_lt_i32 s42, 0\n" \
  "s_cselect_b32 s58, -1, s58\n" \
  "s_cbranch_scc1 Lqalate_%=\n" \
  "s_sub_u32 s42, s55, 1\n" \
  "s_and_b32 s43, s42, 31\n" \
  "s_lshl_b32 s44, s43, 16\n" \
  "s_bfe_u32 s45, s43, 65538\n" \
  "s_lshl_b32 s45, s45, 11\n" \
  "s_or_b32 s44, s44, s45\n" \
  "s_bfe_u32 s45, s43, 65539\n" \
  "s_lshl_b32 s45, s45, 14\n" \
  "s_or_b32 s44, s44, s45\n" \
  "s_cmp_lt_i32 s42, 32\n" \
  "s_cselect_b64 s[48:49], s[24:25], s[26:27]\n" \
  "s_xor_b32 s48, s48, s44\n" \
  "s_sub_u32 s42, s55, 0\n" \
  "s_and_b32 s43, s42, 31\n" \
  "s_lshl_b32 s44, s43, 16\n" \
  "s_bfe_u32 s45, s43, 65538\n" \
  "s_lshl_b32 s45, s45, 11\n" \
  "s_or_b32 s44, s44, s45\n" \
  "s_bfe_u32 s45, s43, 65539\n" \
  "s_lshl_b32 s45, s45, 14\n" \
  "s_or_b32 s44, s44, s45\n" \
  "s_cmp_lt_i32 s42, 32\n" \
  "s_cselect_b64 s[50:51], s[24:25], s[26:27]\n" \
  "s_xor_b32 s50, s50, s44\n" \
  "s_lshl_b32 s42, s40, 12\n" \
  "s_add_u32 s42, s42, 98304\n" \
  "s_mov_b32 exec_lo, 0xff00ff00\n" \
  "s_mov_b32 exec_hi, 0xff00ff00\n" \
  "s_add_u32 m0, s42, 0\n" \
  "s_nop 0\n" \
  "global_load_lds_dwordx4 v218, s[50:51] sc1\n" \
  "s_add_u32 m0, s42, 512\n" \
  "s_nop 0\n" \
  "global_load_lds_dwordx4 v218, s[50:51] offset:512 sc1\n" \
  "s_add_u32 m0, s42, 1024\n" \
  "s_nop 0\n" \
  "global_load_lds_dwordx4 v218, s[50:51] offset:1024 sc1\n" \
  "s_add_u32 m0, s42, 1536\n" \
  "s_nop 0\n" \
  "global_load_lds_dwordx4 v218, s[50:51] offset:1536 sc1\n" \
  "s_mov_b32 exec_lo, 0x00ff00ff\n" \
  "s_mov_b32 exec_hi, 0x00ff00ff\n" \
  "s_cmp_eq_u32 s52, 0\n" \
  "s_cbranch_scc1 Lqam08_%=\n" \
  "s_add_u32 m0, s42, 0\n" \
  "s_nop 0\n" \
  "global_load_lds_dwordx4 v218, s[48:49] sc1\n" \
  "s_add_u32 m0, s42, 512\n" \
  "s_nop 0\n" \
  "global_load_lds_dwordx4 v218, s[48:49] offset:512 sc1\n" \
  "s_add_u32 m0, s42, 1024\n" \
  "s_nop 0\n" \
  "global_load_lds_dwordx4 v218, s[48:49] offset:1024 sc1\n" \
  "s_add_u32 m0, s42, 1536\n" \
  "s_nop 0\n" \
  "global_load_lds_dwordx4 v218, s[48:49] offset:1536 sc1\n" \
  "s_branch Lqam18_%=\n" \
  "Lqam08_%=:\n" \
  "s_add_u32 m0, s42, 0\n" \
  "s_nop 0\n" \
  "global_load_lds_dwordx4 v219, s[28:29]\n" \
  "s_add_u32 m0, s42, 960\n" \
  "s_nop 0\n" \
  "global_load_lds_dwordx4 v219, s[28:29] offset:64\n" \
  "s_add_u32 m0, s42, 1920\n" \
  "s_nop 0\n" \
  "global_load_lds_dwordx4 v219, s[28:29] offset:128\n" \
  "s_add_u32 m0, s42, 2880\n" \
  "s_nop 0\n" \
  "global_load_lds_dwordx4 v219, s[28:29] offset:192\n" \
  "Lqam18_%=:\n" \
  "s_mov_b64 exec, -1\n" \
  "Lqalate_%=:\n" \
  "s_waitcnt lgkmcnt(0)\n" \
  "s_waitcnt lgkmcnt(3)\n" \
  "v_mfma_f32_16x16x32_bf16 v[136:139], v[0:3], v[176:179], 0\n" \
  "ds_read_b128 v[168:171], v208 offset:16640\n" \
  "v_mfma_f32_16x16x32_bf16 v[140:143], v[4:7], v[176:179], 0\n" \
  "s_waitcnt lgkmcnt(3)\n" \
  "v_mfma_f32_16x16x32_bf16 v[136:139], v[8:11], v[180:183], v[136:139]\n" \
  "ds_read_b128 v[172:175], v209 offset:16640\n" \
  "v_mul_f32 v128, v128, v192\n" \
  "v_mfma_f32_16x16x32_bf16 v[140:143], v[12:15], v[180:183], v[140:143]\n" \
  "v_fmac_f32 v128, v193, v184\n" \
  "v_mul_f32 v129, v129, v192\n" \
  "s_waitcnt lgkmcnt(3)\n" \
  "v_mfma_f32_16x16x32_bf16 v[136:139], v[16:19], v[160:163], v[136:139]\n" \
  "ds_read_b128 v[176:179], v210 offset:16640\n" \
  "s_lshl_b32 s42, s40, 12\n" \
  "s_add_u32 m0, s42, 65536\n" \
  "s_nop 0\n" \
  "global_load_lds_dwordx4 v216, s[14:15] sc1\n" \
  "v_mfma_f32_16x16x32_bf16 v[140:143], v[20:23], v[160:163], v[140:143]\n" \
  "v_fmac_f32 v129, v193, v185\n" \
  "v_mul_f32 v130, v130, v192\n" \
  "s_waitcnt lgkmcnt(3)\n" \
  "v_mfma_f32_16x16x32_bf16 v[136:139], v[24:27], v[164:167], v[136:139]\n" \
  "ds_read_b128 v[180:183], v211 offset:16640\n" \
  "v_fmac_f32 v130, v193, v186\n" \
  "v_mfma_f32_16x16x32_bf16 v[140:143], v[28:31], v[164:167], v[140:143]\n" \
  "v_mul_f32 v131, v131, v192\n" \
  "v_fmac_f32 v131, v193, v187\n" \
  "s_waitcnt lgkmcnt(3)\n" \
  "v_mfma_f32_16x16x32_bf16 v[136:139], v[32:35], v[168:171], v[136:139]\n" \
  "ds_read_b128 v[160:163], v208 offset:16896\n" \
  "v_cvt_pk_bf16_f32 v236, v128, v129\n" \
  "v_mfma_f32_16x16x32_bf16 v[140:143], v[36:39], v[168:171], v[140:143]\n" \
  "v_cvt_pk_bf16_f32 v237, v130, v131\n" \
  "v_mul_f32 v132, v132, v192\n" \
  "s_waitcnt lgkmcnt(3)\n" \
  "v_mfma_f32_16x16x32_bf16 v[136:139], v[40:43], v[172:175], v[136:139]\n" \
  "ds_read_b128 v[164:167], v209 offset:16896\n" \
  "v_fmac_f32 v132, v193, v188\n" \
  "v_mfma_f32_16x16x32_bf16 v[140:143], v[44:47], v[172:175], v[140:143]\n" \
  "v_mul_f32 v133, v133, v192\n" \
  "v_fmac_f32 v133, v193, v189\n" \
  "s_waitcnt lgkmcnt(3)\n" \
  "v_mfma_f32_16x16x32_bf16 v[136:139], v[48:51], v[176:179], v[136:139]\n" \
  "ds_read_b128 v[168:171], v210 offset:16896\n" \
  "v_mul_f32 v134, v134, v192\n" \
  "v_mfma_f32_16x16x32_bf16 v[140:143], v[52:55], v[176:179], v[140:143]\n" \
  "global_load_lds_dwordx4 v216, s[14:15] offset:1024 sc1\n" \
  "v_fmac_f32 v134, v193, v190\n" \
  "s_waitcnt lgkmcnt(3)\n" \
  "v_mfma_f32_16x16x32_bf16 v[136:139], v[56:59], v[180:183], v[136:139]\n" \
  "ds_read_b128 v[172:175], v211 offset:16896\n" \
  "v_mul_f32 v135, v135, v192\n" \
  "v_mfma_f32_16x16x32_bf16 v[140:143], v[60:63], v[180:183], v[140:143]\n" \
  "v_fmac_f32 v135, v193, v191\n" \
  "v_cvt_pk_bf16_f32 v238, v132, v133\n" \
  "s_waitcnt lgkmcnt(3)\n" \
  "v_mfma_f32_16x16x32_bf16 v[136:139], v[64:67], v[160:163], v[136:139]\n" \
  "ds_read_b128 v[176:179], v208 offset:17152\n" \
  "v_cvt_pk_bf16_f32 v239, v134, v135\n" \
  "v_mfma_f32_16x16x32_bf16 v[140:143], v[68:71], v[160:163], v[140:143]\n" \
  "v_mov_b32 v128, v236\n" \
  "v_mov_b32 v129, v237\n" \
  "s_waitcnt lgkmcnt(3)\n" \
  "v_mfma_f32_16x16x32_bf16 v[136:139], v[72:75], v[164:167], v[136:139]\n" \
  "ds_read_b128 v[180:183], v209 offset:17152\n" \
  "v_mov_b32 v130, v238\n" \
  "v_mfma_f32_16x16x32_bf16 v[140:143], v[76:79], v[164:167], v[140:143]\n" \
  "v_mov_b32 v131, v239\n" \
  "global_load_lds_dwordx4 v216, s[14:15] offset:2048 sc1\n" \
  "s_waitcnt lgkmcnt(3)\n" \
  "v_mfma_f32_16x16x32_bf16 v[136:139], v[80:83], v[168:171], v[136:139]\n" \
  "ds_read_b128 v[160:163], v210 offset:17152\n" \
  "v_mfma_f32_16x16x32_bf16 v[140:143], v[84:87], v[168:171], v[140:143]\n" \
  "s_waitcnt lgkmcnt(3)\n" \
  "v_mfma_f32_16x16x32_bf16 v[136:139], v[88:91], v[172:175], v[136:139]\n" \
  "ds_read_b128 v[164:167], v211 offset:17152\n" \
  "v_mfma_f32_16x16x32_bf16 v[140:143], v[92:95], v[172:175], v[140:143]\n" \
  "s_waitcnt lgkmcnt(3)\n" \
  "v_mfma_f32_16x16x32_bf16 v[136:139], v[96:99], v[176:179], v[136:139]\n" \
  "ds_read_b128 v[168:171], v208 offset:32768\n" \
  "v_mfma_f32_16x16x32_bf16 v[140:143], v[100:103], v[176:179], v[140:143]\n" \
  "s_waitcnt lgkmcnt(3)\n" \
  "v_mfma_f32_16x16x32_bf16 v[136:139], v[104:107], v[180:183], v[136:139]\n" \
  "ds_read_b128 v[172:175], v209 offset:32768\n" \
  "v_mfma_f32_16x16x32_bf16 v[140:143], v[108:111], v[180:183], v[140:143]\n" \
  "s_waitcnt lgkmcnt(3)\n" \
  "v_mfma_f32_16x16x32_bf16 v[136:139], v[112:115], v[160:163], v[136:139]\n" \
  "ds_read_b128 v[176:179], v210 offset:32768\n" \
  "v_mfma_f32_16x16x32_bf16 v[140:143], v[116:119], v[160:163], v[140:143]\n" \
  "s_waitcnt lgkmcnt(3)\n" \
  "v_mfma_f32_16x16x32_bf16 v[136:139], v[120:123], v[164:167], v[136:139]\n" \
  "ds_read_b128 v[180:183], v211 offset:32768\n" \
  "v_mfma_f32_16x16x32_bf16 v[140:143], v[124:127], v[164:167], v[140:143]\n" \
  "s_waitcnt lgkmcnt(3)\n" \
  "v_mfma_f32_16x16x32_bf16 v[144:147], v[0:3], v[168:171], 0\n" \
  "ds_read_b128 v[160:163], v208 offset:33024\n" \
  "v_mfma_f32_16x16x32_bf16 v[148:151], v[4:7], v[168:171], 0\n" \
  "s_waitcnt lgkmcnt(3)\n" \
  "v_mfma_f32_16x16x32_bf16 v[144:147], v[8:11], v[172:175], v[144:147]\n" \
  "ds_read_b128 v[164:167], v209 offset:33024\n" \
  "v_mul_f32 v136, v136, v194\n" \
  "v_mfma_f32_16x16x32_bf16 v[148:151], v[12:15], v[172:175], v[148:151]\n" \
  "v_fmac_f32 v136, v195, v184\n" \
  "v_mul_f32 v137, v137, v194\n" \
  "s_waitcnt lgkmcnt(3)\n" \
  "v_mfma_f32_16x16x32_bf16 v[144:147], v[16:19], v[176:179], v[144:147]\n" \
  "ds_read_b128 v[168:171], v210 offset:33024\n" \
  "global_load_lds_dwordx4 v216, s[14:15] offset:3072 sc1\n" \
  "s_add_u32 s14, s14, 16384\n" \
  "s_addc_u32 s15, s15, 0\n" \
  "v_mfma_f32_16x16x32_bf16 v[148:151], v[20:23], v[176:179], v[148:151]\n" \
  "v_fmac_f32 v137, v195, v185\n" \
  "v_mul_f32 v138, v138, v194\n" \
  "s_waitcnt lgkmcnt(3)\n" \
  "v_mfma_f32_16x16x32_bf16 v[144:147], v[24:27], v[180:183], v[144:147]\n" \
  "ds_read_b128 v[172:175], v211 offset:33024\n" \
  "v_fmac_f32 v138, v195, v186\n" \
  "v_mfma_f32_16x16x32_bf16 v[148:151], v[28:31], v[180:183], v[148:151]\n" \
  "v_mul_f32 v139, v139, v194\n" \
  "v_fmac_f32 v139, v195, v187\n" \
  "s_waitcnt lgkmcnt(3)\n" \
  "v_mfma_f32_16x16x32_bf16 v[144:147], v[32:35], v[160:163], v[144:147]\n" \
  "ds_read_b128 v[176:179], v208 offset:33280\n" \
  "v_cvt_pk_bf16_f32 v232, v136, v137\n" \
  "v_mfma_f32_16x16x32_bf16 v[148:151], v[36:39], v[160:163], v[148:151]\n" \
  "v_cvt_pk_bf16_f32 v233, v138, v139\n" \
  "v_mul_f32 v140, v140, v194\n" \
  "s_waitcnt lgkmcnt(3)\n" \
  "v_mfma_f32_16x16x32_bf16 v[144:147], v[40:43], v[164:167], v[144:147]\n" \
  "ds_read_b128 v[180:183], v209 offset:33280\n" \
  "v_fmac_f32 v140, v195, v188\n" \
  "v_mfma_f32_16x16x32_bf16 v[148:151], v[44:47], v[164:167], v[148:151]\n" \
  "v_mul_f32 v141, v141, v194\n" \
  "v_fmac_f32 v141, v195, v189\n" \
  "s_waitcnt lgkmcnt(3)\n" \
  "v_mfma_f32_16x16x32_bf16 v[144:147], v[48:51], v[168:171], v[144:147]\n" \
  "ds_read_b128 v[160:163], v210 offset:33280\n" \
  "v_mul_f32 v142, v142, v194\n" \
  "v_mfma_f32_16x16x32_bf16 v[148:151], v[52:55], v[168:171], v[148:151]\n" \
  "s_lshl_b32 s42, s40, 12\n" \
  "s_add_u32 m0, s42, 81920\n" \
  "s_nop 0\n" \
  "global_load_lds_dwordx4 v216, s[14:15] sc1\n" \
  "v_fmac_f32 v142, v195, v190\n" \
  "s_waitcnt lgkmcnt(3)\n" \
  "v_mfma_f32_16x16x32_bf16 v[144:147], v[56:59], v[172:175], v[144:147]\n" \
  "ds_read_b128 v[164:167], v211 offset:33280\n" \
  "v_mul_f32 v143, v143, v194\n" \
  "v_mfma_f32_16x16x32_bf16 v[148:151], v[60:63], v[172:175], v[148:151]\n" \
  "v_fmac_f32 v143, v195, v191\n" \
  "v_cvt_pk_bf16_f32 v234, v140, v141\n" \
  "s_waitcnt lgkmcnt(3)\n" \
  "v_mfma_f32_16x16x32_bf16 v[144:147], v[64:67], v[176:179], v[144:147]\n" \
  "ds_read_b128 v[168:171], v208 offset:33536\n" \
  "v_cvt_pk_bf16_f32 v235, v142, v143\n" \
  "v_mfma_f32_16x16x32_bf16 v[148:151], v[68:71], v[176:179], v[148:151]\n" \
  "v_mov_b32 v136, v232\n" \
  "v_mov_b32 v137, v233\n" \
  "s_waitcnt lgkmcnt(3)\n" \
  "v_mfma_f32_16x16x32_bf16 v[144:147], v[72:75], v[180:183], v[144:147]\n" \
  "ds_read_b128 v[172:175], v209 offset:33536\n" \
  "v_mov_b32 v138, v234\n" \
  "v_mfma_f32_16x16x32_bf16 v[148:151], v[76:79], v[180:183], v[148:151]\n" \
  "v_mov_b32 v139, v235\n" \
  "global_load_lds_dwordx4 v216, s[14:15] offset:1024 sc1\n" \
  "s_waitcnt lgkmcnt(3)\n" \
  "v_mfma_f32_16x16x32_bf16 v[144:147], v[80:83], v[160:163], v[144:147]\n" \
  "ds_read_b128 v[176:179], v210 offset:33536\n" \
  "v_mfma_f32_16x16x32_bf16 v[148:151], v[84:87], v[160:163], v[148:151]\n" \
  "s_waitcnt lgkmcnt(3)\n" \
  "v_mfma_f32_16x16x32_bf16 v[144:147], v[88:91], v[164:167], v[144:147]\n" \
  "ds_read_b128 v[180:183], v211 offset:33536\n" \
  "v_mfma_f32_16x16x32_bf16 v[148:151], v[92:95], v[164:167], v[148:151]\n" \
  "s_waitcnt lgkmcnt(3)\n" \
  "v_mfma_f32_16x16x32_bf16 v[144:147], v[96:99], v[168:171], v[144:147]\n" \
  "ds_read_b128 v[160:163], v208 offset:49152\n" \
  "v_mfma_f32_16x16x32_bf16 v[148:151], v[100:103], v[168:171], v[148:151]\n" \
  "s_waitcnt lgkmcnt(3)\n" \
  "v_mfma_f32_16x16x32_bf16 v[144:147], v[104:107], v[172:175], v[144:147]\n" \
  "ds_read_b128 v[164:167], v209 offset:49152\n" \
  "v_mfma_f32_16x16x32_bf16 v[148:151], v[108:111], v[172:175], v[148:151]\n" \
  "s_waitcnt lgkmcnt(3)\n" \
  "v_mfma_f32_16x16x32_bf16 v[144:147], v[112:115], v[176:179], v[144:147]\n" \
  "ds_read_b128 v[168:171], v210 offset:49152\n" \
  "v_mfma_f32_16x16x32_bf16 v[148:151], v[116:119], v[176:179], v[148:151]\n" \
  "s_waitcnt lgkmcnt(3)\n" \
  "v_mfma_f32_16x16x32_bf16 v[144:147], v[120:123], v[180:183], v[144:147]\n" \
  "ds_read_b128 v[172:175], v211 offset:49152\n" \
  "v_mfma_f32_16x16x32_bf16 v[148:151], v[124:127], v[180:183], v[148:151]\n" \
  "s_waitcnt lgkmcnt(3)\n" \
  "v_mfma_f32_16x16x32_bf16 v[152:155], v[0:3], v[160:163], 0\n" \
  "ds_read_b128 v[176:179], v208 offset:49408\n" \
  "v_mfma_f32_16x16x32_bf16 v[156:159], v[4:7], v[160:163], 0\n" \
  "s_waitcnt lgkmcnt(3)\n" \
  "v_mfma_f32_16x16x32_bf16 v[152:155], v[8:11], v[164:167], v[152:155]\n" \
  "ds_read_b128 v[180:183], v209 offset:49408\n" \
  "v_mul_f32 v144, v144, v196\n" \
  "v_mfma_f32_16x16x32_bf16 v[156:159], v[12:15], v[164:167], v[156:159]\n" \
  "v_fmac_f32 v144, v197, v184\n" \
  "v_mul_f32 v145, v145, v196\n" \
  "s_waitcnt lgkmcnt(3)\n" \
  "v_mfma_f32_16x16x32_bf16 v[152:155], v[16:19], v[168:171], v[152:155]\n" \
  "ds_read_b128 v[160:163], v210 offset:49408\n" \
  "global_load_lds_dwordx4 v216, s[14:15] offset:2048 sc1\n" \
  "v_mfma_f32_16x16x32_bf16 v[156:159], v[20:23], v[168:171], v[156:159]\n" \
  "v_fmac_f32 v145, v197, v185\n" \
  "v_mul_f32 v146, v146, v196\n" \
  "s_waitcnt lgkmcnt(3)\n" \
  "v_mfma_f32_16x16x32_bf16 v[152:155], v[24:27], v[172:175], v[152:155]\n" \
  "ds_read_b128 v[164:167], v211 offset:49408\n" \
  "v_fmac_f32 v146, v197, v186\n" \
  "v_mfma_f32_16x16x32_bf16 v[156:159], v[28:31], v[172:175], v[156:159]\n" \
  "v_mul_f32 v147, v147, v196\n" \
  "v_fmac_f32 v147, v197, v187\n" \
  "s_waitcnt lgkmcnt(3)\n" \
  "v_mfma_f32_16x16x32_bf16 v[152:155], v[32:35], v[176:179], v[152:155]\n" \
  "ds_read_b128 v[168:171], v208 offset:49664\n" \
  "v_cvt_pk_bf16_f32 v236, v144, v145\n" \
  "v_mfma_f32_16x16x32_bf16 v[156:159], v[36:39], v[176:179], v[156:159]\n" \
  "v_cvt_pk_bf16_f32 v237, v146, v147\n" \
  "v_mul_f32 v148, v148, v196\n" \
  "s_waitcnt lgkmcnt(3)\n" \
  "v_mfma_f32_16x16x32_bf16 v[152:155], v[40:43], v[180:183], v[152:155]\n" \
  "ds_read_b128 v[172:175], v209 offset:49664\n" \
  "v_fmac_f32 v148, v197, v188\n" \
  "v_mfma_f32_16x16x32_bf16 v[156:159], v[44:47], v[180:183], v[156:159]\n" \
  "v_mul_f32 v149, v149, v196\n" \
  "v_fmac_f32 v149, v197, v189\n" \
  "s_waitcnt lgkmcnt(3)\n" \
  "v_mfma_f32_16x16x32_bf16 v[152:155], v[48:51], v[160:163], v[152:155]\n" \
  "ds_read_b128 v[176:179], v210 offset:49664\n" \
  "v_mul_f32 v150, v150, v196\n" \
  "v_mfma_f32_16x16x32_bf16 v[156:159], v[52:55], v[160:163], v[156:159]\n" \
  "global_load_lds_dwordx4 v216, s[14:15] offset:3072 sc1\n" \
  "s_add_u32 s14, s14, 49152\n" \
  "s_addc_u32 s15, s15, 0\n" \
  "v_fmac_f32 v150, v197, v190\n" \
  "s_waitcnt lgkmcnt(3)\n" \
  "v_mfma_f32_16x16x32_bf16 v[152:155], v[56:59], v[164:167], v[152:155]\n" \
  "ds_read_b128 v[180:183], v211 offset:49664\n" \
  "v_mul_f32 v151, v151, v196\n" \
  "v_mfma_f32_16x16x32_bf16 v[156:159], v[60:63], v[164:167], v[156:159]\n" \
  "v_fmac_f32 v151, v197, v191\n" \
  "v_cvt_pk_bf16_f32 v238, v148, v149\n" \
  "s_waitcnt lgkmcnt(3)\n" \
  "v_mfma_f32_16x16x32_bf16 v[152:155], v[64:67], v[168:171], v[152:155]\n" \
  "ds_read_b128 v[160:163], v208 offset:49920\n" \
  "v_cvt_pk_bf16_f32 v239, v150, v151\n" \
  "v_mfma_f32_16x16x32_bf16 v[156:159], v[68:71], v[168:171], v[156:159]\n" \
  "v_mov_b32 v144, v236\n" \
  "v_mov_b32 v145, v237\n" \
  "s_waitcnt lgkmcnt(3)\n" \
  "v_mfma_f32_16x16x32_bf16 v[152:155], v[72:75], v[172:175], v[152:155]\n" \
  "ds_read_b128 v[164:167], v209 offset:49920\n" \
  "v_mov_b32 v146, v238\n" \
  "v_mfma_f32_16x16x32_bf16 v[156:159], v[76:79], v[172:175], v[156:159]\n" \
  "v_mov_b32 v147, v239\n" \
  "s_waitcnt lgkmcnt(3)\n" \
  "v_mfma_f32_16x16x32_bf16 v[152:155], v[80:83], v[176:179], v[152:155]\n" \
  "ds_read_b128 v[168:171], v210 offset:49920\n" \
  "v_mfma_f32_16x16x32_bf16 v[156:159], v[84:87], v[176:179], v[156:159]\n" \
  "s_waitcnt lgkmcnt(3)\n" \
  "v_mfma_f32_16x16x32_bf16 v[152:155], v[88:91], v[180:183], v[152:155]\n" \
  "ds_read_b128 v[172:175], v211 offset:49920\n" \
  "v_mfma_f32_16x16x32_bf16 v[156:159], v[92:95], v[180:183], v[156:159]\n" \
  "s_waitcnt lgkmcnt(3)\n" \
  "v_mfma_f32_16x16x32_bf16 v[152:155], v[96:99], v[160:163], v[152:155]\n" \
  "v_mfma_f32_16x16x32_bf16 v[156:159], v[100:103], v[160:163], v[156:159]\n" \
  "s_waitcnt lgkmcnt(2)\n" \
  "v_mfma_f32_16x16x32_bf16 v[152:155], v[104:107], v[164:167], v[152:155]\n" \
  "v_mfma_f32_16x16x32_bf16 v[156:159], v[108:111], v[164:167], v[156:159]\n" \
  "s_waitcnt lgkmcnt(1)\n" \
  "v_mfma_f32_16x16x32_bf16 v[152:155], v[112:115], v[168:171], v[152:155]\n" \
  "v_mfma_f32_16x16x32_bf16 v[156:159], v[116:119], v[168:171], v[156:159]\n" \
  "s_waitcnt lgkmcnt(0)\n" \
  "v_mfma_f32_16x16x32_bf16 v[152:155], v[120:123], v[172:175], v[152:155]\n" \
  "v_mfma_f32_16x16x32_bf16 v[156:159], v[124:127], v[172:175], v[156:159]\n" \
  "ds_write_b128 v215, v[128:131]\n" \
  "ds_write_b128 v215, v[136:139] offset:4096\n" \
  "ds_write_b128 v215, v[144:147] offset:8192\n" \
  "s_nop 0\n" \
  "v_mul_f32 v152, v152, v198\n" \
  "v_fmac_f32 v152, v199, v184\n" \
  "v_mul_f32 v153, v153, v198\n" \
  "v_fmac_f32 v153, v199, v185\n" \
  "v_mul_f32 v154, v154, v198\n" \
  "v_fmac_f32 v154, v199, v186\n" \
  "v_mul_f32 v155, v155, v198\n" \
  "v_fmac_f32 v155, v199, v187\n" \
  "v_cvt_pk_bf16_f32 v232, v152, v153\n" \
  "v_cvt_pk_bf16_f32 v233, v154, v155\n" \
  "v_mul_f32 v156, v156, v198\n" \
  "v_fmac_f32 v156, v199, v188\n" \
  "v_mul_f32 v157, v157, v198\n" \
  "v_fmac_f32 v157, v199, v189\n" \
  "v_mul_f32 v158, v158, v198\n" \
  "v_fmac_f32 v158, v199, v190\n" \
  "v_mul_f32 v159, v159, v198\n" \
  "v_fmac_f32 v159, v199, v191\n" \
  "v_cvt_pk_bf16_f32 v234, v156, v157\n" \
  "v_cvt_pk_bf16_f32 v235, v158, v159\n" \
  "ds_write_b128 v215, v[232:235] offset:12288\n" \
  "s_cmp_eq_u32 s58, -1\n" \
  "s_cbranch_scc0 Lqahave_%=\n" \
  "s_max_u32 s42, s55, 1\n" \
  "s_lshl_b32 s42, s42, 2\n" \
  "s_add_u32 s48, s22, s42\n" \
  "s_addc_u32 s49, s23, 0\n" \
  "Lqacw_%=:\n" \
  "global_load_dword v226, v255, s[48:49] sc1\n" \
  "s_waitcnt vmcnt(0)\n" \
  "v_cmp_eq_u32 vcc, -1, v226\n" \
  "s_nop 0\n" \
  "s_cbranch_vccz Lqact_%=\n" \
  "s_sleep 8\n" \
  "s_sub_u32 s41, s41, 1\n" \
  "s_cmp_gt_i32 s41, 0\n" \
  "s_cbranch_scc1 Lqacw_%=\n" \
  "Lqact_%=:\n" \
  "v_readfirstlane_b32 s43, v226\n" \
  "s_cmp_eq_u32 s52, 0\n" \
  "s_cbranch_scc1 Lqaim_%=\n" \
  "Lqacu_%=:\n" \
  "s_memrealtime s[50:51]\n" \
  "s_waitcnt lgkmcnt(0)\n" \
  "s_sub_u32 s42, s50, s43\n" \
  "s_cmp_lt_i32 s42, -1000\n" \
  "s_cbranch_scc1 Lqaim_%=\n" \
  "s_cmp_lt_i32 s42, 0\n" \
  "s_cbranch_scc1 Lqacu_%=\n" \
  "Lqaim_%=:\n" \
  "s_sub_u32 s42, s55, 1\n" \
  "s_and_b32 s43, s42, 31\n" \
  "s_lshl_b32 s44, s43, 16\n" \
  "s_bfe_u32 s45, s43, 65538\n" \
  "s_lshl_b32 s45, s45, 11\n" \
  "s_or_b32 s44, s44, s45\n" \
  "s_bfe_u32 s45, s43, 65539\n" \
  "s_lshl_b32 s45, s45, 14\n" \
  "s_or_b32 s44, s44, s45\n" \
  "s_cmp_lt_i32 s42, 32\n" \
  "s_cselect_b64 s[48:49], s[24:25], s[26:27]\n" \
  "s_xor_b32 s48, s48, s44\n" \
  "s_sub_u32 s42, s55, 0\n" \
  "s_and_b32 s43, s42, 31\n" \
  "s_lshl_b32 s44, s43, 16\n" \
  "s_bfe_u32 s45, s43, 65538\n" \
  "s_lshl_b32 s45, s45, 11\n" \
  "s_or_b32 s44, s44, s45\n" \
  "s_bfe_u32 s45, s43, 65539\n" \
  "s_lshl_b32 s45, s45, 14\n" \
  "s_or_b32 s44, s44, s45\n" \
  "s_cmp_lt_i32 s42, 32\n" \
  "s_cselect_b64 s[50:51], s[24:25], s[26:27]\n" \
  "s_xor_b32 s50, s50, s44\n" \
  "s_lshl_b32 s42, s40, 12\n" \
  "s_add_u32 s42, s42, 98304\n" \
  "s_mov_b32 exec_lo, 0xff00ff00\n" \
  "s_mov_b32 exec_hi, 0xff00ff00\n" \
  "s_add_u32 m0, s42, 0\n" \
  "s_nop 0\n" \
  "global_load_lds_dwordx4 v218, s[50:51] sc1\n" \
  "s_add_u32 m0, s42, 512\n" \
  "s_nop 0\n" \
  "global_load_lds_dwordx4 v218, s[50:51] offset:512 sc1\n" \
  "s_add_u32 m0, s42, 1024\n" \
  "s_nop 0\n" \
  "global_load_lds_dwordx4 v218, s[50:51] offset:1024 sc1\n" \
  "s_add_u32 m0, s42, 1536\n" \
  "s_nop 0\n" \
  "global_load_lds_dwordx4 v218, s[50:51] offset:1536 sc1\n" \
  "s_mov_b32 exec_lo, 0x00ff00ff\n" \
  "s_mov_b32 exec_hi, 0x00ff00ff\n" \
  "s_cmp_eq_u32 s52, 0\n" \
  "s_cbranch_scc1 Lqam09_%=\n" \
  "s_add_u32 m0, s42, 0\n" \
  "s_nop 0\n" \
  "global_load_lds_dwordx4 v218, s[48:49] sc1\n" \
  "s_add_u32 m0, s42, 512\n" \
  "s_nop 0\n" \
  "global_load_lds_dwordx4 v218, s[48:49] offset:512 sc1\n" \
  "s_add_u32 m0, s42, 1024\n" \
  "s_nop 0\n" \
  "global_load_lds_dwordx4 v218, s[48:49] offset:1024 sc1\n" \
  "s_add_u32 m0, s42, 1536\n" \
  "s_nop 0\n" \
  "global_load_lds_dwordx4 v218, s[48:49] offset:1536 sc1\n" \
  "s_branch Lqam19_%=\n" \
  "Lqam09_%=:\n" \
  "s_add_u32 m0, s42, 0\n" \
  "s_nop 0\n" \
  "global_load_lds_dwordx4 v219, s[28:29]\n" \
  "s_add_u32 m0, s42, 960\n" \
  "s_nop 0\n" \
  "global_load_lds_dwordx4 v219, s[28:29] offset:64\n" \
  "s_add_u32 m0, s42, 1920\n" \
  "s_nop 0\n" \
  "global_load_lds_dwordx4 v219, s[28:29] offset:128\n" \
  "s_add_u32 m0, s42, 2880\n" \
  "s_nop 0\n" \
  "global_load_lds_dwordx4 v219, s[28:29] offset:192\n" \
  "Lqam19_%=:\n" \
  "s_mov_b64 exec, -1\n" \
  "Lqahave_%=:\n" \
  "s_waitcnt vmcnt(0) lgkmcnt(0)\n" \
  "s_barrier\n" \
  "v_add_u32 v249, 98304, v214\n" \
  "ds_read_b128 v[160:163], v249\n" \
  "ds_read_b128 v[164:167], v249 offset:1024\n" \
  "ds_read_b128 v[168:171], v249 offset:2048\n" \
  "ds_read_b128 v[172:175], v249 offset:3072\n" \
  "s_waitcnt lgkmcnt(3)\n" \
  "v_mfma_f32_16x16x32_bf16 v[128:131], a[0:3], v[160:163], 0\n" \
  "ds_read_b128 v[176:179], v249 offset:4096\n" \
  "v_mfma_f32_16x16x32_bf16 v[132:135], a[4:7], v[160:163], 0\n" \
  "v_mfma_f32_16x16x32_bf16 v[136:139], a[8:11], v[160:163], 0\n" \
  "v_mfma_f32_16x16x32_bf16 v[140:143], a[12:15], v[160:163], 0\n" \
  "s_waitcnt lgkmcnt(3)\n" \
  "v_mfma_f32_16x16x32_bf16 v[128:131], a[16:19], v[164:167], v[128:131]\n" \
  "ds_read_b128 v[180:183], v249 offset:5120\n" \
  "v_mfma_f32_16x16x32_bf16 v[132:135], a[20:23], v[164:167], v[132:135]\n" \
  "s_lshl_b32 s47, s40, 14\n" \
  "s_add_u32 m0, s47, 0\n" \
  "v_xor_b32 v227, 0, v212\n" \
  "global_load_lds_dwordx4 v227, s[10:11] sc1\n" \
  "v_mfma_f32_16x16x32_bf16 v[136:139], a[24:27], v[164:167], v[136:139]\n" \
  "v_mfma_f32_16x16x32_bf16 v[140:143], a[28:31], v[164:167], v[140:143]\n" \
  "s_waitcnt lgkmcnt(3)\n" \
  "v_mfma_f32_16x16x32_bf16 v[128:131], a[32:35], v[168:171], v[128:131]\n" \
  "ds_read_b128 v[160:163], v249 offset:6144\n" \
  "v_mfma_f32_16x16x32_bf16 v[132:135], a[36:39], v[168:171], v[132:135]\n" \
  "v_xor_b32 v227, 16, v212\n" \
  "global_load_lds_dwordx4 v227, s[10:11] offset:1024 sc1\n" \
  "v_mfma_f32_16x16x32_bf16 v[136:139], a[40:43], v[168:171], v[136:139]\n" \
  "v_mfma_f32_16x16x32_bf16 v[140:143], a[44:47], v[168:171], v[140:143]\n" \
  "s_waitcnt lgkmcnt(3)\n" \
  "v_mfma_f32_16x16x32_bf16 v[128:131], a[48:51], v[172:175], v[128:131]\n" \
  "ds_read_b128 v[164:167], v249 offset:7168\n" \
  "v_mfma_f32_16x16x32_bf16 v[132:135], a[52:55], v[172:175], v[132:135]\n" \
  "v_xor_b32 v227, 32, v212\n" \
  "global_load_lds_dwordx4 v227, s[10:11] offset:2048 sc1\n" \
  "v_mfma_f32_16x16x32_bf16 v[136:139], a[56:59], v[172:175], v[136:139]\n" \
  "v_mfma_f32_16x16x32_bf16 v[140:143], a[60:63], v[172:175], v[140:143]\n" \
  "s_waitcnt lgkmcnt(3)\n" \
  "v_mfma_f32_16x16x32_bf16 v[128:131], a[64:67], v[176:179], v[128:131]\n" \
  "ds_read_b128 v[168:171], v249 offset:8192\n" \
  "v_mfma_f32_16x16x32_bf16 v[132:135], a[68:71], v[176:179], v[132:135]\n" \
  "v_xor_b32 v227, 48, v212\n" \
  "global_load_lds_dwordx4 v227, s[10:11] offset:3072 sc1\n" \
  "s_add_u32 s10, s10, 4096\n" \
  "s_addc_u32 s11, s11, 0\n" \
  "v_mfma_f32_16x16x32_bf16 v[136:139], a[72:75], v[176:179], v[136:139]\n" \
  "v_mfma_f32_16x16x32_bf16 v[140:143], a[76:79], v[176:179], v[140:143]\n" \
  "s_waitcnt lgkmcnt(3)\n" \
  "v_mfma_f32_16x16x32_bf16 v[128:131], a[80:83], v[180:183], v[128:131]\n" \
  "ds_read_b128 v[172:175], v249 offset:9216\n" \
  "v_mfma_f32_16x16x32_bf16 v[132:135], a[84:87], v[180:183], v[132:135]\n" \
  "s_lshl_b32 s47, s40, 14\n" \
  "s_add_u32 m0, s47, 4096\n" \
  "v_xor_b32 v227, 64, v212\n" \
  "global_load_lds_dwordx4 v227, s[10:11] sc1\n" \
  "v_mfma_f32_16x16x32_bf16 v[136:139], a[88:91], v[180:183], v[136:139]\n" \
  "v_mfma_f32_16x16x32_bf16 v[140:143], a[92:95], v[180:183], v[140:143]\n" \
  "s_waitcnt lgkmcnt(3)\n" \
  "v_mfma_f32_16x16x32_bf16 v[128:131], a[96:99], v[160:163], v[128:131]\n" \
  "ds_read_b128 v[176:179], v249 offset:10240\n" \
  "v_mfma_f32_16x16x32_bf16 v[132:135], a[100:103], v[160:163], v[132:135]\n" \
  "v_xor_b32 v227, 80, v212\n" \
  "global_load_lds_dwordx4 v227, s[10:11] offset:1024 sc1\n" \
  "v_mfma_f32_16x16x32_bf16 v[136:139], a[104:107], v[160:163], v[136:139]\n" \
  "v_mfma_f32_16x16x32_bf16 v[140:143], a[108:111], v[160:163], v[140:143]\n" \
  "s_waitcnt lgkmcnt(3)\n" \
  "v_mfma_f32_16x16x32_bf16 v[128:131], a[112:115], v[164:167], v[128:131]\n" \
  "ds_read_b128 v[180:183], v249 offset:11264\n" \
  "v_mfma_f32_16x16x32_bf16 v[132:135], a[116:119], v[164:167], v[132:135]\n" \
  "v_xor_b32 v227, 96, v212\n" \
  "global_load_lds_dwordx4 v227, s[10:11] offset:2048 sc1\n" \
  "v_mfma_f32_16x16x32_bf16 v[136:139], a[120:123], v[164:167], v[136:139]\n" \
  "v_mfma_f32_16x16x32_bf16 v[140:143], a[124:127], v[164:167], v[140:143]\n" \
  "s_waitcnt lgkmcnt(3)\n" \
  "v_mfma_f32_16x16x32_bf16 v[128:131], a[128:131], v[168:171], v[128:131]\n" \
  "ds_read_b128 v[160:163], v249 offset:12288\n" \
  "v_mfma_f32_16x16x32_bf16 v[132:135], a[132:135], v[168:171], v[132:135]\n" \
  "v_xor_b32 v227, 112, v212\n" \
  "global_load_lds_dwordx4 v227, s[10:11] offset:3072 sc1\n" \
  "s_add_u32 s10, s10, 4096\n" \
  "s_addc_u32 s11, s11, 0\n" \
  "v_mfma_f32_16x16x32_bf16 v[136:139], a[136:139], v[168:171], v[136:139]\n" \
  "v_mfma_f32_16x16x32_bf16 v[140:143], a[140:143], v[168:171], v[140:143]\n" \
  "s_waitcnt lgkmcnt(3)\n" \
  "v_mfma_f32_16x16x32_bf16 v[128:131], a[144:147], v[172:175], v[128:131]\n" \
  "ds_read_b128 v[164:167], v249 offset:13312\n" \
  "v_mfma_f32_16x16x32_bf16 v[132:135], a[148:151], v[172:175], v[132:135]\n" \
  "s_lshl_b32 s47, s40, 14\n" \
  "s_add_u32 m0, s47, 8192\n" \
  "v_xor_b32 v227, 128, v212\n" \
  "global_load_lds_dwordx4 v227, s[10:11] sc1\n" \
  "v_mfma_f32_16x16x32_bf16 v[136:139], a[152:155], v[172:175], v[136:139]\n" \
  "v_mfma_f32_16x16x32_bf16 v[140:143], a[156:159], v[172:175], v[140:143]\n" \
  "s_waitcnt lgkmcnt(3)\n" \
  "v_mfma_f32_16x16x32_bf16 v[128:131], a[160:163], v[176:179], v[128:131]\n" \
  "ds_read_b128 v[168:171], v249 offset:14336\n" \
  "v_mfma_f32_16x16x32_bf16 v[132:135], a[164:167], v[176:179], v[132:135]\n" \
  "v_xor_b32 v227, 144, v212\n" \
  "global_load_lds_dwordx4 v227, s[10:11] offset:1024 sc1\n" \
  "v_mfma_f32_16x16x32_bf16 v[136:139], a[168:171], v[176:179], v[136:139]\n" \
  "v_mfma_f32_16x16x32_bf16 v[140:143], a[172:175], v[176:179], v[140:143]\n" \
  "s_waitcnt lgkmcnt(3)\n" \
  "v_mfma_f32_16x16x32_bf16 v[128:131], a[176:179], v[180:183], v[128:131]\n" \
  "ds_read_b128 v[172:175], v249 offset:15360\n" \
  "v_mfma_f32_16x16x32_bf16 v[132:135], a[180:183], v[180:183], v[132:135]\n" \
  "v_xor_b32 v227, 160, v212\n" \
  "global_load_lds_dwordx4 v227, s[10:11] offset:2048 sc1\n" \
  "v_mfma_f32_16x16x32_bf16 v[136:139], a[184:187], v[180:183], v[136:139]\n" \
  "v_mfma_f32_16x16x32_bf16 v[140:143], a[188:191], v[180:183], v[140:143]\n" \
  "s_waitcnt lgkmcnt(3)\n" \
  "v_mfma_f32_16x16x32_bf16 v[128:131], a[192:195], v[160:163], v[128:131]\n" \
  "v_mfma_f32_16x16x32_bf16 v[132:135], a[196:199], v[160:163], v[132:135]\n" \
  "v_xor_b32 v227, 176, v212\n" \
  "global_load_lds_dwordx4 v227, s[10:11] offset:3072 sc1\n" \
  "s_add_u32 s10, s10, 4096\n" \
  "s_addc_u32 s11, s11, 0\n" \
  "v_mfma_f32_16x16x32_bf16 v[136:139], a[200:203], v[160:163], v[136:139]\n" \
  "v_mfma_f32_16x16x32_bf16 v[140:143], a[204:207], v[160:163], v[140:143]\n" \
  "s_waitcnt lgkmcnt(2)\n" \
  "v_mfma_f32_16x16x32_bf16 v[128:131], a[208:211], v[164:167], v[128:131]\n" \
  "v_mfma_f32_16x16x32_bf16 v[132:135], a[212:215], v[164:167], v[132:135]\n" \
  "s_lshl_b32 s47, s40, 14\n" \
  "s_add_u32 m0, s47, 12288\n" \
  "v_xor_b32 v227, 192, v212\n" \
  "global_load_lds_dwordx4 v227, s[10:11] sc1\n" \
  "v_mfma_f32_16x16x32_bf16 v[136:139], a[216:219], v[164:167], v[136:139]\n" \
  "v_mfma_f32_16x16x32_bf16 v[140:143], a[220:223], v[164:167], v[140:143]\n" \
  "s_waitcnt lgkmcnt(1)\n" \
  "v_mfma_f32_16x16x32_bf16 v[128:131], a[224:227], v[168:171], v[128:131]\n" \
  "v_mfma_f32_16x16x32_bf16 v[132:135], a[228:231], v[168:171], v[132:135]\n" \
  "v_xor_b32 v227, 208, v212\n" \
  "global_load_lds_dwordx4 v227, s[10:11] offset:1024 sc1\n" \
  "v_mfma_f32_16x16x32_bf16 v[136:139], a[232:235], v[168:171], v[136:139]\n" \
  "v_mfma_f32_16x16x32_bf16 v[140:143], a[236:239], v[168:171], v[140:143]\n" \
  "s_waitcnt lgkmcnt(0)\n" \
  "v_mfma_f32_16x16x32_bf16 v[128:131], a[240:243], v[172:175], v[128:131]\n" \
  "v_mfma_f32_16x16x32_bf16 v[132:135], a[244:247], v[172:175], v[132:135]\n" \
  "v_xor_b32 v227, 224, v212\n" \
  "global_load_lds_dwordx4 v227, s[10:11] offset:2048 sc1\n" \
  "v_mfma_f32_16x16x32_bf16 v[136:139], a[248:251], v[172:175], v[136:139]\n" \
  "v_mfma_f32_16x16x32_bf16 v[140:143], a[252:255], v[172:175], v[140:143]\n" \
  "v_xor_b32 v227, 240, v212\n" \
  "global_load_lds_dwordx4 v227, s[10:11] offset:3072 sc1\n" \
  "s_add_u32 s10, s10, 118784\n" \
  "s_addc_u32 s11, s11, 0\n" \
  "global_load_dwordx2 v[192:193], v213, s[12:13] sc1\n" \
  "global_load_dwordx2 v[194:195], v213, s[12:13] offset:128 sc1\n" \
  "global_load_dwordx2 v[196:197], v213, s[12:13] offset:256 sc1\n" \
  "global_load_dwordx2 v[198:199], v213, s[12:13] offset:384 sc1\n" \
  "s_add_u32 s12, s12, 1024\n" \
  "s_addc_u32 s13, s13, 0\n" \
  "v_cmp_u_f32 vcc, v128, v128\n" \
  "s_nop 0\n" \
  "s_cbranch_vccz Lqaok_%=\n" \
  "s_sub_u32 s41, s41, 1\n" \
  "s_cmp_gt_i32 s41, 0\n" \
  "s_cbranch_scc0 Lqaok_%=\n" \
  "s_sub_u32 s10, s10, 131072\n" \
  "s_subb_u32 s11, s11, 0\n" \
  "s_sub_u32 s12, s12, 1024\n" \
  "s_subb_u32 s13, s13, 0\n" \
  "s_barrier\n" \
  "s_branch Lqaim_%=\n" \
  "Lqaok_%=:\n" \
  "s_nop 7\n" \
  "s_nop 1\n" \
  "v_cvt_pk_bf16_f32 v236, v128, v129\n" \
  "v_cvt_pk_bf16_f32 v237, v130, v131\n" \
  "v_cvt_pk_bf16_f32 v238, v132, v133\n" \
  "v_cvt_pk_bf16_f32 v239, v134, v135\n" \
  "ds_write_b128 v220, v[236:239]\n" \
  "v_cvt_pk_bf16_f32 v232, v136, v137\n" \
  "v_cvt_pk_bf16_f32 v233, v138, v139\n" \
  "v_cvt_pk_bf16_f32 v234, v140, v141\n" \
  "v_cvt_pk_bf16_f32 v235, v142, v143\n" \
  "ds_write_b128 v220, v[232:235] offset:512\n" \
  "s_waitcnt lgkmcnt(0)\n" \
  "s_barrier\n" \
  "s_lshl_b32 s42, s40, 12\n" \
  "s_add_u32 s42, s42, 114688\n" \
  "v_add_u32 v226, s42, v214\n" \
  "ds_read_b128 v[128:131], v226\n" \
  "ds_read_b128 v[132:135], v221\n" \
  "ds_read_b128 v[136:139], v221 offset:256\n" \
  "ds_read_b128 v[140:143], v222\n" \
  "ds_read_b128 v[144:147], v226 offset:1024\n" \
  "ds_read_b128 v[148:151], v221 offset:2048\n" \
  "ds_read_b128 v[152:155], v221 offset:2304\n" \
  "ds_read_b128 v[156:159], v222 offset:512\n" \
  "ds_read_b128 v[160:163], v226 offset:2048\n" \
  "ds_read_b128 v[164:167], v221 offset:4096\n" \
  "ds_read_b128 v[168:171], v221 offset:4352\n" \
  "ds_read_b128 v[172:175], v222 offset:1024\n" \
  "v_mov_b32 v243, 0xff61b1e6\n" \
  "v_cmp_lt_u32 s[56:57], 31, v254\n" \
  "v_mov_b32 v244, 0\n" \
  "v_mov_b32 v245, 0\n" \
  "s_waitcnt lgkmcnt(8)\n" \
  "v_mfma_f32_16x16x32_bf16 v[200:203], v[132:135], v[128:131], 0\n" \
  "v_mfma_f32_16x16x32_bf16 v[204:207], v[136:139], v[128:131], 0\n" \
  "v_mfma_f32_16x16x32_bf16 v[232:235], v[140:143], v[128:131], 0\n" \
  "ds_read_b128 v[128:131], v226 offset:3072\n" \
  "ds_read_b128 v[132:135], v221 offset:6144\n" \
  "ds_read_b128 v[136:139], v221 offset:6400\n" \
  "ds_read_b128 v[140:143], v222 offset:1536\n" \
  "s_waitcnt lgkmcnt(8)\n" \
  "v_mfma_f32_16x16x32_bf16 v[200:203], v[148:151], v[144:147], v[200:203]\n" \
  "v_mfma_f32_16x16x32_bf16 v[204:207], v[152:155], v[144:147], v[204:207]\n" \
  "v_mfma_f32_16x16x32_bf16 v[232:235], v[156:159], v[144:147], v[232:235]\n" \
  "s_waitcnt lgkmcnt(4)\n" \
  "v_mfma_f32_16x16x32_bf16 v[200:203], v[164:167], v[160:163], v[200:203]\n" \
  "v_mfma_f32_16x16x32_bf16 v[204:207], v[168:171], v[160:163], v[204:207]\n" \
  "v_mfma_f32_16x16x32_bf16 v[232:235], v[172:175], v[160:163], v[232:235]\n" \
  "s_waitcnt lgkmcnt(0)\n" \
  "v_mfma_f32_16x16x32_bf16 v[200:203], v[132:135], v[128:131], v[200:203]\n" \
  "v_mfma_f32_16x16x32_bf16 v[204:207], v[136:139], v[128:131], v[204:207]\n" \
  "v_mfma_f32_16x16x32_bf16 v[232:235], v[140:143], v[128:131], v[232:235]\n" \
  "ds_read_b64_tr_b16 v[160:161], v223\n" \
  "ds_read_b64_tr_b16 v[162:163], v223 offset:256\n" \
  "ds_read_b64_tr_b16 v[164:165], v224\n" \
  "ds_read_b64_tr_b16 v[166:167], v223 offset:1024\n" \
  "ds_read_b64_tr_b16 v[168:169], v223 offset:1280\n" \
  "ds_read_b64_tr_b16 v[170:171], v224 offset:256\n" \
  "ds_read_b64_tr_b16 v[172:173], v223 offset:2048\n" \
  "ds_read_b64_tr_b16 v[174:175], v223 offset:2304\n" \
  "ds_read_b64_tr_b16 v[176:177], v224 offset:512\n" \
  "ds_read_b64_tr_b16 v[178:179], v223 offset:3072\n" \
  "ds_read_b64_tr_b16 v[180:181], v223 offset:3328\n" \
  "ds_read_b64_tr_b16 v[182:183], v224 offset:768\n" \
  "v_cmp_gt_i32 vcc, 0, v228\n" \
  "s_nop 1\n" \
  "v_cndmask_b32 v200, v200, v243, vcc\n" \
  "v_cmp_gt_i32 vcc, 1, v228\n" \
  "s_nop 1\n" \
  "v_cndmask_b32 v201, v201, v243, vcc\n" \
  "v_cmp_gt_i32 vcc, 2, v228\n" \
  "s_nop 1\n" \
  "v_cndmask_b32 v202, v202, v243, vcc\n" \
  "v_cmp_gt_i32 vcc, 3, v228\n" \
  "s_nop 1\n" \
  "v_cndmask_b32 v203, v203, v243, vcc\n" \
  "v_cmp_gt_i32 vcc, 16, v228\n" \
  "s_nop 1\n" \
  "v_cndmask_b32 v204, v204, v243, vcc\n" \
  "v_cmp_gt_i32 vcc, 17, v228\n" \
  "s_nop 1\n" \
  "v_cndmask_b32 v205, v205, v243, vcc\n" \
  "v_cmp_gt_i32 vcc, 18, v228\n" \
  "s_nop 1\n" \
  "v_cndmask_b32 v206, v206, v243, vcc\n" \
  "v_cmp_gt_i32 vcc, 19, v228\n" \
  "s_nop 1\n" \
  "v_cndmask_b32 v207, v207, v243, vcc\n" \
  "v_cndmask_b32 v232, v232, v243, s[56:57]\n" \
  "v_cndmask_b32 v233, v233, v243, s[56:57]\n" \
  "v_cndmask_b32 v234, v234, v243, s[56:57]\n" \
  "v_cndmask_b32 v235, v235, v243, s[56:57]\n" \
  "v_max3_f32 v242, v200, v201, v202\n" \
  "v_max3_f32 v242, v242, v203, v204\n" \
  "v_max3_f32 v242, v242, v205, v206\n" \
  "v_max3_f32 v242, v242, v207, v232\n" \
  "v_max3_f32 v242, v242, v233, v234\n" \
  "v_max_f32 v242, v242, v235\n" \
  "v_mov_b32 v226, v242\n" \
  "s_nop 1\n" \
  "v_permlane16_swap_b32 v242, v226\n" \
  "v_max_f32 v242, v242, v226\n" \
  "v_mov_b32 v226, v242\n" \
  "s_nop 1\n" \
  "v_permlane32_swap_b32 v242, v226\n" \
  "v_max_f32 v242, v242, v226\n" \
  "v_mul_f32_e64 v242, -v242, s54\n" \
  "v_fma_f32 v200, v200, s54, v242\n" \
  "v_fma_f32 v201, v201, s54, v242\n" \
  "v_fma_f32 v202, v202, s54, v242\n" \
  "v_fma_f32 v203, v203, s54, v242\n" \
  "v_fma_f32 v204, v204, s54, v242\n" \
  "v_fma_f32 v205, v205, s54, v242\n" \
  "v_fma_f32 v206, v206, s54, v242\n" \
  "v_fma_f32 v207, v207, s54, v242\n" \
  "v_fma_f32 v232, v232, s54, v242\n" \
  "v_fma_f32 v233, v233, s54, v242\n" \
  "v_fma_f32 v234, v234, s54, v242\n" \
  "v_fma_f32 v235, v235, s54, v242\n" \
  "v_exp_f32 v200, v200\n" \
  "v_exp_f32 v201, v201\n" \
  "v_exp_f32 v202, v202\n" \
  "v_exp_f32 v203, v203\n" \
  "v_exp_f32 v204, v204\n" \
  "v_exp_f32 v205, v205\n" \
  "v_exp_f32 v206, v206\n" \
  "v_exp_f32 v207, v207\n" \
  "v_exp_f32 v232, v232\n" \
  "v_exp_f32 v233, v233\n" \
  "v_exp_f32 v234, v234\n" \
  "v_exp_f32 v235, v235\n" \
  "v_add_f32 v248, v200, v201\n" \
  "v_add_f32 v248, v248, v202\n" \
  "v_add_f32 v248, v248, v203\n" \
  "v_add_f32 v248, v248, v204\n" \
  "v_add_f32 v248, v248, v205\n" \
  "v_add_f32 v248, v248, v206\n" \
  "v_add_f32 v248, v248, v207\n" \
  "v_add_f32 v248, v248, v232\n" \
  "v_add_f32 v248, v248, v233\n" \
  "v_add_f32 v248, v248, v234\n" \
  "v_add_f32 v248, v248, v235\n" \
  "v_cvt_pk_bf16_f32 v236, v200, v201\n" \
  "v_cvt_pk_bf16_f32 v237, v202, v203\n" \
  "v_cvt_pk_bf16_f32 v238, v204, v205\n" \
  "v_cvt_pk_bf16_f32 v239, v206, v207\n" \
  "v_cvt_pk_bf16_f32 v246, v232, v233\n" \
  "v_cvt_pk_bf16_f32 v247, v234, v235\n" \
  "s_waitcnt lgkmcnt(9)\n" \
  "v_mfma_f32_16x16x32_bf16 v[128:131], v[160:163], v[236:239], 0\n" \
  "v_mfma_f32_16x16x32_bf16 v[128:131], v[162:165], v[244:247], v[128:131]\n" \
  "s_waitcnt lgkmcnt(6)\n" \
  "v_mfma_f32_16x16x32_bf16 v[132:135], v[166:169], v[236:239], 0\n" \
  "v_mfma_f32_16x16x32_bf16 v[132:135], v[168:171], v[244:247], v[132:135]\n" \
  "s_waitcnt lgkmcnt(3)\n" \
  "v_mfma_f32_16x16x32_bf16 v[136:139], v[172:175], v[236:239], 0\n" \
  "v_mfma_f32_16x16x32_bf16 v[136:139], v[174:177], v[244:247], v[136:139]\n" \
  "s_waitcnt lgkmcnt(0)\n" \
  "v_mfma_f32_16x16x32_bf16 v[140:143], v[178:181], v[236:239], 0\n" \
  "v_mfma_f32_16x16x32_bf16 v[140:143], v[180:183], v[244:247], v[140:143]\n" \
  "ds_read_b64_tr_b16 v[160:161], v223 offset:4096\n" \
  "ds_read_b64_tr_b16 v[162:163], v223 offset:4352\n" \
  "ds_read_b64_tr_b16 v[164:165], v224 offset:1024\n" \
  "ds_read_b64_tr_b16 v[166:167], v223 offset:5120\n" \
  "ds_read_b64_tr_b16 v[168:169], v223 offset:5376\n" \
  "ds_read_b64_tr_b16 v[170:171], v224 offset:1280\n" \
  "ds_read_b64_tr_b16 v[172:173], v223 offset:6144\n" \
  "ds_read_b64_tr_b16 v[174:175], v223 offset:6400\n" \
  "ds_read_b64_tr_b16 v[176:177], v224 offset:1536\n" \
  "ds_read_b64_tr_b16 v[178:179], v223 offset:7168\n" \
  "ds_read_b64_tr_b16 v[180:181], v223 offset:7424\n" \
  "ds_read_b64_tr_b16 v[182:183], v224 offset:1792\n" \
  "v_mov_b32 v226, v248\n" \
  "s_nop 1\n" \
  "v_permlane16_swap_b32 v248, v226\n" \
  "v_add_f32 v248, v248, v226\n" \
  "v_mov_b32 v226, v248\n" \
  "s_nop 1\n" \
  "v_permlane32_swap_b32 v248, v226\n" \
  "v_add_f32 v248, v248, v226\n" \
  "v_rcp_f32 v248, v248\n" \
  "s_waitcnt lgkmcnt(9)\n" \
  "v_mfma_f32_16x16x32_bf16 v[144:147], v[160:163], v[236:239], 0\n" \
  "v_mfma_f32_16x16x32_bf16 v[144:147], v[162:165], v[244:247], v[144:147]\n" \
  "s_waitcnt lgkmcnt(6)\n" \
  "v_mfma_f32_16x16x32_bf16 v[148:151], v[166:169], v[236:239], 0\n" \
  "v_mfma_f32_16x16x32_bf16 v[148:151], v[168:171], v[244:247], v[148:151]\n" \
  "s_waitcnt lgkmcnt(3)\n" \
  "v_mfma_f32_16x16x32_bf16 v[152:155], v[172:175], v[236:239], 0\n" \
  "v_mfma_f32_16x16x32_bf16 v[152:155], v[174:177], v[244:247], v[152:155]\n" \
  "s_waitcnt lgkmcnt(0)\n" \
  "v_mfma_f32_16x16x32_bf16 v[156:159], v[178:181], v[236:239], 0\n" \
  "v_mfma_f32_16x16x32_bf16 v[156:159], v[180:183], v[244:247], v[156:159]\n" \
  "v_mul_f32 v128, v128, v248\n" \
  "v_mul_f32 v129, v129, v248\n" \
  "v_mul_f32 v130, v130, v248\n" \
  "v_mul_f32 v131, v131, v248\n" \
  "v_cvt_pk_bf16_f32 v160, v128, v129\n" \
  "v_cvt_pk_bf16_f32 v161, v130, v131\n" \
  "v_mul_f32 v132, v132, v248\n" \
  "v_mul_f32 v133, v133, v248\n" \
  "v_mul_f32 v134, v134, v248\n" \
  "v_mul_f32 v135, v135, v248\n" \
  "v_cvt_pk_bf16_f32 v162, v132, v133\n" \
  "v_cvt_pk_bf16_f32 v163, v134, v135\n" \
  "global_store_dwordx4 v214, v[160:163], s[20:21] sc1\n" \
  "v_mul_f32 v136, v136, v248\n" \
  "v_mul_f32 v137, v137, v248\n" \
  "v_mul_f32 v138, v138, v248\n" \
  "v_mul_f32 v139, v139, v248\n" \
  "v_cvt_pk_bf16_f32 v164, v136, v137\n" \
  "v_cvt_pk_bf16_f32 v165, v138, v139\n" \
  "v_mul_f32 v140, v140, v248\n" \
  "v_mul_f32 v141, v141, v248\n" \
  "v_mul_f32 v142, v142, v248\n" \
  "v_mul_f32 v143, v143, v248\n" \
  "v_cvt_pk_bf16_f32 v166, v140, v141\n" \
  "v_cvt_pk_bf16_f32 v167, v142, v143\n" \
  "global_store_dwordx4 v214, v[164:167], s[20:21] offset:1024 sc1\n" \
  "v_mul_f32 v144, v144, v248\n" \
  "v_mul_f32 v145, v145, v248\n" \
  "v_mul_f32 v146, v146, v248\n" \
  "v_mul_f32 v147, v147, v248\n" \
  "v_cvt_pk_bf16_f32 v168, v144, v145\n" \
  "v_cvt_pk_bf16_f32 v169, v146, v147\n" \
  "v_mul_f32 v148, v148, v248\n" \
  "v_mul_f32 v149, v149, v248\n" \
  "v_mul_f32 v150, v150, v248\n" \
  "v_mul_f32 v151, v151, v248\n" \
  "v_cvt_pk_bf16_f32 v170, v148, v149\n" \
  "v_cvt_pk_bf16_f32 v171, v150, v151\n" \
  "global_store_dwordx4 v214, v[168:171], s[20:21] offset:2048 sc1\n" \
  "v_mul_f32 v152, v152, v248\n" \
  "v_mul_f32 v153, v153, v248\n" \
  "v_mul_f32 v154, v154, v248\n" \
  "v_mul_f32 v155, v155, v248\n" \
  "v_cvt_pk_bf16_f32 v172, v152, v153\n" \
  "v_cvt_pk_bf16_f32 v173, v154, v155\n" \
  "v_mul_f32 v156, v156, v248\n" \
  "v_mul_f32 v157, v157, v248\n" \
  "v_mul_f32 v158, v158, v248\n" \
  "v_mul_f32 v159, v159, v248\n" \
  "v_cvt_pk_bf16_f32 v174, v156, v157\n" \
  "v_cvt_pk_bf16_f32 v175, v158, v159\n" \
  "global_store_dwordx4 v214, v[172:175], s[20:21] offset:3072 sc1\n" \
  "s_add_u32 s20, s20, 1048576\n" \
  "s_addc_u32 s21, s21, 0\n" \
  "s_add_u32 s52, s52, 2\n" \
  "s_waitcnt vmcnt(0)\n" \
  "s_barrier\n" \
  "s_lshr_b32 s42, s52, 1\n" \
  "v_mov_b32 v225, s42\n" \
  "s_mov_b64 exec, s[60:61]\n" \
  "global_store_dword v255, v225, s[18:19] sc1\n" \
  "s_mov_b64 exec, -1\n" \
  "s_lshl_b32 s55, s52, 1\n" \
  "s_max_u32 s42, s55, 1\n" \
  "s_lshl_b32 s42, s42, 2\n" \
  "s_add_u32 s48, s22, s42\n" \
  "s_addc_u32 s49, s23, 0\n" \
  "global_load_dword v250, v255, s[16:17] sc1\n" \
  "global_load_dword v251, v255, s[48:49] sc1\n" \
  "s_cmp_lt_u32 s52, 32\n" \
  "s_cbranch_scc1 Lqatop_%=\n" \
  "s_waitcnt vmcnt(0)\n" \
  "s_cmp_eq_u32 s69, 1\n" \
  "s_cbranch_scc1 Lqaend_%=\n" \
  "s_mov_b32 s69, 1\n" \
  "s_branch Lqalayer_%=\n" \
  "Lqaend_%=:\n" \
  "s_endpgm\n"
#define O_ASM \
  "s_mov_b32 s40, %[w]\n" \
  "v_mbcnt_lo_u32_b32 v254, -1, 0\n" \
  "v_mbcnt_hi_u32_b32 v254, -1, v254\n" \
  "v_lshlrev_b32 v255, 2, v254\n" \
  "v_add_u32 v255, 162816, v255\n" \
  "ds_read_b32 v255, v255\n" \
  "s_waitcnt lgkmcnt(0)\n" \
  "v_readlane_b32 s70, v255, 0\n" \
  "v_readlane_b32 s71, v255, 1\n" \
  "v_readlane_b32 s72, v255, 2\n" \
  "v_readlane_b32 s73, v255, 3\n" \
  "v_readlane_b32 s74, v255, 4\n" \
  "v_readlane_b32 s75, v255, 5\n" \
  "v_readlane_b32 s76, v255, 6\n" \
  "v_readlane_b32 s77, v255, 7\n" \
  "v_readlane_b32 s78, v255, 8\n" \
  "v_readlane_b32 s79, v255, 9\n" \
  "v_readlane_b32 s80, v255, 10\n" \
  "v_readlane_b32 s81, v255, 11\n" \
  "v_readlane_b32 s82, v255, 12\n" \
  "v_readlane_b32 s83, v255, 13\n" \
  "v_readlane_b32 s84, v255, 14\n" \
  "v_readlane_b32 s85, v255, 15\n" \
  "v_readlane_b32 s86, v255, 16\n" \
  "v_readlane_b32 s87, v255, 17\n" \
  "v_readlane_b32 s88, v255, 18\n" \
  "v_readlane_b32 s89, v255, 19\n" \
  "v_readlane_b32 s90, v255, 20\n" \
  "v_readlane_b32 s91, v255, 21\n" \
  "v_readlane_b32 s92, v255, 22\n" \
  "v_readlane_b32 s93, v255, 23\n" \
  "v_readlane_b32 s94, v255, 24\n" \
  "v_readlane_b32 s95, v255, 25\n" \
  "v_readlane_b32 s96, v255, 26\n" \
  "v_readlane_b32 s97, v255, 27\n" \
  "v_readlane_b32 s98, v255, 28\n" \
  "v_readlane_b32 s99, v255, 29\n" \
  "v_readlane_b32 s34, v255, 32\n" \
  "v_readlane_b32 s35, v255, 33\n" \
  "v_readlane_b32 s36, v255, 34\n" \
  "v_readlane_b32 s37, v255, 35\n" \
  "v_readlane_b32 s38, v255, 36\n" \
  "v_readlane_b32 s39, v255, 37\n" \
  "s_mov_b32 s69, 0\n" \
  "s_nop 7\n" \
  "s_nop 1\n" \
  "s_mov_b64 s[4:5], s[70:71]\n" \
  "s_mov_b64 s[6:7], s[72:73]\n" \
  "s_mov_b64 s[8:9], s[74:75]\n" \
  "s_mov_b64 s[10:11], s[76:77]\n" \
  "s_mov_b64 s[12:13], s[78:79]\n" \
  "s_mov_b64 s[14:15], s[80:81]\n" \
  "s_mov_b64 s[16:17], s[82:83]\n" \
  "s_mov_b64 s[18:19], s[84:85]\n" \
  "s_mov_b64 s[20:21], s[86:87]\n" \
  "s_mov_b64 s[22:23], s[88:89]\n" \
  "s_mov_b64 s[24:25], s[90:91]\n" \
  "s_mov_b64 s[26:27], s[92:93]\n" \
  "s_mov_b64 s[28:29], s[94:95]\n" \
  "v_mov_b32 v255, 0\n" \
  "s_mov_b32 s41, 400000\n" \
  "s_cmp_eq_u32 s40, 0\n" \
  "s_cselect_b64 s[60:61], 1, 0\n" \
  "s_nop 7\n" \
  "s_nop 1\n" \
  "s_barrier\n" \
  "v_lshlrev_b32 v240, 4, v254\n" \
  "v_and_b32 v241, 7, v254\n" \
  "s_lshl_b32 s42, s40, 3\n" \
  "v_add_u32 v241, s42, v241\n" \
  "v_lshlrev_b32 v241, 7, v241\n" \
  "v_and_b32 v245, 15, v254\n" \
  "s_and_b32 s42, s40, 1\n" \
  "s_lshl_b32 s43, s42, 4\n" \
  "v_add_u32 v245, s43, v245\n" \
  "v_lshrrev_b32 v246, 4, v254\n" \
  "v_lshlrev_b32 v242, 10, v245\n" \
  "v_lshl_add_u32 v242, v246, 4, v242\n" \
  "s_lshl_b32 s43, s40, 12\n" \
  "v_add_u32 v243, s43, v240\n" \
  "s_lshl_b32 s43, s42, 11\n" \
  "v_add_u32 v244, s43, v240\n" \
  "Lolayer_%=:\n" \
  "s_mov_b64 s[4:5], s[70:71]\n" \
  "s_mov_b64 s[6:7], s[72:73]\n" \
  "s_mov_b64 s[8:9], s[74:75]\n" \
  "s_mov_b64 s[10:11], s[76:77]\n" \
  "s_mov_b64 s[12:13], s[78:79]\n" \
  "s_mov_b64 s[14:15], s[80:81]\n" \
  "s_mov_b64 s[16:17], s[82:83]\n" \
  "s_mov_b64 s[18:19], s[84:85]\n" \
  "s_mov_b64 s[20:21], s[86:87]\n" \
  "s_mov_b64 s[22:23], s[88:89]\n" \
  "s_mov_b64 s[24:25], s[90:91]\n" \
  "s_mov_b64 s[26:27], s[92:93]\n" \
  "s_mov_b64 s[28:29], s[94:95]\n" \
  "s_lshl_b32 s42, s69, 16\n" \
  "s_add_u32 s8, s8, s42\n" \
  "s_addc_u32 s9, s9, 0\n" \
  "s_add_u32 s24, s24, s42\n" \
  "s_addc_u32 s25, s25, 0\n" \
  "s_add_u32 s26, s26, s42\n" \
  "s_addc_u32 s27, s27, 0\n" \
  "s_cmp_eq_u32 s69, 1\n" \
  "s_cselect_b64 s[16:17], s[96:97], s[16:17]\n" \
  "s_cselect_b64 s[12:13], s[98:99], s[12:13]\n" \
  "v_lshrrev_b32 v246, 4, v254\n" \
  "v_lshlrev_b32 v247, 3, v254\n" \
  "v_mov_b32 v249, 1\n" \
  "s_and_b32 s56, s15, 3\n" \
  "s_lshr_b32 s53, s15, 2\n" \
  "s_lshl_b32 s53, s53, 1\n" \
  "s_add_u32 s53, s53, s14\n" \
  "v_and_b32 v245, 15, v254\n" \
  "v_lshlrev_b32 v250, 10, v245\n" \
  "v_lshl_add_u32 v250, v246, 4, v250\n" \
  "s_lshl_b32 s42, s56, 8\n" \
  "s_lshl_b32 s43, s40, 6\n" \
  "s_add_u32 s42, s42, s43\n" \
  "v_add_u32 v250, s42, v250\n" \
  "s_lshl_b32 s43, s40, 12\n" \
  "v_add_u32 v251, s43, v240\n" \
  "s_lshl_b32 s42, s15, 6\n" \
  "s_sub_u32 s54, s10, s42\n" \
  "s_subb_u32 s55, s11, 0\n" \
  "s_lshl_b32 s42, s53, 15\n" \
  "s_add_u32 s16, s16, s42\n" \
  "s_addc_u32 s17, s17, 0\n" \
  "s_add_u32 s54, s54, s42\n" \
  "s_addc_u32 s55, s55, 0\n" \
  "s_add_u32 s42, s42, s43\n" \
  "s_add_u32 s18, s18, s42\n" \
  "s_addc_u32 s19, s19, 0\n" \
  "s_lshl_b32 s42, s53, 10\n" \
  "s_lshl_b32 s44, s56, 7\n" \
  "s_add_u32 s42, s42, s44\n" \
  "s_add_u32 s28, s28, s42\n" \
  "s_addc_u32 s29, s29, 0\n" \
  "s_lshl_b32 s42, s53, 2\n" \
  "s_add_u32 s42, s42, s56\n" \
  "s_lshl_b32 s42, s42, 7\n" \
  "s_add_u32 s26, s26, s42\n" \
  "s_addc_u32 s27, s27, 0\n" \
  "s_add_u32 s22, s22, s43\n" \
  "s_addc_u32 s23, s23, 0\n" \
  "s_lshl_b32 s42, s56, 13\n" \
  "s_lshl_b32 s44, s40, 11\n" \
  "s_add_u32 s42, s42, s44\n" \
  "s_add_u32 s20, s20, s42\n" \
  "s_addc_u32 s21, s21, 0\n" \
  "v_lshlrev_b32 v252, 3, v245\n" \
  "s_lshl_b32 s42, s40, 8\n" \
  "s_add_u32 s42, s42, 32768\n" \
  "v_add_u32 v252, s42, v252\n" \
  "v_add_u32 v253, 32768, v247\n" \
  "v_lshlrev_b32 v248, 3, v245\n" \
  "v_lshl_add_u32 v248, v246, 9, v248\n" \
  "v_lshlrev_b32 v208, 7, v254\n" \
  "s_mov_b32 s52, 1\n" \
  "s_cmp_eq_u32 s40, 0\n" \
  "s_cbranch_scc0 Low1_%=\n" \
  "Los2_%=:\n" \
  "global_load_dword v245, v208, s[24:25] sc1\n" \
  "s_waitcnt vmcnt(0)\n" \
  "v_cmp_gt_u32 vcc, s52, v245\n" \
  "s_nop 0\n" \
  "s_cbranch_vccz Los2b_%=\n" \
  "s_sleep 2\n" \
  "s_sub_u32 s41, s41, 1\n" \
  "s_cmp_gt_i32 s41, 0\n" \
  "s_cbranch_scc1 Los2_%=\n" \
  "Los2b_%=:\n" \
  "Low1_%=:\n" \
  "s_barrier\n" \
  "global_load_dwordx4 v[160:163], v240, s[22:23] sc1\n" \
  "global_load_dwordx4 v[164:167], v240, s[22:23] offset:1024 sc1\n" \
  "global_load_dwordx4 v[168:171], v240, s[22:23] offset:2048 sc1\n" \
  "global_load_dwordx4 v[172:175], v240, s[22:23] offset:3072 sc1\n" \
  "s_add_u32 s48, s18, 16384\n" \
  "s_addc_u32 s49, s19, 0\n" \
  "global_load_dwordx4 v[128:131], v240, s[18:19] sc1\n" \
  "global_load_dwordx4 v[132:135], v240, s[18:19] offset:1024 sc1\n" \
  "global_load_dwordx4 v[136:139], v240, s[18:19] offset:2048 sc1\n" \
  "global_load_dwordx4 v[140:143], v240, s[18:19] offset:3072 sc1\n" \
  "global_load_dwordx4 v[144:147], v240, s[48:49] sc1\n" \
  "global_load_dwordx4 v[148:151], v240, s[48:49] offset:1024 sc1\n" \
  "global_load_dwordx4 v[152:155], v240, s[48:49] offset:2048 sc1\n" \
  "global_load_dwordx4 v[156:159], v240, s[48:49] offset:3072 sc1\n" \
  "s_add_u32 s18, s18, 262144\n" \
  "s_addc_u32 s19, s19, 0\n" \
  "global_load_dwordx4 v[0:3], v240, s[20:21] sc1\n" \
  "global_load_dwordx4 v[4:7], v240, s[20:21] offset:1024 sc1\n" \
  "s_add_u32 s20, s20, 32768\n" \
  "s_addc_u32 s21, s21, 0\n" \
  "global_load_dwordx4 v[8:11], v240, s[20:21] sc1\n" \
  "global_load_dwordx4 v[12:15], v240, s[20:21] offset:1024 sc1\n" \
  "s_add_u32 s20, s20, 32768\n" \
  "s_addc_u32 s21, s21, 0\n" \
  "global_load_dwordx4 v[16:19], v240, s[20:21] sc1\n" \
  "global_load_dwordx4 v[20:23], v240, s[20:21] offset:1024 sc1\n" \
  "s_add_u32 s20, s20, 32768\n" \
  "s_addc_u32 s21, s21, 0\n" \
  "global_load_dwordx4 v[24:27], v240, s[20:21] sc1\n" \
  "global_load_dwordx4 v[28:31], v240, s[20:21] offset:1024 sc1\n" \
  "s_add_u32 s20, s20, 32768\n" \
  "s_addc_u32 s21, s21, 0\n" \
  "global_load_dwordx4 v[32:35], v240, s[20:21] sc1\n" \
  "global_load_dwordx4 v[36:39], v240, s[20:21] offset:1024 sc1\n" \
  "s_add_u32 s20, s20, 32768\n" \
  "s_addc_u32 s21, s21, 0\n" \
  "global_load_dwordx4 v[40:43], v240, s[20:21] sc1\n" \
  "global_load_dwordx4 v[44:47], v240, s[20:21] offset:1024 sc1\n" \
  "s_add_u32 s20, s20, 32768\n" \
  "s_addc_u32 s21, s21, 0\n" \
  "global_load_dwordx4 v[48:51], v240, s[20:21] sc1\n" \
  "global_load_dwordx4 v[52:55], v240, s[20:21] offset:1024 sc1\n" \
  "s_add_u32 s20, s20, 32768\n" \
  "s_addc_u32 s21, s21, 0\n" \
  "global_load_dwordx4 v[56:59], v240, s[20:21] sc1\n" \
  "global_load_dwordx4 v[60:63], v240, s[20:21] offset:1024 sc1\n" \
  "s_add_u32 s20, s20, 32768\n" \
  "s_addc_u32 s21, s21, 0\n" \
  "global_load_dwordx4 v[64:67], v240, s[20:21] sc1\n" \
  "global_load_dwordx4 v[68:71], v240, s[20:21] offset:1024 sc1\n" \
  "s_add_u32 s20, s20, 32768\n" \
  "s_addc_u32 s21, s21, 0\n" \
  "global_load_dwordx4 v[72:75], v240, s[20:21] sc1\n" \
  "global_load_dwordx4 v[76:79], v240, s[20:21] offset:1024 sc1\n" \
  "s_add_u32 s20, s20, 32768\n" \
  "s_addc_u32 s21, s21, 0\n" \
  "global_load_dwordx4 v[80:83], v240, s[20:21] sc1\n" \
  "global_load_dwordx4 v[84:87], v240, s[20:21] offset:1024 sc1\n" \
  "s_add_u32 s20, s20, 32768\n" \
  "s_addc_u32 s21, s21, 0\n" \
  "global_load_dwordx4 v[88:91], v240, s[20:21] sc1\n" \
  "global_load_dwordx4 v[92:95], v240, s[20:21] offset:1024 sc1\n" \
  "s_add_u32 s20, s20, 32768\n" \
  "s_addc_u32 s21, s21, 0\n" \
  "global_load_dwordx4 v[96:99], v240, s[20:21] sc1\n" \
  "global_load_dwordx4 v[100:103], v240, s[20:21] offset:1024 sc1\n" \
  "s_add_u32 s20, s20, 32768\n" \
  "s_addc_u32 s21, s21, 0\n" \
  "global_load_dwordx4 v[104:107], v240, s[20:21] sc1\n" \
  "global_load_dwordx4 v[108:111], v240, s[20:21] offset:1024 sc1\n" \
  "s_add_u32 s20, s20, 32768\n" \
  "s_addc_u32 s21, s21, 0\n" \
  "global_load_dwordx4 v[112:115], v240, s[20:21] sc1\n" \
  "global_load_dwordx4 v[116:119], v240, s[20:21] offset:1024 sc1\n" \
  "s_add_u32 s20, s20, 32768\n" \
  "s_addc_u32 s21, s21, 0\n" \
  "global_load_dwordx4 v[120:123], v240, s[20:21] sc1\n" \
  "global_load_dwordx4 v[124:127], v240, s[20:21] offset:1024 sc1\n" \
  "s_add_u32 s20, s20, 32768\n" \
  "s_addc_u32 s21, s21, 0\n" \
  "Loa2_%=:\n" \
  "s_add_u32 s50, s16, 16384\n" \
  "s_addc_u32 s51, s17, 0\n" \
  "global_load_dwordx4 v[228:231], v250, s[16:17] sc1\n" \
  "global_load_dwordx4 v[232:235], v250, s[50:51] sc1\n" \
  "s_waitcnt vmcnt(34)\n" \
  "s_cmp_lt_u32 s53, 8\n" \
  "s_cbranch_scc1 Loa2f_%=\n" \
  "s_waitcnt vmcnt(4)\n" \
  "Loa2f_%=:\n" \
  "v_mfma_f32_16x16x32_bf16 v[192:195], v[160:163], v[128:131], 0\n" \
  "v_mfma_f32_16x16x32_bf16 v[196:199], v[164:167], v[132:135], 0\n" \
  "v_mfma_f32_16x16x32_bf16 v[200:203], v[168:171], v[136:139], 0\n" \
  "v_mfma_f32_16x16x32_bf16 v[204:207], v[172:175], v[140:143], 0\n" \
  "v_add_f32 v208, 0x358637bd, v192\n" \
  "v_rcp_f32 v208, v208\n" \
  "v_add_f32 v209, 0x358637bd, v193\n" \
  "v_rcp_f32 v209, v209\n" \
  "v_lshlrev_b32 v210, 16, v128\n" \
  "v_and_b32 v211, 0xffff0000, v128\n" \
  "v_mul_f32 v210, v210, v208\n" \
  "v_mul_f32 v211, v211, v208\n" \
  "v_cvt_pk_bf16_f32 v128, v210, v211\n" \
  "v_lshlrev_b32 v210, 16, v129\n" \
  "v_and_b32 v211, 0xffff0000, v129\n" \
  "v_mul_f32 v210, v210, v208\n" \
  "v_mul_f32 v211, v211, v208\n" \
  "v_cvt_pk_bf16_f32 v129, v210, v211\n" \
  "v_lshlrev_b32 v210, 16, v130\n" \
  "v_and_b32 v211, 0xffff0000, v130\n" \
  "v_mul_f32 v210, v210, v209\n" \
  "v_mul_f32 v211, v211, v209\n" \
  "v_cvt_pk_bf16_f32 v130, v210, v211\n" \
  "v_lshlrev_b32 v210, 16, v131\n" \
  "v_and_b32 v211, 0xffff0000, v131\n" \
  "v_mul_f32 v210, v210, v209\n" \
  "v_mul_f32 v211, v211, v209\n" \
  "v_cvt_pk_bf16_f32 v131, v210, v211\n" \
  "ds_write_b128 v251, v[128:131]\n" \
  "v_add_f32 v208, 0x358637bd, v196\n" \
  "v_rcp_f32 v208, v208\n" \
  "v_add_f32 v209, 0x358637bd, v197\n" \
  "v_rcp_f32 v209, v209\n" \
  "v_lshlrev_b32 v210, 16, v132\n" \
  "v_and_b32 v211, 0xffff0000, v132\n" \
  "v_mul_f32 v210, v210, v208\n" \
  "v_mul_f32 v211, v211, v208\n" \
  "v_cvt_pk_bf16_f32 v132, v210, v211\n" \
  "v_lshlrev_b32 v210, 16, v133\n" \
  "v_and_b32 v211, 0xffff0000, v133\n" \
  "v_mul_f32 v210, v210, v208\n" \
  "v_mul_f32 v211, v211, v208\n" \
  "v_cvt_pk_bf16_f32 v133, v210, v211\n" \
  "v_lshlrev_b32 v210, 16, v134\n" \
  "v_and_b32 v211, 0xffff0000, v134\n" \
  "v_mul_f32 v210, v210, v209\n" \
  "v_mul_f32 v211, v211, v209\n" \
  "v_cvt_pk_bf16_f32 v134, v210, v211\n" \
  "v_lshlrev_b32 v210, 16, v135\n" \
  "v_and_b32 v211, 0xffff0000, v135\n" \
  "v_mul_f32 v210, v210, v209\n" \
  "v_mul_f32 v211, v211, v209\n" \
  "v_cvt_pk_bf16_f32 v135, v210, v211\n" \
  "ds_write_b128 v251, v[132:135] offset:1024\n" \
  "v_add_f32 v208, 0x358637bd, v200\n" \
  "v_rcp_f32 v208, v208\n" \
  "v_add_f32 v209, 0x358637bd, v201\n" \
  "v_rcp_f32 v209, v209\n" \
  "v_lshlrev_b32 v210, 16, v136\n" \
  "v_and_b32 v211, 0xffff0000, v136\n" \
  "v_mul_f32 v210, v210, v208\n" \
  "v_mul_f32 v211, v211, v208\n" \
  "v_cvt_pk_bf16_f32 v136, v210, v211\n" \
  "v_lshlrev_b32 v210, 16, v137\n" \
  "v_and_b32 v211, 0xffff0000, v137\n" \
  "v_mul_f32 v210, v210, v208\n" \
  "v_mul_f32 v211, v211, v208\n" \
  "v_cvt_pk_bf16_f32 v137, v210, v211\n" \
  "v_lshlrev_b32 v210, 16, v138\n" \
  "v_and_b32 v211, 0xffff0000, v138\n" \
  "v_mul_f32 v210, v210, v209\n" \
  "v_mul_f32 v211, v211, v209\n" \
  "v_cvt_pk_bf16_f32 v138, v210, v211\n" \
  "v_lshlrev_b32 v210, 16, v139\n" \
  "v_and_b32 v211, 0xffff0000, v139\n" \
  "v_mul_f32 v210, v210, v209\n" \
  "v_mul_f32 v211, v211, v209\n" \
  "v_cvt_pk_bf16_f32 v139, v210, v211\n" \
  "ds_write_b128 v251, v[136:139] offset:2048\n" \
  "v_add_f32 v208, 0x358637bd, v204\n" \
  "v_rcp_f32 v208, v208\n" \
  "v_add_f32 v209, 0x358637bd, v205\n" \
  "v_rcp_f32 v209, v209\n" \
  "v_lshlrev_b32 v210, 16, v140\n" \
  "v_and_b32 v211, 0xffff0000, v140\n" \
  "v_mul_f32 v210, v210, v208\n" \
  "v_mul_f32 v211, v211, v208\n" \
  "v_cvt_pk_bf16_f32 v140, v210, v211\n" \
  "v_lshlrev_b32 v210, 16, v141\n" \
  "v_and_b32 v211, 0xffff0000, v141\n" \
  "v_mul_f32 v210, v210, v208\n" \
  "v_mul_f32 v211, v211, v208\n" \
  "v_cvt_pk_bf16_f32 v141, v210, v211\n" \
  "v_lshlrev_b32 v210, 16, v142\n" \
  "v_and_b32 v211, 0xffff0000, v142\n" \
  "v_mul_f32 v210, v210, v209\n" \
  "v_mul_f32 v211, v211, v209\n" \
  "v_cvt_pk_bf16_f32 v142, v210, v211\n" \
  "v_lshlrev_b32 v210, 16, v143\n" \
  "v_and_b32 v211, 0xffff0000, v143\n" \
  "v_mul_f32 v210, v210, v209\n" \
  "v_mul_f32 v211, v211, v209\n" \
  "v_cvt_pk_bf16_f32 v143, v210, v211\n" \
  "ds_write_b128 v251, v[140:143] offset:3072\n" \
  "v_mfma_f32_16x16x32_bf16 v[192:195], v[160:163], v[144:147], 0\n" \
  "v_mfma_f32_16x16x32_bf16 v[196:199], v[164:167], v[148:151], 0\n" \
  "v_mfma_f32_16x16x32_bf16 v[200:203], v[168:171], v[152:155], 0\n" \
  "v_mfma_f32_16x16x32_bf16 v[204:207], v[172:175], v[156:159], 0\n" \
  "v_add_f32 v208, 0x358637bd, v192\n" \
  "v_rcp_f32 v208, v208\n" \
  "v_add_f32 v209, 0x358637bd, v193\n" \
  "v_rcp_f32 v209, v209\n" \
  "v_lshlrev_b32 v210, 16, v144\n" \
  "v_and_b32 v211, 0xffff0000, v144\n" \
  "v_mul_f32 v210, v210, v208\n" \
  "v_mul_f32 v211, v211, v208\n" \
  "v_cvt_pk_bf16_f32 v144, v210, v211\n" \
  "v_lshlrev_b32 v210, 16, v145\n" \
  "v_and_b32 v211, 0xffff0000, v145\n" \
  "v_mul_f32 v210, v210, v208\n" \
  "v_mul_f32 v211, v211, v208\n" \
  "v_cvt_pk_bf16_f32 v145, v210, v211\n" \
  "v_lshlrev_b32 v210, 16, v146\n" \
  "v_and_b32 v211, 0xffff0000, v146\n" \
  "v_mul_f32 v210, v210, v209\n" \
  "v_mul_f32 v211, v211, v209\n" \
  "v_cvt_pk_bf16_f32 v146, v210, v211\n" \
  "v_lshlrev_b32 v210, 16, v147\n" \
  "v_and_b32 v211, 0xffff0000, v147\n" \
  "v_mul_f32 v210, v210, v209\n" \
  "v_mul_f32 v211, v211, v209\n" \
  "v_cvt_pk_bf16_f32 v147, v210, v211\n" \
  "ds_write_b128 v251, v[144:147] offset:16384\n" \
  "v_add_f32 v208, 0x358637bd, v196\n" \
  "v_rcp_f32 v208, v208\n" \
  "v_add_f32 v209, 0x358637bd, v197\n" \
  "v_rcp_f32 v209, v209\n" \
  "v_lshlrev_b32 v210, 16, v148\n" \
  "v_and_b32 v211, 0xffff0000, v148\n" \
  "v_mul_f32 v210, v210, v208\n" \
  "v_mul_f32 v211, v211, v208\n" \
  "v_cvt_pk_bf16_f32 v148, v210, v211\n" \
  "v_lshlrev_b32 v210, 16, v149\n" \
  "v_and_b32 v211, 0xffff0000, v149\n" \
  "v_mul_f32 v210, v210, v208\n" \
  "v_mul_f32 v211, v211, v208\n" \
  "v_cvt_pk_bf16_f32 v149, v210, v211\n" \
  "v_lshlrev_b32 v210, 16, v150\n" \
  "v_and_b32 v211, 0xffff0000, v150\n" \
  "v_mul_f32 v210, v210, v209\n" \
  "v_mul_f32 v211, v211, v209\n" \
  "v_cvt_pk_bf16_f32 v150, v210, v211\n" \
  "v_lshlrev_b32 v210, 16, v151\n" \
  "v_and_b32 v211, 0xffff0000, v151\n" \
  "v_mul_f32 v210, v210, v209\n" \
  "v_mul_f32 v211, v211, v209\n" \
  "v_cvt_pk_bf16_f32 v151, v210, v211\n" \
  "ds_write_b128 v251, v[148:151] offset:17408\n" \
  "v_add_f32 v208, 0x358637bd, v200\n" \
  "v_rcp_f32 v208, v208\n" \
  "v_add_f32 v209, 0x358637bd, v201\n" \
  "v_rcp_f32 v209, v209\n" \
  "v_lshlrev_b32 v210, 16, v152\n" \
  "v_and_b32 v211, 0xffff0000, v152\n" \
  "v_mul_f32 v210, v210, v208\n" \
  "v_mul_f32 v211, v211, v208\n" \
  "v_cvt_pk_bf16_f32 v152, v210, v211\n" \
  "v_lshlrev_b32 v210, 16, v153\n" \
  "v_and_b32 v211, 0xffff0000, v153\n" \
  "v_mul_f32 v210, v210, v208\n" \
  "v_mul_f32 v211, v211, v208\n" \
  "v_cvt_pk_bf16_f32 v153, v210, v211\n" \
  "v_lshlrev_b32 v210, 16, v154\n" \
  "v_and_b32 v211, 0xffff0000, v154\n" \
  "v_mul_f32 v210, v210, v209\n" \
  "v_mul_f32 v211, v211, v209\n" \
  "v_cvt_pk_bf16_f32 v154, v210, v211\n" \
  "v_lshlrev_b32 v210, 16, v155\n" \
  "v_and_b32 v211, 0xffff0000, v155\n" \
  "v_mul_f32 v210, v210, v209\n" \
  "v_mul_f32 v211, v211, v209\n" \
  "v_cvt_pk_bf16_f32 v155, v210, v211\n" \
  "ds_write_b128 v251, v[152:155] offset:18432\n" \
  "v_add_f32 v208, 0x358637bd, v204\n" \
  "v_rcp_f32 v208, v208\n" \
  "v_add_f32 v209, 0x358637bd, v205\n" \
  "v_rcp_f32 v209, v209\n" \
  "v_lshlrev_b32 v210, 16, v156\n" \
  "v_and_b32 v211, 0xffff0000, v156\n" \
  "v_mul_f32 v210, v210, v208\n" \
  "v_mul_f32 v211, v211, v208\n" \
  "v_cvt_pk_bf16_f32 v156, v210, v211\n" \
  "v_lshlrev_b32 v210, 16, v157\n" \
  "v_and_b32 v211, 0xffff0000, v157\n" \
  "v_mul_f32 v210, v210, v208\n" \
  "v_mul_f32 v211, v211, v208\n" \
  "v_cvt_pk_bf16_f32 v157, v210, v211\n" \
  "v_lshlrev_b32 v210, 16, v158\n" \
  "v_and_b32 v211, 0xffff0000, v158\n" \
  "v_mul_f32 v210, v210, v209\n" \
  "v_mul_f32 v211, v211, v209\n" \
  "v_cvt_pk_bf16_f32 v158, v210, v211\n" \
  "v_lshlrev_b32 v210, 16, v159\n" \
  "v_and_b32 v211, 0xffff0000, v159\n" \
  "v_mul_f32 v210, v210, v209\n" \
  "v_mul_f32 v211, v211, v209\n" \
  "v_cvt_pk_bf16_f32 v159, v210, v211\n" \
  "ds_write_b128 v251, v[156:159] offset:19456\n" \
  "s_waitcnt vmcnt(0) lgkmcnt(0)\n" \
  "s_barrier\n" \
  "s_lshr_b32 s42, s53, 3\n" \
  "v_mov_b32 v249, s42\n" \
  "s_mov_b64 exec, s[60:61]\n" \
  "global_store_dword v255, v249, s[26:27] sc1\n" \
  "s_mov_b64 exec, -1\n" \
  "s_add_u32 s48, s18, 16384\n" \
  "s_addc_u32 s49, s19, 0\n" \
  "global_load_dwordx4 v[128:131], v240, s[18:19] sc1\n" \
  "global_load_dwordx4 v[132:135], v240, s[18:19] offset:1024 sc1\n" \
  "global_load_dwordx4 v[136:139], v240, s[18:19] offset:2048 sc1\n" \
  "global_load_dwordx4 v[140:143], v240, s[18:19] offset:3072 sc1\n" \
  "global_load_dwordx4 v[144:147], v240, s[48:49] sc1\n" \
  "global_load_dwordx4 v[148:151], v240, s[48:49] offset:1024 sc1\n" \
  "global_load_dwordx4 v[152:155], v240, s[48:49] offset:2048 sc1\n" \
  "global_load_dwordx4 v[156:159], v240, s[48:49] offset:3072 sc1\n" \
  "s_add_u32 s18, s18, 262144\n" \
  "s_addc_u32 s19, s19, 0\n" \
  "ds_read_b128 v[212:215], v240\n" \
  "ds_read_b128 v[216:219], v240 offset:16384\n" \
  "ds_read_b128 v[220:223], v240 offset:1024\n" \
  "s_waitcnt lgkmcnt(2)\n" \
  "v_mfma_f32_16x16x32_bf16 v[176:179], v[0:3], v[212:215], 0\n" \
  "ds_read_b128 v[224:227], v240 offset:17408\n" \
  "v_mfma_f32_16x16x32_bf16 v[180:183], v[4:7], v[212:215], 0\n" \
  "s_waitcnt lgkmcnt(2)\n" \
  "v_mfma_f32_16x16x32_bf16 v[184:187], v[0:3], v[216:219], 0\n" \
  "ds_read_b128 v[212:215], v240 offset:2048\n" \
  "v_mfma_f32_16x16x32_bf16 v[188:191], v[4:7], v[216:219], 0\n" \
  "s_waitcnt lgkmcnt(2)\n" \
  "v_mfma_f32_16x16x32_bf16 v[176:179], v[8:11], v[220:223], v[176:179]\n" \
  "ds_read_b128 v[216:219], v240 offset:18432\n" \
  "v_mfma_f32_16x16x32_bf16 v[180:183], v[12:15], v[220:223], v[180:183]\n" \
  "s_waitcnt lgkmcnt(2)\n" \
  "v_mfma_f32_16x16x32_bf16 v[184:187], v[8:11], v[224:227], v[184:187]\n" \
  "ds_read_b128 v[220:223], v240 offset:3072\n" \
  "v_mfma_f32_16x16x32_bf16 v[188:191], v[12:15], v[224:227], v[188:191]\n" \
  "s_waitcnt lgkmcnt(2)\n" \
  "v_mfma_f32_16x16x32_bf16 v[176:179], v[16:19], v[212:215], v[176:179]\n" \
  "ds_read_b128 v[224:227], v240 offset:19456\n" \
  "v_mfma_f32_16x16x32_bf16 v[180:183], v[20:23], v[212:215], v[180:183]\n" \
  "s_waitcnt lgkmcnt(2)\n" \
  "v_mfma_f32_16x16x32_bf16 v[184:187], v[16:19], v[216:219], v[184:187]\n" \
  "ds_read_b128 v[212:215], v240 offset:4096\n" \
  "v_mfma_f32_16x16x32_bf16 v[188:191], v[20:23], v[216:219], v[188:191]\n" \
  "s_waitcnt lgkmcnt(2)\n" \
  "v_mfma_f32_16x16x32_bf16 v[176:179], v[24:27], v[220:223], v[176:179]\n" \
  "ds_read_b128 v[216:219], v240 offset:20480\n" \
  "v_mfma_f32_16x16x32_bf16 v[180:183], v[28:31], v[220:223], v[180:183]\n" \
  "s_waitcnt lgkmcnt(2)\n" \
  "v_mfma_f32_16x16x32_bf16 v[184:187], v[24:27], v[224:227], v[184:187]\n" \
  "ds_read_b128 v[220:223], v240 offset:5120\n" \
  "v_mfma_f32_16x16x32_bf16 v[188:191], v[28:31], v[224:227], v[188:191]\n" \
  "s_waitcnt lgkmcnt(2)\n" \
  "v_mfma_f32_16x16x32_bf16 v[176:179], v[32:35], v[212:215], v[176:179]\n" \
  "ds_read_b128 v[224:227], v240 offset:21504\n" \
  "v_mfma_f32_16x16x32_bf16 v[180:183], v[36:39], v[212:215], v[180:183]\n" \
  "s_waitcnt lgkmcnt(2)\n" \
  "v_mfma_f32_16x16x32_bf16 v[184:187], v[32:35], v[216:219], v[184:187]\n" \
  "ds_read_b128 v[212:215], v240 offset:6144\n" \
  "v_mfma_f32_16x16x32_bf16 v[188:191], v[36:39], v[216:219], v[188:191]\n" \
  "s_waitcnt lgkmcnt(2)\n" \
  "v_mfma_f32_16x16x32_bf16 v[176:179], v[40:43], v[220:223], v[176:179]\n" \
  "ds_read_b128 v[216:219], v240 offset:22528\n" \
  "v_mfma_f32_16x16x32_bf16 v[180:183], v[44:47], v[220:223], v[180:183]\n" \
  "s_waitcnt lgkmcnt(2)\n" \
  "v_mfma_f32_16x16x32_bf16 v[184:187], v[40:43], v[224:227], v[184:187]\n" \
  "ds_read_b128 v[220:223], v240 offset:7168\n" \
  "v_mfma_f32_16x16x32_bf16 v[188:191], v[44:47], v[224:227], v[188:191]\n" \
  "s_waitcnt lgkmcnt(2)\n" \
  "v_mfma_f32_16x16x32_bf16 v[176:179], v[48:51], v[212:215], v[176:179]\n" \
  "ds_read_b128 v[224:227], v240 offset:23552\n" \
  "v_mfma_f32_16x16x32_bf16 v[180:183], v[52:55], v[212:215], v[180:183]\n" \
  "s_waitcnt lgkmcnt(2)\n" \
  "v_mfma_f32_16x16x32_bf16 v[184:187], v[48:51], v[216:219], v[184:187]\n" \
  "ds_read_b128 v[212:215], v240 offset:8192\n" \
  "v_mfma_f32_16x16x32_bf16 v[188:191], v[52:55], v[216:219], v[188:191]\n" \
  "s_waitcnt lgkmcnt(2)\n" \
  "v_mfma_f32_16x16x32_bf16 v[176:179], v[56:59], v[220:223], v[176:179]\n" \
  "ds_read_b128 v[216:219], v240 offset:24576\n" \
  "v_mfma_f32_16x16x32_bf16 v[180:183], v[60:63], v[220:223], v[180:183]\n" \
  "s_waitcnt lgkmcnt(2)\n" \
  "v_mfma_f32_16x16x32_bf16 v[184:187], v[56:59], v[224:227], v[184:187]\n" \
  "ds_read_b128 v[220:223], v240 offset:9216\n" \
  "v_mfma_f32_16x16x32_bf16 v[188:191], v[60:63], v[224:227], v[188:191]\n" \
  "s_waitcnt lgkmcnt(2)\n" \
  "v_mfma_f32_16x16x32_bf16 v[176:179], v[64:67], v[212:215], v[176:179]\n" \
  "ds_read_b128 v[224:227], v240 offset:25600\n" \
  "v_mfma_f32_16x16x32_bf16 v[180:183], v[68:71], v[212:215], v[180:183]\n" \
  "s_waitcnt lgkmcnt(2)\n" \
  "v_mfma_f32_16x16x32_bf16 v[184:187], v[64:67], v[216:219], v[184:187]\n" \
  "ds_read_b128 v[212:215], v240 offset:10240\n" \
  "v_mfma_f32_16x16x32_bf16 v[188:191], v[68:71], v[216:219], v[188:191]\n" \
  "s_waitcnt lgkmcnt(2)\n" \
  "v_mfma_f32_16x16x32_bf16 v[176:179], v[72:75], v[220:223], v[176:179]\n" \
  "ds_read_b128 v[216:219], v240 offset:26624\n" \
  "v_mfma_f32_16x16x32_bf16 v[180:183], v[76:79], v[220:223], v[180:183]\n" \
  "s_waitcnt lgkmcnt(2)\n" \
  "v_mfma_f32_16x16x32_bf16 v[184:187], v[72:75], v[224:227], v[184:187]\n" \
  "ds_read_b128 v[220:223], v240 offset:11264\n" \
  "v_mfma_f32_16x16x32_bf16 v[188:191], v[76:79], v[224:227], v[188:191]\n" \
  "s_waitcnt lgkmcnt(2)\n" \
  "v_mfma_f32_16x16x32_bf16 v[176:179], v[80:83], v[212:215], v[176:179]\n" \
  "ds_read_b128 v[224:227], v240 offset:27648\n" \
  "v_mfma_f32_16x16x32_bf16 v[180:183], v[84:87], v[212:215], v[180:183]\n" \
  "s_waitcnt lgkmcnt(2)\n" \
  "v_mfma_f32_16x16x32_bf16 v[184:187], v[80:83], v[216:219], v[184:187]\n" \
  "ds_read_b128 v[212:215], v240 offset:12288\n" \
  "v_mfma_f32_16x16x32_bf16 v[188:191], v[84:87], v[216:219], v[188:191]\n" \
  "s_waitcnt lgkmcnt(2)\n" \
  "v_mfma_f32_16x16x32_bf16 v[176:179], v[88:91], v[220:223], v[176:179]\n" \
  "ds_read_b128 v[216:219], v240 offset:28672\n" \
  "v_mfma_f32_16x16x32_bf16 v[180:183], v[92:95], v[220:223], v[180:183]\n" \
  "s_waitcnt lgkmcnt(2)\n" \
  "v_mfma_f32_16x16x32_bf16 v[184:187], v[88:91], v[224:227], v[184:187]\n" \
  "ds_read_b128 v[220:223], v240 offset:13312\n" \
  "v_mfma_f32_16x16x32_bf16 v[188:191], v[92:95], v[224:227], v[188:191]\n" \
  "s_waitcnt lgkmcnt(2)\n" \
  "v_mfma_f32_16x16x32_bf16 v[176:179], v[96:99], v[212:215], v[176:179]\n" \
  "ds_read_b128 v[224:227], v240 offset:29696\n" \
  "v_mfma_f32_16x16x32_bf16 v[180:183], v[100:103], v[212:215], v[180:183]\n" \
  "s_waitcnt lgkmcnt(2)\n" \
  "v_mfma_f32_16x16x32_bf16 v[184:187], v[96:99], v[216:219], v[184:187]\n" \
  "ds_read_b128 v[212:215], v240 offset:14336\n" \
  "v_mfma_f32_16x16x32_bf16 v[188:191], v[100:103], v[216:219], v[188:191]\n" \
  "s_waitcnt lgkmcnt(2)\n" \
  "v_mfma_f32_16x16x32_bf16 v[176:179], v[104:107], v[220:223], v[176:179]\n" \
  "ds_read_b128 v[216:219], v240 offset:30720\n" \
  "v_mfma_f32_16x16x32_bf16 v[180:183], v[108:111], v[220:223], v[180:183]\n" \
  "s_waitcnt lgkmcnt(2)\n" \
  "v_mfma_f32_16x16x32_bf16 v[184:187], v[104:107], v[224:227], v[184:187]\n" \
  "ds_read_b128 v[220:223], v240 offset:15360\n" \
  "v_mfma_f32_16x16x32_bf16 v[188:191], v[108:111], v[224:227], v[188:191]\n" \
  "s_waitcnt lgkmcnt(2)\n" \
  "v_mfma_f32_16x16x32_bf16 v[176:179], v[112:115], v[212:215], v[176:179]\n" \
  "ds_read_b128 v[224:227], v240 offset:31744\n" \
  "v_mfma_f32_16x16x32_bf16 v[180:183], v[116:119], v[212:215], v[180:183]\n" \
  "s_waitcnt lgkmcnt(2)\n" \
  "v_mfma_f32_16x16x32_bf16 v[184:187], v[112:115], v[216:219], v[184:187]\n" \
  "v_mfma_f32_16x16x32_bf16 v[188:191], v[116:119], v[216:219], v[188:191]\n" \
  "s_waitcnt lgkmcnt(1)\n" \
  "v_mfma_f32_16x16x32_bf16 v[176:179], v[120:123], v[220:223], v[176:179]\n" \
  "v_mfma_f32_16x16x32_bf16 v[180:183], v[124:127], v[220:223], v[180:183]\n" \
  "s_waitcnt lgkmcnt(0)\n" \
  "v_mfma_f32_16x16x32_bf16 v[184:187], v[120:123], v[224:227], v[184:187]\n" \
  "v_mfma_f32_16x16x32_bf16 v[188:191], v[124:127], v[224:227], v[188:191]\n" \
  "s_add_u32 s50, s54, 16384\n" \
  "s_addc_u32 s51, s55, 0\n" \
  "v_cvt_pk_bf16_f32 v236, v176, v177\n" \
  "v_cvt_pk_bf16_f32 v237, v178, v179\n" \
  "v_cvt_pk_bf16_f32 v238, v180, v181\n" \
  "v_cvt_pk_bf16_f32 v239, v182, v183\n" \
  "v_lshlrev_b32 v210, 16, v236\n" \
  "v_and_b32 v211, 0xffff0000, v236\n" \
  "v_lshlrev_b32 v192, 16, v228\n" \
  "v_and_b32 v193, 0xffff0000, v228\n" \
  "v_add_f32 v210, v210, v192\n" \
  "v_add_f32 v211, v211, v193\n" \
  "v_cvt_pk_bf16_f32 v228, v210, v211\n" \
  "v_lshlrev_b32 v210, 16, v228\n" \
  "v_and_b32 v211, 0xffff0000, v228\n" \
  "v_add_f32 v208, v210, v211\n" \
  "v_mul_f32 v209, v210, v210\n" \
  "v_fmac_f32 v209, v211, v211\n" \
  "v_lshlrev_b32 v210, 16, v237\n" \
  "v_and_b32 v211, 0xffff0000, v237\n" \
  "v_lshlrev_b32 v192, 16, v229\n" \
  "v_and_b32 v193, 0xffff0000, v229\n" \
  "v_add_f32 v210, v210, v192\n" \
  "v_add_f32 v211, v211, v193\n" \
  "v_cvt_pk_bf16_f32 v229, v210, v211\n" \
  "v_lshlrev_b32 v210, 16, v229\n" \
  "v_and_b32 v211, 0xffff0000, v229\n" \
  "v_add_f32 v208, v208, v210\n" \
  "v_add_f32 v208, v208, v211\n" \
  "v_fmac_f32 v209, v210, v210\n" \
  "v_fmac_f32 v209, v211, v211\n" \
  "v_lshlrev_b32 v210, 16, v238\n" \
  "v_and_b32 v211, 0xffff0000, v238\n" \
  "v_lshlrev_b32 v192, 16, v230\n" \
  "v_and_b32 v193, 0xffff0000, v230\n" \
  "v_add_f32 v210, v210, v192\n" \
  "v_add_f32 v211, v211, v193\n" \
  "v_cvt_pk_bf16_f32 v230, v210, v211\n" \
  "v_lshlrev_b32 v210, 16, v230\n" \
  "v_and_b32 v211, 0xffff0000, v230\n" \
  "v_add_f32 v208, v208, v210\n" \
  "v_add_f32 v208, v208, v211\n" \
  "v_fmac_f32 v209, v210, v210\n" \
  "v_fmac_f32 v209, v211, v211\n" \
  "v_lshlrev_b32 v210, 16, v239\n" \
  "v_and_b32 v211, 0xffff0000, v239\n" \
  "v_lshlrev_b32 v192, 16, v231\n" \
  "v_and_b32 v193, 0xffff0000, v231\n" \
  "v_add_f32 v210, v210, v192\n" \
  "v_add_f32 v211, v211, v193\n" \
  "v_cvt_pk_bf16_f32 v231, v210, v211\n" \
  "v_lshlrev_b32 v210, 16, v231\n" \
  "v_and_b32 v211, 0xffff0000, v231\n" \
  "v_add_f32 v208, v208, v210\n" \
  "v_add_f32 v208, v208, v211\n" \
  "v_fmac_f32 v209, v210, v210\n" \
  "v_fmac_f32 v209, v211, v211\n" \
  "global_store_dwordx4 v250, v[228:231], s[54:55] sc1\n" \
  "v_mov_b32 v245, v208\n" \
  "s_nop 1\n" \
  "v_permlane16_swap_b32 v208, v245\n" \
  "v_add_f32 v208, v208, v245\n" \
  "v_mov_b32 v245, v208\n" \
  "s_nop 1\n" \
  "v_permlane32_swap_b32 v208, v245\n" \
  "v_add_f32 v208, v208, v245\n" \
  "v_mov_b32 v245, v209\n" \
  "s_nop 1\n" \
  "v_permlane16_swap_b32 v209, v245\n" \
  "v_add_f32 v209, v209, v245\n" \
  "v_mov_b32 v245, v209\n" \
  "s_nop 1\n" \
  "v_permlane32_swap_b32 v209, v245\n" \
  "v_add_f32 v209, v209, v245\n" \
  "ds_write_b64 v252, v[208:209]\n" \
  "v_cvt_pk_bf16_f32 v236, v184, v185\n" \
  "v_cvt_pk_bf16_f32 v237, v186, v187\n" \
  "v_cvt_pk_bf16_f32 v238, v188, v189\n" \
  "v_cvt_pk_bf16_f32 v239, v190, v191\n" \
  "v_lshlrev_b32 v210, 16, v236\n" \
  "v_and_b32 v211, 0xffff0000, v236\n" \
  "v_lshlrev_b32 v192, 16, v232\n" \
  "v_and_b32 v193, 0xffff0000, v232\n" \
  "v_add_f32 v210, v210, v192\n" \
  "v_add_f32 v211, v211, v193\n" \
  "v_cvt_pk_bf16_f32 v232, v210, v211\n" \
  "v_lshlrev_b32 v210, 16, v232\n" \
  "v_and_b32 v211, 0xffff0000, v232\n" \
  "v_add_f32 v208, v210, v211\n" \
  "v_mul_f32 v209, v210, v210\n" \
  "v_fmac_f32 v209, v211, v211\n" \
  "v_lshlrev_b32 v210, 16, v237\n" \
  "v_and_b32 v211, 0xffff0000, v237\n" \
  "v_lshlrev_b32 v192, 16, v233\n" \
  "v_and_b32 v193, 0xffff0000, v233\n" \
  "v_add_f32 v210, v210, v192\n" \
  "v_add_f32 v211, v211, v193\n" \
  "v_cvt_pk_bf16_f32 v233, v210, v211\n" \
  "v_lshlrev_b32 v210, 16, v233\n" \
  "v_and_b32 v211, 0xffff0000, v233\n" \
  "v_add_f32 v208, v208, v210\n" \
  "v_add_f32 v208, v208, v211\n" \
  "v_fmac_f32 v209, v210, v210\n" \
  "v_fmac_f32 v209, v211, v211\n" \
  "v_lshlrev_b32 v210, 16, v238\n" \
  "v_and_b32 v211, 0xffff0000, v238\n" \
  "v_lshlrev_b32 v192, 16, v234\n" \
  "v_and_b32 v193, 0xffff0000, v234\n" \
  "v_add_f32 v210, v210, v192\n" \
  "v_add_f32 v211, v211, v193\n" \
  "v_cvt_pk_bf16_f32 v234, v210, v211\n" \
  "v_lshlrev_b32 v210, 16, v234\n" \
  "v_and_b32 v211, 0xffff0000, v234\n" \
  "v_add_f32 v208, v208, v210\n" \
  "v_add_f32 v208, v208, v211\n" \
  "v_fmac_f32 v209, v210, v210\n" \
  "v_fmac_f32 v209, v211, v211\n" \
  "v_lshlrev_b32 v210, 16, v239\n" \
  "v_and_b32 v211, 0xffff0000, v239\n" \
  "v_lshlrev_b32 v192, 16, v235\n" \
  "v_and_b32 v193, 0xffff0000, v235\n" \
  "v_add_f32 v210, v210, v192\n" \
  "v_add_f32 v211, v211, v193\n" \
  "v_cvt_pk_bf16_f32 v235, v210, v211\n" \
  "v_lshlrev_b32 v210, 16, v235\n" \
  "v_and_b32 v211, 0xffff0000, v235\n" \
  "v_add_f32 v208, v208, v210\n" \
  "v_add_f32 v208, v208, v211\n" \
  "v_fmac_f32 v209, v210, v210\n" \
  "v_fmac_f32 v209, v211, v211\n" \
  "global_store_dwordx4 v250, v[232:235], s[50:51] sc1\n" \
  "v_mov_b32 v245, v208\n" \
  "s_nop 1\n" \
  "v_permlane16_swap_b32 v208, v245\n" \
  "v_add_f32 v208, v208, v245\n" \
  "v_mov_b32 v245, v208\n" \
  "s_nop 1\n" \
  "v_permlane32_swap_b32 v208, v245\n" \
  "v_add_f32 v208, v208, v245\n" \
  "v_mov_b32 v245, v209\n" \
  "s_nop 1\n" \
  "v_permlane16_swap_b32 v209, v245\n" \
  "v_add_f32 v209, v209, v245\n" \
  "v_mov_b32 v245, v209\n" \
  "s_nop 1\n" \
  "v_permlane32_swap_b32 v209, v245\n" \
  "v_add_f32 v209, v209, v245\n" \
  "ds_write_b64 v252, v[208:209] offset:128\n" \
  "s_waitcnt lgkmcnt(0)\n" \
  "s_barrier\n" \
  "s_cmp_eq_u32 s40, 0\n" \
  "s_cbranch_scc0 Loa2x_%=\n" \
  "ds_read_b64 v[192:193], v253\n" \
  "ds_read_b64 v[196:197], v253 offset:256\n" \
  "ds_read_b64 v[200:201], v253 offset:512\n" \
  "ds_read_b64 v[204:205], v253 offset:768\n" \
  "s_waitcnt lgkmcnt(0)\n" \
  "v_add_f32 v192, v192, v196\n" \
  "v_add_f32 v200, v200, v204\n" \
  "v_add_f32 v192, v192, v200\n" \
  "v_add_f32 v193, v193, v197\n" \
  "v_add_f32 v201, v201, v205\n" \
  "v_add_f32 v193, v193, v201\n" \
  "s_mov_b64 exec, 0xffffffff\n" \
  "global_store_dwordx2 v248, v[192:193], s[28:29] sc1\n" \
  "s_mov_b64 exec, -1\n" \
  "Loa2x_%=:\n" \
  "s_cmp_lt_u32 s53, 8\n" \
  "s_cbranch_scc0 Loa2y_%=\n" \
  "s_waitcnt vmcnt(0)\n" \
  "s_barrier\n" \
  "v_mov_b32 v249, 1\n" \
  "s_mov_b64 exec, s[60:61]\n" \
  "global_store_dword v255, v249, s[26:27] sc1\n" \
  "s_mov_b64 exec, -1\n" \
  "Loa2y_%=:\n" \
  "s_add_u32 s16, s16, 262144\n" \
  "s_addc_u32 s17, s17, 0\n" \
  "s_add_u32 s54, s54, 262144\n" \
  "s_addc_u32 s55, s55, 0\n" \
  "s_add_u32 s28, s28, 8192\n" \
  "s_addc_u32 s29, s29, 0\n" \
  "s_add_u32 s53, s53, 8\n" \
  "s_cmp_lt_u32 s53, 64\n" \
  "s_cbranch_scc1 Loa2_%=\n" \
  "s_waitcnt vmcnt(0)\n" \
  "s_barrier\n" \
  "v_mov_b32 v249, 8\n" \
  "s_mov_b64 exec, s[60:61]\n" \
  "global_store_dword v255, v249, s[26:27] sc1\n" \
  "s_mov_b64 exec, -1\n" \
  "s_waitcnt vmcnt(0)\n" \
  "s_nop 7\n" \
  "s_nop 1\n" \
  "s_cmp_eq_u32 s69, 1\n" \
  "s_cbranch_scc1 Lohave_%=\n" \
  "s_lshl_b32 s42, s40, 20\n" \
  "s_add_u32 s48, s4, s42\n" \
  "s_addc_u32 s49, s5, 0\n" \
  "s_lshl_b32 s44, s40, 14\n" \
  "s_add_u32 s44, s44, 16384\n" \
  "v_lshrrev_b32 v201, 2, v254\n" \
  "v_and_b32 v200, 3, v254\n" \
  "v_lshlrev_b32 v200, 4, v200\n" \
  "v_mul_u32_u24 v201, 1024, v201\n" \
  "v_add_u32 v200, v200, v201\n" \
  "v_lshrrev_b32 v201, 4, v254\n" \
  "v_lshlrev_b32 v201, 2, v201\n" \
  "v_bfe_u32 v202, v254, 2, 2\n" \
  "v_add_u32 v201, v201, v202\n" \
  "v_mul_u32_u24 v201, 64, v201\n" \
  "v_and_b32 v202, 3, v254\n" \
  "v_lshl_add_u32 v201, v202, 4, v201\n" \
  "v_add_u32 v201, s44, v201\n" \
  "s_add_u32 m0, s44, 0\n" \
  "s_nop 0\n" \
  "global_load_lds_dwordx4 v200, s[48:49]\n" \
  "s_add_u32 s48, s48, 16384\n" \
  "s_addc_u32 s49, s49, 0\n" \
  "s_add_u32 m0, s44, 1024\n" \
  "s_nop 0\n" \
  "global_load_lds_dwordx4 v200, s[48:49]\n" \
  "s_add_u32 s48, s48, 16384\n" \
  "s_addc_u32 s49, s49, 0\n" \
  "s_add_u32 m0, s44, 2048\n" \
  "s_nop 0\n" \
  "global_load_lds_dwordx4 v200, s[48:49]\n" \
  "s_add_u32 s48, s48, 16384\n" \
  "s_addc_u32 s49, s49, 0\n" \
  "s_add_u32 m0, s44, 3072\n" \
  "s_nop 0\n" \
  "global_load_lds_dwordx4 v200, s[48:49]\n" \
  "s_add_u32 s48, s48, 16384\n" \
  "s_addc_u32 s49, s49, 0\n" \
  "s_add_u32 m0, s44, 4096\n" \
  "s_nop 0\n" \
  "global_load_lds_dwordx4 v200, s[48:49]\n" \
  "s_add_u32 s48, s48, 16384\n" \
  "s_addc_u32 s49, s49, 0\n" \
  "s_add_u32 m0, s44, 5120\n" \
  "s_nop 0\n" \
  "global_load_lds_dwordx4 v200, s[48:49]\n" \
  "s_add_u32 s48, s48, 16384\n" \
  "s_addc_u32 s49, s49, 0\n" \
  "s_add_u32 m0, s44, 6144\n" \
  "s_nop 0\n" \
  "global_load_lds_dwordx4 v200, s[48:49]\n" \
  "s_add_u32 s48, s48, 16384\n" \
  "s_addc_u32 s49, s49, 0\n" \
  "s_add_u32 m0, s44, 7168\n" \
  "s_nop 0\n" \
  "global_load_lds_dwordx4 v200, s[48:49]\n" \
  "s_add_u32 s48, s48, 16384\n" \
  "s_addc_u32 s49, s49, 0\n" \
  "s_add_u32 m0, s44, 8192\n" \
  "s_nop 0\n" \
  "global_load_lds_dwordx4 v200, s[48:49]\n" \
  "s_add_u32 s48, s48, 16384\n" \
  "s_addc_u32 s49, s49, 0\n" \
  "s_add_u32 m0, s44, 9216\n" \
  "s_nop 0\n" \
  "global_load_lds_dwordx4 v200, s[48:49]\n" \
  "s_add_u32 s48, s48, 16384\n" \
  "s_addc_u32 s49, s49, 0\n" \
  "s_add_u32 m0, s44, 10240\n" \
  "s_nop 0\n" \
  "global_load_lds_dwordx4 v200, s[48:49]\n" \
  "s_add_u32 s48, s48, 16384\n" \
  "s_addc_u32 s49, s49, 0\n" \
  "s_add_u32 m0, s44, 11264\n" \
  "s_nop 0\n" \
  "global_load_lds_dwordx4 v200, s[48:49]\n" \
  "s_add_u32 s48, s48, 16384\n" \
  "s_addc_u32 s49, s49, 0\n" \
  "s_waitcnt vmcnt(8)\n" \
  "ds_read_b64_tr_b16 a[0:1], v201\n" \
  "ds_read_b64_tr_b16 a[2:3], v201 offset:1024\n" \
  "ds_read_b64_tr_b16 a[4:5], v201 offset:8\n" \
  "ds_read_b64_tr_b16 a[6:7], v201 offset:1032\n" \
  "ds_read_b64_tr_b16 a[8:9], v201 offset:2048\n" \
  "ds_read_b64_tr_b16 a[10:11], v201 offset:3072\n" \
  "ds_read_b64_tr_b16 a[12:13], v201 offset:2056\n" \
  "ds_read_b64_tr_b16 a[14:15], v201 offset:3080\n" \
  "s_add_u32 m0, s44, 12288\n" \
  "s_nop 0\n" \
  "global_load_lds_dwordx4 v200, s[48:49]\n" \
  "s_add_u32 s48, s48, 16384\n" \
  "s_addc_u32 s49, s49, 0\n" \
  "s_add_u32 m0, s44, 13312\n" \
  "s_nop 0\n" \
  "global_load_lds_dwordx4 v200, s[48:49]\n" \
  "s_add_u32 s48, s48, 16384\n" \
  "s_addc_u32 s49, s49, 0\n" \
  "s_add_u32 m0, s44, 14336\n" \
  "s_nop 0\n" \
  "global_load_lds_dwordx4 v200, s[48:49]\n" \
  "s_add_u32 s48, s48, 16384\n" \
  "s_addc_u32 s49, s49, 0\n" \
  "s_add_u32 m0, s44, 15360\n" \
  "s_nop 0\n" \
  "global_load_lds_dwordx4 v200, s[48:49]\n" \
  "s_add_u32 s48, s48, 16384\n" \
  "s_addc_u32 s49, s49, 0\n" \
  "s_waitcnt lgkmcnt(0)\n" \
  "s_waitcnt vmcnt(8)\n" \
  "ds_read_b64_tr_b16 a[16:17], v201 offset:4096\n" \
  "ds_read_b64_tr_b16 a[18:19], v201 offset:5120\n" \
  "ds_read_b64_tr_b16 a[20:21], v201 offset:4104\n" \
  "ds_read_b64_tr_b16 a[22:23], v201 offset:5128\n" \
  "ds_read_b64_tr_b16 a[24:25], v201 offset:6144\n" \
  "ds_read_b64_tr_b16 a[26:27], v201 offset:7168\n" \
  "ds_read_b64_tr_b16 a[28:29], v201 offset:6152\n" \
  "ds_read_b64_tr_b16 a[30:31], v201 offset:7176\n" \
  "s_add_u32 m0, s44, 0\n" \
  "s_nop 0\n" \
  "global_load_lds_dwordx4 v200, s[48:49]\n" \
  "s_add_u32 s48, s48, 16384\n" \
  "s_addc_u32 s49, s49, 0\n" \
  "s_add_u32 m0, s44, 1024\n" \
  "s_nop 0\n" \
  "global_load_lds_dwordx4 v200, s[48:49]\n" \
  "s_add_u32 s48, s48, 16384\n" \
  "s_addc_u32 s49, s49, 0\n" \
  "s_add_u32 m0, s44, 2048\n" \
  "s_nop 0\n" \
  "global_load_lds_dwordx4 v200, s[48:49]\n" \
  "s_add_u32 s48, s48, 16384\n" \
  "s_addc_u32 s49, s49, 0\n" \
  "s_add_u32 m0, s44, 3072\n" \
  "s_nop 0\n" \
  "global_load_lds_dwordx4 v200, s[48:49]\n" \
  "s_add_u32 s48, s48, 16384\n" \
  "s_addc_u32 s49, s49, 0\n" \
  "s_waitcnt lgkmcnt(0)\n" \
  "s_waitcnt vmcnt(8)\n" \
  "ds_read_b64_tr_b16 a[32:33], v201 offset:8192\n" \
  "ds_read_b64_tr_b16 a[34:35], v201 offset:9216\n" \
  "ds_read_b64_tr_b16 a[36:37], v201 offset:8200\n" \
  "ds_read_b64_tr_b16 a[38:39], v201 offset:9224\n" \
  "ds_read_b64_tr_b16 a[40:41], v201 offset:10240\n" \
  "ds_read_b64_tr_b16 a[42:43], v201 offset:11264\n" \
  "ds_read_b64_tr_b16 a[44:45], v201 offset:10248\n" \
  "ds_read_b64_tr_b16 a[46:47], v201 offset:11272\n" \
  "s_add_u32 m0, s44, 4096\n" \
  "s_nop 0\n" \
  "global_load_lds_dwordx4 v200, s[48:49]\n" \
  "s_add_u32 s48, s48, 16384\n" \
  "s_addc_u32 s49, s49, 0\n" \
  "s_add_u32 m0, s44, 5120\n" \
  "s_nop 0\n" \
  "global_load_lds_dwordx4 v200, s[48:49]\n" \
  "s_add_u32 s48, s48, 16384\n" \
  "s_addc_u32 s49, s49, 0\n" \
  "s_add_u32 m0, s44, 6144\n" \
  "s_nop 0\n" \
  "global_load_lds_dwordx4 v200, s[48:49]\n" \
  "s_add_u32 s48, s48, 16384\n" \
  "s_addc_u32 s49, s49, 0\n" \
  "s_add_u32 m0, s44, 7168\n" \
  "s_nop 0\n" \
  "global_load_lds_dwordx4 v200, s[48:49]\n" \
  "s_add_u32 s48, s48, 16384\n" \
  "s_addc_u32 s49, s49, 0\n" \
  "s_waitcnt lgkmcnt(0)\n" \
  "s_waitcnt vmcnt(8)\n" \
  "ds_read_b64_tr_b16 a[48:49], v201 offset:12288\n" \
  "ds_read_b64_tr_b16 a[50:51], v201 offset:13312\n" \
  "ds_read_b64_tr_b16 a[52:53], v201 offset:12296\n" \
  "ds_read_b64_tr_b16 a[54:55], v201 offset:13320\n" \
  "ds_read_b64_tr_b16 a[56:57], v201 offset:14336\n" \
  "ds_read_b64_tr_b16 a[58:59], v201 offset:15360\n" \
  "ds_read_b64_tr_b16 a[60:61], v201 offset:14344\n" \
  "ds_read_b64_tr_b16 a[62:63], v201 offset:15368\n" \
  "s_add_u32 m0, s44, 8192\n" \
  "s_nop 0\n" \
  "global_load_lds_dwordx4 v200, s[48:49]\n" \
  "s_add_u32 s48, s48, 16384\n" \
  "s_addc_u32 s49, s49, 0\n" \
  "s_add_u32 m0, s44, 9216\n" \
  "s_nop 0\n" \
  "global_load_lds_dwordx4 v200, s[48:49]\n" \
  "s_add_u32 s48, s48, 16384\n" \
  "s_addc_u32 s49, s49, 0\n" \
  "s_add_u32 m0, s44, 10240\n" \
  "s_nop 0\n" \
  "global_load_lds_dwordx4 v200, s[48:49]\n" \
  "s_add_u32 s48, s48, 16384\n" \
  "s_addc_u32 s49, s49, 0\n" \
  "s_add_u32 m0, s44, 11264\n" \
  "s_nop 0\n" \
  "global_load_lds_dwordx4 v200, s[48:49]\n" \
  "s_add_u32 s48, s48, 16384\n" \
  "s_addc_u32 s49, s49, 0\n" \
  "s_waitcnt lgkmcnt(0)\n" \
  "s_waitcnt vmcnt(8)\n" \
  "ds_read_b64_tr_b16 a[64:65], v201\n" \
  "ds_read_b64_tr_b16 a[66:67], v201 offset:1024\n" \
  "ds_read_b64_tr_b16 a[68:69], v201 offset:8\n" \
  "ds_read_b64_tr_b16 a[70:71], v201 offset:1032\n" \
  "ds_read_b64_tr_b16 a[72:73], v201 offset:2048\n" \
  "ds_read_b64_tr_b16 a[74:75], v201 offset:3072\n" \
  "ds_read_b64_tr_b16 a[76:77], v201 offset:2056\n" \
  "ds_read_b64_tr_b16 a[78:79], v201 offset:3080\n" \
  "s_add_u32 m0, s44, 12288\n" \
  "s_nop 0\n" \
  "global_load_lds_dwordx4 v200, s[48:49]\n" \
  "s_add_u32 s48, s48, 16384\n" \
  "s_addc_u32 s49, s49, 0\n" \
  "s_add_u32 m0, s44, 13312\n" \
  "s_nop 0\n" \
  "global_load_lds_dwordx4 v200, s[48:49]\n" \
  "s_add_u32 s48, s48, 16384\n" \
  "s_addc_u32 s49, s49, 0\n" \
  "s_add_u32 m0, s44, 14336\n" \
  "s_nop 0\n" \
  "global_load_lds_dwordx4 v200, s[48:49]\n" \
  "s_add_u32 s48, s48, 16384\n" \
  "s_addc_u32 s49, s49, 0\n" \
  "s_add_u32 m0, s44, 15360\n" \
  "s_nop 0\n" \
  "global_load_lds_dwordx4 v200, s[48:49]\n" \
  "s_add_u32 s48, s48, 16384\n" \
  "s_addc_u32 s49, s49, 0\n" \
  "s_waitcnt lgkmcnt(0)\n" \
  "s_waitcnt vmcnt(8)\n" \
  "ds_read_b64_tr_b16 a[80:81], v201 offset:4096\n" \
  "ds_read_b64_tr_b16 a[82:83], v201 offset:5120\n" \
  "ds_read_b64_tr_b16 a[84:85], v201 offset:4104\n" \
  "ds_read_b64_tr_b16 a[86:87], v201 offset:5128\n" \
  "ds_read_b64_tr_b16 a[88:89], v201 offset:6144\n" \
  "ds_read_b64_tr_b16 a[90:91], v201 offset:7168\n" \
  "ds_read_b64_tr_b16 a[92:93], v201 offset:6152\n" \
  "ds_read_b64_tr_b16 a[94:95], v201 offset:7176\n" \
  "s_add_u32 m0, s44, 0\n" \
  "s_nop 0\n" \
  "global_load_lds_dwordx4 v200, s[48:49]\n" \
  "s_add_u32 s48, s48, 16384\n" \
  "s_addc_u32 s49, s49, 0\n" \
  "s_add_u32 m0, s44, 1024\n" \
  "s_nop 0\n" \
  "global_load_lds_dwordx4 v200, s[48:49]\n" \
  "s_add_u32 s48, s48, 16384\n" \
  "s_addc_u32 s49, s49, 0\n" \
  "s_add_u32 m0, s44, 2048\n" \
  "s_nop 0\n" \
  "global_load_lds_dwordx4 v200, s[48:49]\n" \
  "s_add_u32 s48, s48, 16384\n" \
  "s_addc_u32 s49, s49, 0\n" \
  "s_add_u32 m0, s44, 3072\n" \
  "s_nop 0\n" \
  "global_load_lds_dwordx4 v200, s[48:49]\n" \
  "s_add_u32 s48, s48, 16384\n" \
  "s_addc_u32 s49, s49, 0\n" \
  "s_waitcnt lgkmcnt(0)\n" \
  "s_waitcnt vmcnt(8)\n" \
  "ds_read_b64_tr_b16 a[96:97], v201 offset:8192\n" \
  "ds_read_b64_tr_b16 a[98:99], v201 offset:9216\n" \
  "ds_read_b64_tr_b16 a[100:101], v201 offset:8200\n" \
  "ds_read_b64_tr_b16 a[102:103], v201 offset:9224\n" \
  "ds_read_b64_tr_b16 a[104:105], v201 offset:10240\n" \
  "ds_read_b64_tr_b16 a[106:107], v201 offset:11264\n" \
  "ds_read_b64_tr_b16 a[108:109], v201 offset:10248\n" \
  "ds_read_b64_tr_b16 a[110:111], v201 offset:11272\n" \
  "s_add_u32 m0, s44, 4096\n" \
  "s_nop 0\n" \
  "global_load_lds_dwordx4 v200, s[48:49]\n" \
  "s_add_u32 s48, s48, 16384\n" \
  "s_addc_u32 s49, s49, 0\n" \
  "s_add_u32 m0, s44, 5120\n" \
  "s_nop 0\n" \
  "global_load_lds_dwordx4 v200, s[48:49]\n" \
  "s_add_u32 s48, s48, 16384\n" \
  "s_addc_u32 s49, s49, 0\n" \
  "s_add_u32 m0, s44, 6144\n" \
  "s_nop 0\n" \
  "global_load_lds_dwordx4 v200, s[48:49]\n" \
  "s_add_u32 s48, s48, 16384\n" \
  "s_addc_u32 s49, s49, 0\n" \
  "s_add_u32 m0, s44, 7168\n" \
  "s_nop 0\n" \
  "global_load_lds_dwordx4 v200, s[48:49]\n" \
  "s_add_u32 s48, s48, 16384\n" \
  "s_addc_u32 s49, s49, 0\n" \
  "s_waitcnt lgkmcnt(0)\n" \
  "s_waitcnt vmcnt(8)\n" \
  "ds_read_b64_tr_b16 a[112:113], v201 offset:12288\n" \
  "ds_read_b64_tr_b16 a[114:115], v201 offset:13312\n" \
  "ds_read_b64_tr_b16 a[116:117], v201 offset:12296\n" \
  "ds_read_b64_tr_b16 a[118:119], v201 offset:13320\n" \
  "ds_read_b64_tr_b16 a[120:121], v201 offset:14336\n" \
  "ds_read_b64_tr_b16 a[122:123], v201 offset:15360\n" \
  "ds_read_b64_tr_b16 a[124:125], v201 offset:14344\n" \
  "ds_read_b64_tr_b16 a[126:127], v201 offset:15368\n" \
  "s_add_u32 m0, s44, 8192\n" \
  "s_nop 0\n" \
  "global_load_lds_dwordx4 v200, s[48:49]\n" \
  "s_add_u32 s48, s48, 16384\n" \
  "s_addc_u32 s49, s49, 0\n" \
  "s_add_u32 m0, s44, 9216\n" \
  "s_nop 0\n" \
  "global_load_lds_dwordx4 v200, s[48:49]\n" \
  "s_add_u32 s48, s48, 16384\n" \
  "s_addc_u32 s49, s49, 0\n" \
  "s_add_u32 m0, s44, 10240\n" \
  "s_nop 0\n" \
  "global_load_lds_dwordx4 v200, s[48:49]\n" \
  "s_add_u32 s48, s48, 16384\n" \
  "s_addc_u32 s49, s49, 0\n" \
  "s_add_u32 m0, s44, 11264\n" \
  "s_nop 0\n" \
  "global_load_lds_dwordx4 v200, s[48:49]\n" \
  "s_add_u32 s48, s48, 16384\n" \
  "s_addc_u32 s49, s49, 0\n" \
  "s_waitcnt lgkmcnt(0)\n" \
  "s_waitcnt vmcnt(8)\n" \
  "ds_read_b64_tr_b16 a[128:129], v201\n" \
  "ds_read_b64_tr_b16 a[130:131], v201 offset:1024\n" \
  "ds_read_b64_tr_b16 a[132:133], v201 offset:8\n" \
  "ds_read_b64_tr_b16 a[134:135], v201 offset:1032\n" \
  "ds_read_b64_tr_b16 a[136:137], v201 offset:2048\n" \
  "ds_read_b64_tr_b16 a[138:139], v201 offset:3072\n" \
  "ds_read_b64_tr_b16 a[140:141], v201 offset:2056\n" \
  "ds_read_b64_tr_b16 a[142:143], v201 offset:3080\n" \
  "s_add_u32 m0, s44, 12288\n" \
  "s_nop 0\n" \
  "global_load_lds_dwordx4 v200, s[48:49]\n" \
  "s_add_u32 s48, s48, 16384\n" \
  "s_addc_u32 s49, s49, 0\n" \
  "s_add_u32 m0, s44, 13312\n" \
  "s_nop 0\n" \
  "global_load_lds_dwordx4 v200, s[48:49]\n" \
  "s_add_u32 s48, s48, 16384\n" \
  "s_addc_u32 s49, s49, 0\n" \
  "s_add_u32 m0, s44, 14336\n" \
  "s_nop 0\n" \
  "global_load_lds_dwordx4 v200, s[48:49]\n" \
  "s_add_u32 s48, s48, 16384\n" \
  "s_addc_u32 s49, s49, 0\n" \
  "s_add_u32 m0, s44, 15360\n" \
  "s_nop 0\n" \
  "global_load_lds_dwordx4 v200, s[48:49]\n" \
  "s_add_u32 s48, s48, 16384\n" \
  "s_addc_u32 s49, s49, 0\n" \
  "s_waitcnt lgkmcnt(0)\n" \
  "s_waitcnt vmcnt(8)\n" \
  "ds_read_b64_tr_b16 a[144:145], v201 offset:4096\n" \
  "ds_read_b64_tr_b16 a[146:147], v201 offset:5120\n" \
  "ds_read_b64_tr_b16 a[148:149], v201 offset:4104\n" \
  "ds_read_b64_tr_b16 a[150:151], v201 offset:5128\n" \
  "ds_read_b64_tr_b16 a[152:153], v201 offset:6144\n" \
  "ds_read_b64_tr_b16 a[154:155], v201 offset:7168\n" \
  "ds_read_b64_tr_b16 a[156:157], v201 offset:6152\n" \
  "ds_read_b64_tr_b16 a[158:159], v201 offset:7176\n" \
  "s_add_u32 m0, s44, 0\n" \
  "s_nop 0\n" \
  "global_load_lds_dwordx4 v200, s[48:49]\n" \
  "s_add_u32 s48, s48, 16384\n" \
  "s_addc_u32 s49, s49, 0\n" \
  "s_add_u32 m0, s44, 1024\n" \
  "s_nop 0\n" \
  "global_load_lds_dwordx4 v200, s[48:49]\n" \
  "s_add_u32 s48, s48, 16384\n" \
  "s_addc_u32 s49, s49, 0\n" \
  "s_add_u32 m0, s44, 2048\n" \
  "s_nop 0\n" \
  "global_load_lds_dwordx4 v200, s[48:49]\n" \
  "s_add_u32 s48, s48, 16384\n" \
  "s_addc_u32 s49, s49, 0\n" \
  "s_add_u32 m0, s44, 3072\n" \
  "s_nop 0\n" \
  "global_load_lds_dwordx4 v200, s[48:49]\n" \
  "s_add_u32 s48, s48, 16384\n" \
  "s_addc_u32 s49, s49, 0\n" \
  "s_waitcnt lgkmcnt(0)\n" \
  "s_waitcnt vmcnt(8)\n" \
  "ds_read_b64_tr_b16 a[160:161], v201 offset:8192\n" \
  "ds_read_b64_tr_b16 a[162:163], v201 offset:9216\n" \
  "ds_read_b64_tr_b16 a[164:165], v201 offset:8200\n" \
  "ds_read_b64_tr_b16 a[166:167], v201 offset:9224\n" \
  "ds_read_b64_tr_b16 a[168:169], v201 offset:10240\n" \
  "ds_read_b64_tr_b16 a[170:171], v201 offset:11264\n" \
  "ds_read_b64_tr_b16 a[172:173], v201 offset:10248\n" \
  "ds_read_b64_tr_b16 a[174:175], v201 offset:11272\n" \
  "s_add_u32 m0, s44, 4096\n" \
  "s_nop 0\n" \
  "global_load_lds_dwordx4 v200, s[48:49]\n" \
  "s_add_u32 s48, s48, 16384\n" \
  "s_addc_u32 s49, s49, 0\n" \
  "s_add_u32 m0, s44, 5120\n" \
  "s_nop 0\n" \
  "global_load_lds_dwordx4 v200, s[48:49]\n" \
  "s_add_u32 s48, s48, 16384\n" \
  "s_addc_u32 s49, s49, 0\n" \
  "s_add_u32 m0, s44, 6144\n" \
  "s_nop 0\n" \
  "global_load_lds_dwordx4 v200, s[48:49]\n" \
  "s_add_u32 s48, s48, 16384\n" \
  "s_addc_u32 s49, s49, 0\n" \
  "s_add_u32 m0, s44, 7168\n" \
  "s_nop 0\n" \
  "global_load_lds_dwordx4 v200, s[48:49]\n" \
  "s_add_u32 s48, s48, 16384\n" \
  "s_addc_u32 s49, s49, 0\n" \
  "s_waitcnt lgkmcnt(0)\n" \
  "s_waitcnt vmcnt(8)\n" \
  "ds_read_b64_tr_b16 a[176:177], v201 offset:12288\n" \
  "ds_read_b64_tr_b16 a[178:179], v201 offset:13312\n" \
  "ds_read_b64_tr_b16 a[180:181], v201 offset:12296\n" \
  "ds_read_b64_tr_b16 a[182:183], v201 offset:13320\n" \
  "ds_read_b64_tr_b16 a[184:185], v201 offset:14336\n" \
  "ds_read_b64_tr_b16 a[186:187], v201 offset:15360\n" \
  "ds_read_b64_tr_b16 a[188:189], v201 offset:14344\n" \
  "ds_read_b64_tr_b16 a[190:191], v201 offset:15368\n" \
  "s_add_u32 m0, s44, 8192\n" \
  "s_nop 0\n" \
  "global_load_lds_dwordx4 v200, s[48:49]\n" \
  "s_add_u32 s48, s48, 16384\n" \
  "s_addc_u32 s49, s49, 0\n" \
  "s_add_u32 m0, s44, 9216\n" \
  "s_nop 0\n" \
  "global_load_lds_dwordx4 v200, s[48:49]\n" \
  "s_add_u32 s48, s48, 16384\n" \
  "s_addc_u32 s49, s49, 0\n" \
  "s_add_u32 m0, s44, 10240\n" \
  "s_nop 0\n" \
  "global_load_lds_dwordx4 v200, s[48:49]\n" \
  "s_add_u32 s48, s48, 16384\n" \
  "s_addc_u32 s49, s49, 0\n" \
  "s_add_u32 m0, s44, 11264\n" \
  "s_nop 0\n" \
  "global_load_lds_dwordx4 v200, s[48:49]\n" \
  "s_add_u32 s48, s48, 16384\n" \
  "s_addc_u32 s49, s49, 0\n" \
  "s_waitcnt lgkmcnt(0)\n" \
  "s_waitcnt vmcnt(8)\n" \
  "ds_read_b64_tr_b16 a[192:193], v201\n" \
  "ds_read_b64_tr_b16 a[194:195], v201 offset:1024\n" \
  "ds_read_b64_tr_b16 a[196:197], v201 offset:8\n" \
  "ds_read_b64_tr_b16 a[198:199], v201 offset:1032\n" \
  "ds_read_b64_tr_b16 a[200:201], v201 offset:2048\n" \
  "ds_read_b64_tr_b16 a[202:203], v201 offset:3072\n" \
  "ds_read_b64_tr_b16 a[204:205], v201 offset:2056\n" \
  "ds_read_b64_tr_b16 a[206:207], v201 offset:3080\n" \
  "s_add_u32 m0, s44, 12288\n" \
  "s_nop 0\n" \
  "global_load_lds_dwordx4 v200, s[48:49]\n" \
  "s_add_u32 s48, s48, 16384\n" \
  "s_addc_u32 s49, s49, 0\n" \
  "s_add_u32 m0, s44, 13312\n" \
  "s_nop 0\n" \
  "global_load_lds_dwordx4 v200, s[48:49]\n" \
  "s_add_u32 s48, s48, 16384\n" \
  "s_addc_u32 s49, s49, 0\n" \
  "s_add_u32 m0, s44, 14336\n" \
  "s_nop 0\n" \
  "global_load_lds_dwordx4 v200, s[48:49]\n" \
  "s_add_u32 s48, s48, 16384\n" \
  "s_addc_u32 s49, s49, 0\n" \
  "s_add_u32 m0, s44, 15360\n" \
  "s_nop 0\n" \
  "global_load_lds_dwordx4 v200, s[48:49]\n" \
  "s_add_u32 s48, s48, 16384\n" \
  "s_addc_u32 s49, s49, 0\n" \
  "s_waitcnt lgkmcnt(0)\n" \
  "s_waitcnt vmcnt(8)\n" \
  "ds_read_b64_tr_b16 a[208:209], v201 offset:4096\n" \
  "ds_read_b64_tr_b16 a[210:211], v201 offset:5120\n" \
  "ds_read_b64_tr_b16 a[212:213], v201 offset:4104\n" \
  "ds_read_b64_tr_b16 a[214:215], v201 offset:5128\n" \
  "ds_read_b64_tr_b16 a[216:217], v201 offset:6144\n" \
  "ds_read_b64_tr_b16 a[218:219], v201 offset:7168\n" \
  "ds_read_b64_tr_b16 a[220:221], v201 offset:6152\n" \
  "ds_read_b64_tr_b16 a[222:223], v201 offset:7176\n" \
  "s_waitcnt lgkmcnt(0)\n" \
  "s_waitcnt vmcnt(4)\n" \
  "ds_read_b64_tr_b16 a[224:225], v201 offset:8192\n" \
  "ds_read_b64_tr_b16 a[226:227], v201 offset:9216\n" \
  "ds_read_b64_tr_b16 a[228:229], v201 offset:8200\n" \
  "ds_read_b64_tr_b16 a[230:231], v201 offset:9224\n" \
  "ds_read_b64_tr_b16 a[232:233], v201 offset:10240\n" \
  "ds_read_b64_tr_b16 a[234:235], v201 offset:11264\n" \
  "ds_read_b64_tr_b16 a[236:237], v201 offset:10248\n" \
  "ds_read_b64_tr_b16 a[238:239], v201 offset:11272\n" \
  "s_waitcnt lgkmcnt(0)\n" \
  "s_waitcnt vmcnt(0)\n" \
  "ds_read_b64_tr_b16 a[240:241], v201 offset:12288\n" \
  "ds_read_b64_tr_b16 a[242:243], v201 offset:13312\n" \
  "ds_read_b64_tr_b16 a[244:245], v201 offset:12296\n" \
  "ds_read_b64_tr_b16 a[246:247], v201 offset:13320\n" \
  "ds_read_b64_tr_b16 a[248:249], v201 offset:14336\n" \
  "ds_read_b64_tr_b16 a[250:251], v201 offset:15360\n" \
  "ds_read_b64_tr_b16 a[252:253], v201 offset:14344\n" \
  "ds_read_b64_tr_b16 a[254:255], v201 offset:15368\n" \
  "s_waitcnt lgkmcnt(0)\n" \
  "s_and_b32 s42, s40, 1\n" \
  "s_lshl_b32 s42, s42, 7\n" \
  "s_add_u32 s34, s34, s42\n" \
  "s_addc_u32 s35, s35, 0\n" \
  "Lohave_%=:\n" \
  "s_cmp_lt_u32 s40, 2\n" \
  "s_cselect_b64 s[30:31], 1, 0\n" \
  "s_mov_b64 s[62:63], 0\n" \
  "s_mov_b32 s55, 0\n" \
  "s_lshl_b32 s42, s14, 18\n" \
  "s_lshl_b32 s43, s40, 16\n" \
  "s_add_u32 s42, s42, s43\n" \
  "s_add_u32 s6, s6, s42\n" \
  "s_addc_u32 s7, s7, 0\n" \
  "s_lshl_b32 s42, s14, 15\n" \
  "s_add_u32 s10, s10, s42\n" \
  "s_addc_u32 s11, s11, 0\n" \
  "s_add_u32 s12, s12, s42\n" \
  "s_addc_u32 s13, s13, 0\n" \
  "s_lshr_b32 s52, s14, 2\n" \
  "s_add_u32 s52, s52, 1\n" \
  "Lotop_%=:\n" \
  "Los3_%=:\n" \
  "global_load_dword v245, v241, s[8:9] sc1\n" \
  "s_waitcnt vmcnt(0)\n" \
  "v_cmp_gt_u32 vcc, s52, v245\n" \
  "s_nop 0\n" \
  "s_cbranch_vccz Los3b_%=\n" \
  "s_sleep 2\n" \
  "s_sub_u32 s41, s41, 1\n" \
  "s_cmp_gt_i32 s41, 0\n" \
  "s_cbranch_scc1 Los3_%=\n" \
  "Los3b_%=:\n" \
  "Logo_%=:\n" \
  "s_mov_b64 s[48:49], s[6:7]\n" \
  "global_load_dwordx4 v[0:3], v240, s[48:49] sc1\n" \
  "global_load_dwordx4 v[4:7], v240, s[48:49] offset:1024 sc1\n" \
  "global_load_dwordx4 v[8:11], v240, s[48:49] offset:2048 sc1\n" \
  "global_load_dwordx4 v[12:15], v240, s[48:49] offset:3072 sc1\n" \
  "s_add_u32 s48, s48, 4096\n" \
  "s_addc_u32 s49, s49, 0\n" \
  "global_load_dwordx4 v[16:19], v240, s[48:49] sc1\n" \
  "global_load_dwordx4 v[20:23], v240, s[48:49] offset:1024 sc1\n" \
  "global_load_dwordx4 v[24:27], v240, s[48:49] offset:2048 sc1\n" \
  "global_load_dwordx4 v[28:31], v240, s[48:49] offset:3072 sc1\n" \
  "s_add_u32 s48, s48, 4096\n" \
  "s_addc_u32 s49, s49, 0\n" \
  "global_load_dwordx4 v[32:35], v240, s[48:49] sc1\n" \
  "global_load_dwordx4 v[36:39], v240, s[48:49] offset:1024 sc1\n" \
  "global_load_dwordx4 v[40:43], v240, s[48:49] offset:2048 sc1\n" \
  "global_load_dwordx4 v[44:47], v240, s[48:49] offset:3072 sc1\n" \
  "s_add_u32 s48, s48, 4096\n" \
  "s_addc_u32 s49, s49, 0\n" \
  "global_load_dwordx4 v[48:51], v240, s[48:49] sc1\n" \
  "global_load_dwordx4 v[52:55], v240, s[48:49] offset:1024 sc1\n" \
  "global_load_dwordx4 v[56:59], v240, s[48:49] offset:2048 sc1\n" \
  "global_load_dwordx4 v[60:63], v240, s[48:49] offset:3072 sc1\n" \
  "s_add_u32 s48, s48, 4096\n" \
  "s_addc_u32 s49, s49, 0\n" \
  "global_load_dwordx4 v[64:67], v240, s[48:49] sc1\n" \
  "global_load_dwordx4 v[68:71], v240, s[48:49] offset:1024 sc1\n" \
  "global_load_dwordx4 v[72:75], v240, s[48:49] offset:2048 sc1\n" \
  "global_load_dwordx4 v[76:79], v240, s[48:49] offset:3072 sc1\n" \
  "s_add_u32 s48, s48, 4096\n" \
  "s_addc_u32 s49, s49, 0\n" \
  "global_load_dwordx4 v[80:83], v240, s[48:49] sc1\n" \
  "global_load_dwordx4 v[84:87], v240, s[48:49] offset:1024 sc1\n" \
  "global_load_dwordx4 v[88:91], v240, s[48:49] offset:2048 sc1\n" \
  "global_load_dwordx4 v[92:95], v240, s[48:49] offset:3072 sc1\n" \
  "s_add_u32 s48, s48, 4096\n" \
  "s_addc_u32 s49, s49, 0\n" \
  "global_load_dwordx4 v[96:99], v240, s[48:49] sc1\n" \
  "global_load_dwordx4 v[100:103], v240, s[48:49] offset:1024 sc1\n" \
  "global_load_dwordx4 v[104:107], v240, s[48:49] offset:2048 sc1\n" \
  "global_load_dwordx4 v[108:111], v240, s[48:49] offset:3072 sc1\n" \
  "s_add_u32 s48, s48, 4096\n" \
  "s_addc_u32 s49, s49, 0\n" \
  "global_load_dwordx4 v[112:115], v240, s[48:49] sc1\n" \
  "global_load_dwordx4 v[116:119], v240, s[48:49] offset:1024 sc1\n" \
  "global_load_dwordx4 v[120:123], v240, s[48:49] offset:2048 sc1\n" \
  "global_load_dwordx4 v[124:127], v240, s[48:49] offset:3072 sc1\n" \
  "s_add_u32 s48, s48, 4096\n" \
  "s_addc_u32 s49, s49, 0\n" \
  "Lobody_%=:\n" \
  "v_xor_b32 v241, 4096, v241\n" \
  "s_waitcnt vmcnt(31)\n" \
  "v_mfma_f32_16x16x32_bf16 v[128:131], a[0:3], v[0:3], 0\n" \
  "v_mfma_f32_16x16x32_bf16 v[132:135], a[4:7], v[0:3], 0\n" \
  "global_load_dwordx4 v[144:147], v242, s[10:11] sc1\n" \
  "global_load_dwordx4 v[0:3], v240, s[48:49] sc1\n" \
  "s_waitcnt vmcnt(31)\n" \
  "v_mfma_f32_16x16x32_bf16 v[128:131], a[8:11], v[4:7], v[128:131]\n" \
  "v_mfma_f32_16x16x32_bf16 v[132:135], a[12:15], v[4:7], v[132:135]\n" \
  "global_load_dwordx4 v[4:7], v240, s[48:49] offset:1024 sc1\n" \
  "s_waitcnt vmcnt(31)\n" \
  "v_mfma_f32_16x16x32_bf16 v[128:131], a[16:19], v[8:11], v[128:131]\n" \
  "v_mfma_f32_16x16x32_bf16 v[132:135], a[20:23], v[8:11], v[132:135]\n" \
  "global_load_dwordx4 v[8:11], v240, s[48:49] offset:2048 sc1\n" \
  "s_waitcnt vmcnt(31)\n" \
  "v_mfma_f32_16x16x32_bf16 v[128:131], a[24:27], v[12:15], v[128:131]\n" \
  "v_mfma_f32_16x16x32_bf16 v[132:135], a[28:31], v[12:15], v[132:135]\n" \
  "global_load_dwordx4 v[12:15], v240, s[48:49] offset:3072 sc1\n" \
  "s_add_u32 s48, s48, 4096\n" \
  "s_addc_u32 s49, s49, 0\n" \
  "s_waitcnt vmcnt(31)\n" \
  "v_mfma_f32_16x16x32_bf16 v[136:139], a[0:3], v[16:19], 0\n" \
  "v_mfma_f32_16x16x32_bf16 v[140:143], a[4:7], v[16:19], 0\n" \
  "global_load_dwordx4 v[16:19], v240, s[48:49] sc1\n" \
  "s_waitcnt vmcnt(31)\n" \
  "v_mfma_f32_16x16x32_bf16 v[136:139], a[8:11], v[20:23], v[136:139]\n" \
  "v_mfma_f32_16x16x32_bf16 v[140:143], a[12:15], v[20:23], v[140:143]\n" \
  "global_load_dwordx4 v[20:23], v240, s[48:49] offset:1024 sc1\n" \
  "s_waitcnt vmcnt(31)\n" \
  "v_mfma_f32_16x16x32_bf16 v[136:139], a[16:19], v[24:27], v[136:139]\n" \
  "v_mfma_f32_16x16x32_bf16 v[140:143], a[20:23], v[24:27], v[140:143]\n" \
  "global_load_dwordx4 v[24:27], v240, s[48:49] offset:2048 sc1\n" \
  "s_waitcnt vmcnt(31)\n" \
  "v_mfma_f32_16x16x32_bf16 v[136:139], a[24:27], v[28:31], v[136:139]\n" \
  "v_mfma_f32_16x16x32_bf16 v[140:143], a[28:31], v[28:31], v[140:143]\n" \
  "global_load_dwordx4 v[28:31], v240, s[48:49] offset:3072 sc1\n" \
  "s_add_u32 s48, s48, 4096\n" \
  "s_addc_u32 s49, s49, 0\n" \
  "s_waitcnt vmcnt(31)\n" \
  "v_mfma_f32_16x16x32_bf16 v[128:131], a[32:35], v[32:35], v[128:131]\n" \
  "v_mfma_f32_16x16x32_bf16 v[132:135], a[36:39], v[32:35], v[132:135]\n" \
  "global_load_dwordx4 v[32:35], v240, s[48:49] sc1\n" \
  "s_waitcnt vmcnt(31)\n" \
  "v_mfma_f32_16x16x32_bf16 v[128:131], a[40:43], v[36:39], v[128:131]\n" \
  "v_mfma_f32_16x16x32_bf16 v[132:135], a[44:47], v[36:39], v[132:135]\n" \
  "global_load_dwordx4 v[36:39], v240, s[48:49] offset:1024 sc1\n" \
  "s_waitcnt vmcnt(31)\n" \
  "v_mfma_f32_16x16x32_bf16 v[128:131], a[48:51], v[40:43], v[128:131]\n" \
  "v_mfma_f32_16x16x32_bf16 v[132:135], a[52:55], v[40:43], v[132:135]\n" \
  "global_load_dwordx4 v[40:43], v240, s[48:49] offset:2048 sc1\n" \
  "s_waitcnt vmcnt(31)\n" \
  "v_mfma_f32_16x16x32_bf16 v[128:131], a[56:59], v[44:47], v[128:131]\n" \
  "v_mfma_f32_16x16x32_bf16 v[132:135], a[60:63], v[44:47], v[132:135]\n" \
  "global_load_dwordx4 v[44:47], v240, s[48:49] offset:3072 sc1\n" \
  "s_add_u32 s48, s48, 4096\n" \
  "s_addc_u32 s49, s49, 0\n" \
  "s_waitcnt vmcnt(31)\n" \
  "v_mfma_f32_16x16x32_bf16 v[136:139], a[32:35], v[48:51], v[136:139]\n" \
  "v_mfma_f32_16x16x32_bf16 v[140:143], a[36:39], v[48:51], v[140:143]\n" \
  "global_load_dwordx4 v[48:51], v240, s[48:49] sc1\n" \
  "s_waitcnt vmcnt(31)\n" \
  "v_mfma_f32_16x16x32_bf16 v[136:139], a[40:43], v[52:55], v[136:139]\n" \
  "v_mfma_f32_16x16x32_bf16 v[140:143], a[44:47], v[52:55], v[140:143]\n" \
  "global_load_dwordx4 v[52:55], v240, s[48:49] offset:1024 sc1\n" \
  "s_waitcnt vmcnt(31)\n" \
  "v_mfma_f32_16x16x32_bf16 v[136:139], a[48:51], v[56:59], v[136:139]\n" \
  "v_mfma_f32_16x16x32_bf16 v[140:143], a[52:55], v[56:59], v[140:143]\n" \
  "global_load_dwordx4 v[56:59], v240, s[48:49] offset:2048 sc1\n" \
  "s_waitcnt vmcnt(31)\n" \
  "v_mfma_f32_16x16x32_bf16 v[136:139], a[56:59], v[60:63], v[136:139]\n" \
  "v_mfma_f32_16x16x32_bf16 v[140:143], a[60:63], v[60:63], v[140:143]\n" \
  "global_load_dwordx4 v[60:63], v240, s[48:49] offset:3072 sc1\n" \
  "s_add_u32 s48, s48, 4096\n" \
  "s_addc_u32 s49, s49, 0\n" \
  "s_waitcnt vmcnt(31)\n" \
  "v_mfma_f32_16x16x32_bf16 v[128:131], a[64:67], v[64:67], v[128:131]\n" \
  "v_mfma_f32_16x16x32_bf16 v[132:135], a[68:71], v[64:67], v[132:135]\n" \
  "global_load_dwordx4 v[64:67], v240, s[48:49] sc1\n" \
  "s_waitcnt vmcnt(31)\n" \
  "v_mfma_f32_16x16x32_bf16 v[128:131], a[72:75], v[68:71], v[128:131]\n" \
  "v_mfma_f32_16x16x32_bf16 v[132:135], a[76:79], v[68:71], v[132:135]\n" \
  "global_load_dwordx4 v[68:71], v240, s[48:49] offset:1024 sc1\n" \
  "s_waitcnt vmcnt(31)\n" \
  "v_mfma_f32_16x16x32_bf16 v[128:131], a[80:83], v[72:75], v[128:131]\n" \
  "v_mfma_f32_16x16x32_bf16 v[132:135], a[84:87], v[72:75], v[132:135]\n" \
  "global_load_dwordx4 v[72:75], v240, s[48:49] offset:2048 sc1\n" \
  "s_waitcnt vmcnt(31)\n" \
  "v_mfma_f32_16x16x32_bf16 v[128:131], a[88:91], v[76:79], v[128:131]\n" \
  "v_mfma_f32_16x16x32_bf16 v[132:135], a[92:95], v[76:79], v[132:135]\n" \
  "global_load_dwordx4 v[76:79], v240, s[48:49] offset:3072 sc1\n" \
  "s_add_u32 s48, s48, 4096\n" \
  "s_addc_u32 s49, s49, 0\n" \
  "s_waitcnt vmcnt(31)\n" \
  "v_mfma_f32_16x16x32_bf16 v[136:139], a[64:67], v[80:83], v[136:139]\n" \
  "v_mfma_f32_16x16x32_bf16 v[140:143], a[68:71], v[80:83], v[140:143]\n" \
  "global_load_dwordx4 v[80:83], v240, s[48:49] sc1\n" \
  "s_waitcnt vmcnt(31)\n" \
  "v_mfma_f32_16x16x32_bf16 v[136:139], a[72:75], v[84:87], v[136:139]\n" \
  "v_mfma_f32_16x16x32_bf16 v[140:143], a[76:79], v[84:87], v[140:143]\n" \
  "global_load_dwordx4 v[84:87], v240, s[48:49] offset:1024 sc1\n" \
  "s_waitcnt vmcnt(31)\n" \
  "v_mfma_f32_16x16x32_bf16 v[136:139], a[80:83], v[88:91], v[136:139]\n" \
  "v_mfma_f32_16x16x32_bf16 v[140:143], a[84:87], v[88:91], v[140:143]\n" \
  "global_load_dwordx4 v[88:91], v240, s[48:49] offset:2048 sc1\n" \
  "s_waitcnt vmcnt(31)\n" \
  "v_mfma_f32_16x16x32_bf16 v[136:139], a[88:91], v[92:95], v[136:139]\n" \
  "v_mfma_f32_16x16x32_bf16 v[140:143], a[92:95], v[92:95], v[140:143]\n" \
  "global_load_dwordx4 v[92:95], v240, s[48:49] offset:3072 sc1\n" \
  "s_add_u32 s48, s48, 4096\n" \
  "s_addc_u32 s49, s49, 0\n" \
  "s_waitcnt vmcnt(31)\n" \
  "v_mfma_f32_16x16x32_bf16 v[128:131], a[96:99], v[96:99], v[128:131]\n" \
  "v_mfma_f32_16x16x32_bf16 v[132:135], a[100:103], v[96:99], v[132:135]\n" \
  "global_load_dwordx4 v[96:99], v240, s[48:49] sc1\n" \
  "s_waitcnt vmcnt(31)\n" \
  "v_mfma_f32_16x16x32_bf16 v[128:131], a[104:107], v[100:103], v[128:131]\n" \
  "v_mfma_f32_16x16x32_bf16 v[132:135], a[108:111], v[100:103], v[132:135]\n" \
  "global_load_dwordx4 v[100:103], v240, s[48:49] offset:1024 sc1\n" \
  "s_waitcnt vmcnt(31)\n" \
  "v_mfma_f32_16x16x32_bf16 v[128:131], a[112:115], v[104:107], v[128:131]\n" \
  "v_mfma_f32_16x16x32_bf16 v[132:135], a[116:119], v[104:107], v[132:135]\n" \
  "global_load_dwordx4 v[104:107], v240, s[48:49] offset:2048 sc1\n" \
  "s_waitcnt vmcnt(31)\n" \
  "v_mfma_f32_16x16x32_bf16 v[128:131], a[120:123], v[108:111], v[128:131]\n" \
  "v_mfma_f32_16x16x32_bf16 v[132:135], a[124:127], v[108:111], v[132:135]\n" \
  "global_load_dwordx4 v[108:111], v240, s[48:49] offset:3072 sc1\n" \
  "s_add_u32 s48, s48, 4096\n" \
  "s_addc_u32 s49, s49, 0\n" \
  "s_waitcnt vmcnt(31)\n" \
  "v_mfma_f32_16x16x32_bf16 v[136:139], a[96:99], v[112:115], v[136:139]\n" \
  "v_mfma_f32_16x16x32_bf16 v[140:143], a[100:103], v[112:115], v[140:143]\n" \
  "global_load_dwordx4 v[112:115], v240, s[48:49] sc1\n" \
  "s_waitcnt vmcnt(31)\n" \
  "v_mfma_f32_16x16x32_bf16 v[136:139], a[104:107], v[116:119], v[136:139]\n" \
  "v_mfma_f32_16x16x32_bf16 v[140:143], a[108:111], v[116:119], v[140:143]\n" \
  "global_load_dwordx4 v[116:119], v240, s[48:49] offset:1024 sc1\n" \
  "s_waitcnt vmcnt(31)\n" \
  "v_mfma_f32_16x16x32_bf16 v[136:139], a[112:115], v[120:123], v[136:139]\n" \
  "v_mfma_f32_16x16x32_bf16 v[140:143], a[116:119], v[120:123], v[140:143]\n" \
  "global_load_dwordx4 v[120:123], v240, s[48:49] offset:2048 sc1\n" \
  "s_waitcnt vmcnt(31)\n" \
  "v_mfma_f32_16x16x32_bf16 v[136:139], a[120:123], v[124:127], v[136:139]\n" \
  "v_mfma_f32_16x16x32_bf16 v[140:143], a[124:127], v[124:127], v[140:143]\n" \
  "global_load_dwordx4 v[124:127], v240, s[48:49] offset:3072 sc1\n" \
  "s_add_u32 s48, s48, 4096\n" \
  "s_addc_u32 s49, s49, 0\n" \
  "s_waitcnt vmcnt(31)\n" \
  "v_mfma_f32_16x16x32_bf16 v[128:131], a[128:131], v[0:3], v[128:131]\n" \
  "v_mfma_f32_16x16x32_bf16 v[132:135], a[132:135], v[0:3], v[132:135]\n" \
  "s_waitcnt vmcnt(30)\n" \
  "v_mfma_f32_16x16x32_bf16 v[128:131], a[136:139], v[4:7], v[128:131]\n" \
  "v_mfma_f32_16x16x32_bf16 v[132:135], a[140:143], v[4:7], v[132:135]\n" \
  "s_waitcnt vmcnt(29)\n" \
  "v_mfma_f32_16x16x32_bf16 v[128:131], a[144:147], v[8:11], v[128:131]\n" \
  "v_mfma_f32_16x16x32_bf16 v[132:135], a[148:151], v[8:11], v[132:135]\n" \
  "s_waitcnt vmcnt(28)\n" \
  "v_mfma_f32_16x16x32_bf16 v[128:131], a[152:155], v[12:15], v[128:131]\n" \
  "v_mfma_f32_16x16x32_bf16 v[132:135], a[156:159], v[12:15], v[132:135]\n" \
  "v_mov_b32 v245, s55\n" \
  "s_mov_b64 exec, s[62:63]\n" \
  "global_store_dword v255, v245, s[34:35] sc1\n" \
  "s_mov_b64 exec, -1\n" \
  "s_waitcnt vmcnt(27)\n" \
  "v_mfma_f32_16x16x32_bf16 v[136:139], a[128:131], v[16:19], v[136:139]\n" \
  "v_mfma_f32_16x16x32_bf16 v[140:143], a[132:135], v[16:19], v[140:143]\n" \
  "s_waitcnt vmcnt(26)\n" \
  "v_mfma_f32_16x16x32_bf16 v[136:139], a[136:139], v[20:23], v[136:139]\n" \
  "v_mfma_f32_16x16x32_bf16 v[140:143], a[140:143], v[20:23], v[140:143]\n" \
  "s_waitcnt vmcnt(25)\n" \
  "v_mfma_f32_16x16x32_bf16 v[136:139], a[144:147], v[24:27], v[136:139]\n" \
  "v_mfma_f32_16x16x32_bf16 v[140:143], a[148:151], v[24:27], v[140:143]\n" \
  "s_waitcnt vmcnt(24)\n" \
  "v_mfma_f32_16x16x32_bf16 v[136:139], a[152:155], v[28:31], v[136:139]\n" \
  "v_mfma_f32_16x16x32_bf16 v[140:143], a[156:159], v[28:31], v[140:143]\n" \
  "s_waitcnt vmcnt(23)\n" \
  "v_mfma_f32_16x16x32_bf16 v[128:131], a[160:163], v[32:35], v[128:131]\n" \
  "v_mfma_f32_16x16x32_bf16 v[132:135], a[164:167], v[32:35], v[132:135]\n" \
  "s_waitcnt vmcnt(22)\n" \
  "v_mfma_f32_16x16x32_bf16 v[128:131], a[168:171], v[36:39], v[128:131]\n" \
  "v_mfma_f32_16x16x32_bf16 v[132:135], a[172:175], v[36:39], v[132:135]\n" \
  "s_waitcnt vmcnt(21)\n" \
  "v_mfma_f32_16x16x32_bf16 v[128:131], a[176:179], v[40:43], v[128:131]\n" \
  "v_mfma_f32_16x16x32_bf16 v[132:135], a[180:183], v[40:43], v[132:135]\n" \
  "s_waitcnt vmcnt(20)\n" \
  "v_mfma_f32_16x16x32_bf16 v[128:131], a[184:187], v[44:47], v[128:131]\n" \
  "v_mfma_f32_16x16x32_bf16 v[132:135], a[188:191], v[44:47], v[132:135]\n" \
  "s_waitcnt vmcnt(19)\n" \
  "v_mfma_f32_16x16x32_bf16 v[136:139], a[160:163], v[48:51], v[136:139]\n" \
  "v_mfma_f32_16x16x32_bf16 v[140:143], a[164:167], v[48:51], v[140:143]\n" \
  "s_waitcnt vmcnt(18)\n" \
  "v_mfma_f32_16x16x32_bf16 v[136:139], a[168:171], v[52:55], v[136:139]\n" \
  "v_mfma_f32_16x16x32_bf16 v[140:143], a[172:175], v[52:55], v[140:143]\n" \
  "s_waitcnt vmcnt(17)\n" \
  "v_mfma_f32_16x16x32_bf16 v[136:139], a[176:179], v[56:59], v[136:139]\n" \
  "v_mfma_f32_16x16x32_bf16 v[140:143], a[180:183], v[56:59], v[140:143]\n" \
  "s_waitcnt vmcnt(16)\n" \
  "v_mfma_f32_16x16x32_bf16 v[136:139], a[184:187], v[60:63], v[136:139]\n" \
  "v_mfma_f32_16x16x32_bf16 v[140:143], a[188:191], v[60:63], v[140:143]\n" \
  "s_waitcnt vmcnt(15)\n" \
  "v_mfma_f32_16x16x32_bf16 v[128:131], a[192:195], v[64:67], v[128:131]\n" \
  "v_mfma_f32_16x16x32_bf16 v[132:135], a[196:199], v[64:67], v[132:135]\n" \
  "s_waitcnt vmcnt(14)\n" \
  "v_mfma_f32_16x16x32_bf16 v[128:131], a[200:203], v[68:71], v[128:131]\n" \
  "v_mfma_f32_16x16x32_bf16 v[132:135], a[204:207], v[68:71], v[132:135]\n" \
  "s_waitcnt vmcnt(13)\n" \
  "v_mfma_f32_16x16x32_bf16 v[128:131], a[208:211], v[72:75], v[128:131]\n" \
  "v_mfma_f32_16x16x32_bf16 v[132:135], a[212:215], v[72:75], v[132:135]\n" \
  "s_waitcnt vmcnt(12)\n" \
  "v_mfma_f32_16x16x32_bf16 v[128:131], a[216:219], v[76:79], v[128:131]\n" \
  "v_mfma_f32_16x16x32_bf16 v[132:135], a[220:223], v[76:79], v[132:135]\n" \
  "s_waitcnt vmcnt(11)\n" \
  "v_mfma_f32_16x16x32_bf16 v[136:139], a[192:195], v[80:83], v[136:139]\n" \
  "v_mfma_f32_16x16x32_bf16 v[140:143], a[196:199], v[80:83], v[140:143]\n" \
  "s_waitcnt vmcnt(10)\n" \
  "v_mfma_f32_16x16x32_bf16 v[136:139], a[200:203], v[84:87], v[136:139]\n" \
  "v_mfma_f32_16x16x32_bf16 v[140:143], a[204:207], v[84:87], v[140:143]\n" \
  "s_waitcnt vmcnt(9)\n" \
  "v_mfma_f32_16x16x32_bf16 v[136:139], a[208:211], v[88:91], v[136:139]\n" \
  "v_mfma_f32_16x16x32_bf16 v[140:143], a[212:215], v[88:91], v[140:143]\n" \
  "global_load_dword v184, v241, s[8:9] sc1\n" \
  "s_waitcnt vmcnt(8)\n" \
  "v_mfma_f32_16x16x32_bf16 v[136:139], a[216:219], v[92:95], v[136:139]\n" \
  "v_mfma_f32_16x16x32_bf16 v[140:143], a[220:223], v[92:95], v[140:143]\n" \
  "s_waitcnt vmcnt(7)\n" \
  "v_mfma_f32_16x16x32_bf16 v[128:131], a[224:227], v[96:99], v[128:131]\n" \
  "v_mfma_f32_16x16x32_bf16 v[132:135], a[228:231], v[96:99], v[132:135]\n" \
  "s_waitcnt vmcnt(6)\n" \
  "v_mfma_f32_16x16x32_bf16 v[128:131], a[232:235], v[100:103], v[128:131]\n" \
  "v_mfma_f32_16x16x32_bf16 v[132:135], a[236:239], v[100:103], v[132:135]\n" \
  "s_waitcnt vmcnt(5)\n" \
  "v_mfma_f32_16x16x32_bf16 v[128:131], a[240:243], v[104:107], v[128:131]\n" \
  "v_mfma_f32_16x16x32_bf16 v[132:135], a[244:247], v[104:107], v[132:135]\n" \
  "s_waitcnt vmcnt(4)\n" \
  "v_mfma_f32_16x16x32_bf16 v[128:131], a[248:251], v[108:111], v[128:131]\n" \
  "v_mfma_f32_16x16x32_bf16 v[132:135], a[252:255], v[108:111], v[132:135]\n" \
  "s_waitcnt vmcnt(3)\n" \
  "v_mfma_f32_16x16x32_bf16 v[136:139], a[224:227], v[112:115], v[136:139]\n" \
  "v_mfma_f32_16x16x32_bf16 v[140:143], a[228:231], v[112:115], v[140:143]\n" \
  "s_waitcnt vmcnt(2)\n" \
  "v_mfma_f32_16x16x32_bf16 v[136:139], a[232:235], v[116:119], v[136:139]\n" \
  "v_mfma_f32_16x16x32_bf16 v[140:143], a[236:239], v[116:119], v[140:143]\n" \
  "s_waitcnt vmcnt(1)\n" \
  "v_mfma_f32_16x16x32_bf16 v[136:139], a[240:243], v[120:123], v[136:139]\n" \
  "v_mfma_f32_16x16x32_bf16 v[140:143], a[244:247], v[120:123], v[140:143]\n" \
  "s_waitcnt vmcnt(0)\n" \
  "v_mfma_f32_16x16x32_bf16 v[136:139], a[248:251], v[124:127], v[136:139]\n" \
  "v_mfma_f32_16x16x32_bf16 v[140:143], a[252:255], v[124:127], v[140:143]\n" \
  "s_mov_b64 s[58:59], s[12:13]\n" \
  "s_add_u32 s6, s6, 524288\n" \
  "s_addc_u32 s7, s7, 0\n" \
  "s_add_u32 s10, s10, 65536\n" \
  "s_addc_u32 s11, s11, 0\n" \
  "s_add_u32 s12, s12, 65536\n" \
  "s_addc_u32 s13, s13, 0\n" \
  "s_add_u32 s14, s14, 2\n" \
  "s_lshr_b32 s52, s14, 2\n" \
  "s_add_u32 s52, s52, 1\n" \
  "v_cmp_gt_u32 vcc, s52, v184\n" \
  "s_mov_b32 s57, 0\n" \
  "s_cbranch_vccnz Lono_%=\n" \
  "s_mov_b32 s57, 1\n" \
  "s_mov_b64 s[48:49], s[6:7]\n" \
  "global_load_dwordx4 v[0:3], v240, s[48:49] sc1\n" \
  "global_load_dwordx4 v[4:7], v240, s[48:49] offset:1024 sc1\n" \
  "global_load_dwordx4 v[8:11], v240, s[48:49] offset:2048 sc1\n" \
  "global_load_dwordx4 v[12:15], v240, s[48:49] offset:3072 sc1\n" \
  "s_add_u32 s48, s48, 4096\n" \
  "s_addc_u32 s49, s49, 0\n" \
  "global_load_dwordx4 v[16:19], v240, s[48:49] sc1\n" \
  "global_load_dwordx4 v[20:23], v240, s[48:49] offset:1024 sc1\n" \
  "global_load_dwordx4 v[24:27], v240, s[48:49] offset:2048 sc1\n" \
  "global_load_dwordx4 v[28:31], v240, s[48:49] offset:3072 sc1\n" \
  "s_add_u32 s48, s48, 4096\n" \
  "s_addc_u32 s49, s49, 0\n" \
  "global_load_dwordx4 v[32:35], v240, s[48:49] sc1\n" \
  "global_load_dwordx4 v[36:39], v240, s[48:49] offset:1024 sc1\n" \
  "global_load_dwordx4 v[40:43], v240, s[48:49] offset:2048 sc1\n" \
  "global_load_dwordx4 v[44:47], v240, s[48:49] offset:3072 sc1\n" \
  "s_add_u32 s48, s48, 4096\n" \
  "s_addc_u32 s49, s49, 0\n" \
  "global_load_dwordx4 v[48:51], v240, s[48:49] sc1\n" \
  "global_load_dwordx4 v[52:55], v240, s[48:49] offset:1024 sc1\n" \
  "global_load_dwordx4 v[56:59], v240, s[48:49] offset:2048 sc1\n" \
  "global_load_dwordx4 v[60:63], v240, s[48:49] offset:3072 sc1\n" \
  "s_add_u32 s48, s48, 4096\n" \
  "s_addc_u32 s49, s49, 0\n" \
  "global_load_dwordx4 v[64:67], v240, s[48:49] sc1\n" \
  "global_load_dwordx4 v[68:71], v240, s[48:49] offset:1024 sc1\n" \
  "global_load_dwordx4 v[72:75], v240, s[48:49] offset:2048 sc1\n" \
  "global_load_dwordx4 v[76:79], v240, s[48:49] offset:3072 sc1\n" \
  "s_add_u32 s48, s48, 4096\n" \
  "s_addc_u32 s49, s49, 0\n" \
  "global_load_dwordx4 v[80:83], v240, s[48:49] sc1\n" \
  "global_load_dwordx4 v[84:87], v240, s[48:49] offset:1024 sc1\n" \
  "global_load_dwordx4 v[88:91], v240, s[48:49] offset:2048 sc1\n" \
  "global_load_dwordx4 v[92:95], v240, s[48:49] offset:3072 sc1\n" \
  "s_add_u32 s48, s48, 4096\n" \
  "s_addc_u32 s49, s49, 0\n" \
  "global_load_dwordx4 v[96:99], v240, s[48:49] sc1\n" \
  "global_load_dwordx4 v[100:103], v240, s[48:49] offset:1024 sc1\n" \
  "global_load_dwordx4 v[104:107], v240, s[48:49] offset:2048 sc1\n" \
  "global_load_dwordx4 v[108:111], v240, s[48:49] offset:3072 sc1\n" \
  "s_add_u32 s48, s48, 4096\n" \
  "s_addc_u32 s49, s49, 0\n" \
  "global_load_dwordx4 v[112:115], v240, s[48:49] sc1\n" \
  "global_load_dwordx4 v[116:119], v240, s[48:49] offset:1024 sc1\n" \
  "global_load_dwordx4 v[120:123], v240, s[48:49] offset:2048 sc1\n" \
  "global_load_dwordx4 v[124:127], v240, s[48:49] offset:3072 sc1\n" \
  "s_add_u32 s48, s48, 4096\n" \
  "s_addc_u32 s49, s49, 0\n" \
  "Lono_%=:\n" \
  "s_barrier\n" \
  "ds_write_b128 v243, v[128:131]\n" \
  "ds_write_b128 v243, v[132:135] offset:1024\n" \
  "ds_write_b128 v243, v[136:139] offset:2048\n" \
  "ds_write_b128 v243, v[140:143] offset:3072\n" \
  "s_waitcnt lgkmcnt(0)\n" \
  "s_barrier\n" \
  "s_cmp_lt_u32 s40, 2\n" \
  "s_cbranch_scc0 Lonx_%=\n" \
  "ds_read_b128 v[148:151], v244\n" \
  "ds_read_b128 v[152:155], v244 offset:1024\n" \
  "ds_read_b128 v[156:159], v244 offset:4096\n" \
  "ds_read_b128 v[160:163], v244 offset:5120\n" \
  "ds_read_b128 v[164:167], v244 offset:8192\n" \
  "ds_read_b128 v[168:171], v244 offset:9216\n" \
  "ds_read_b128 v[172:175], v244 offset:12288\n" \
  "ds_read_b128 v[176:179], v244 offset:13312\n" \
  "s_waitcnt lgkmcnt(0)\n" \
  "v_add_f32 v148, v148, v156\n" \
  "v_add_f32 v164, v164, v172\n" \
  "v_add_f32 v148, v148, v164\n" \
  "v_add_f32 v149, v149, v157\n" \
  "v_add_f32 v165, v165, v173\n" \
  "v_add_f32 v149, v149, v165\n" \
  "v_add_f32 v150, v150, v158\n" \
  "v_add_f32 v166, v166, v174\n" \
  "v_add_f32 v150, v150, v166\n" \
  "v_add_f32 v151, v151, v159\n" \
  "v_add_f32 v167, v167, v175\n" \
  "v_add_f32 v151, v151, v167\n" \
  "v_cvt_pk_bf16_f32 v180, v148, v149\n" \
  "v_cvt_pk_bf16_f32 v181, v150, v151\n" \
  "v_add_f32 v152, v152, v160\n" \
  "v_add_f32 v168, v168, v176\n" \
  "v_add_f32 v152, v152, v168\n" \
  "v_add_f32 v153, v153, v161\n" \
  "v_add_f32 v169, v169, v177\n" \
  "v_add_f32 v153, v153, v169\n" \
  "v_add_f32 v154, v154, v162\n" \
  "v_add_f32 v170, v170, v178\n" \
  "v_add_f32 v154, v154, v170\n" \
  "v_add_f32 v155, v155, v163\n" \
  "v_add_f32 v171, v171, v179\n" \
  "v_add_f32 v155, v155, v171\n" \
  "v_cvt_pk_bf16_f32 v182, v152, v153\n" \
  "v_cvt_pk_bf16_f32 v183, v154, v155\n" \
  "v_lshlrev_b32 v148, 16, v180\n" \
  "v_and_b32 v149, 0xffff0000, v180\n" \
  "v_lshlrev_b32 v150, 16, v144\n" \
  "v_and_b32 v151, 0xffff0000, v144\n" \
  "v_add_f32 v148, v148, v150\n" \
  "v_add_f32 v149, v149, v151\n" \
  "v_cvt_pk_bf16_f32 v180, v148, v149\n" \
  "v_lshlrev_b32 v148, 16, v181\n" \
  "v_and_b32 v149, 0xffff0000, v181\n" \
  "v_lshlrev_b32 v150, 16, v145\n" \
  "v_and_b32 v151, 0xffff0000, v145\n" \
  "v_add_f32 v148, v148, v150\n" \
  "v_add_f32 v149, v149, v151\n" \
  "v_cvt_pk_bf16_f32 v181, v148, v149\n" \
  "v_lshlrev_b32 v148, 16, v182\n" \
  "v_and_b32 v149, 0xffff0000, v182\n" \
  "v_lshlrev_b32 v150, 16, v146\n" \
  "v_and_b32 v151, 0xffff0000, v146\n" \
  "v_add_f32 v148, v148, v150\n" \
  "v_add_f32 v149, v149, v151\n" \
  "v_cvt_pk_bf16_f32 v182, v148, v149\n" \
  "v_lshlrev_b32 v148, 16, v183\n" \
  "v_and_b32 v149, 0xffff0000, v183\n" \
  "v_lshlrev_b32 v150, 16, v147\n" \
  "v_and_b32 v151, 0xffff0000, v147\n" \
  "v_add_f32 v148, v148, v150\n" \
  "v_add_f32 v149, v149, v151\n" \
  "v_cvt_pk_bf16_f32 v183, v148, v149\n" \
  "global_store_dwordx4 v242, v[180:183], s[58:59] sc1\n" \
  "Lonx_%=:\n" \
  "s_add_u32 s55, s55, 1\n" \
  "s_mov_b64 s[62:63], s[30:31]\n" \
  "s_cmp_eq_u32 s57, 1\n" \
  "s_cbranch_scc1 Lobody_%=\n" \
  "global_load_dword v184, v241, s[8:9] sc1\n" \
  "s_waitcnt vmcnt(0)\n" \
  "v_mov_b32 v245, s55\n" \
  "s_mov_b64 exec, s[30:31]\n" \
  "global_store_dword v255, v245, s[34:35] sc1\n" \
  "s_mov_b64 exec, -1\n" \
  "s_mov_b64 s[62:63], 0\n" \
  "v_cmp_gt_u32 vcc, s52, v184\n" \
  "s_nop 0\n" \
  "s_cbranch_vccz Logo_%=\n" \
  "s_cmp_lt_u32 s14, 64\n" \
  "s_cbranch_scc1 Lotop_%=\n" \
  "s_cmp_eq_u32 s69, 1\n" \
  "s_cbranch_scc1 Loend_%=\n" \
  "s_add_u32 s34, s34, 65536\n" \
  "s_addc_u32 s35, s35, 0\n" \
  "s_mov_b32 s69, 1\n" \
  "s_branch Lolayer_%=\n" \
  "Loend_%=:\n" \
  "s_waitcnt vmcnt(0)\n" \
  "s_endpgm\n"
#define POOL_CLOBBERS "v255", "a255", "s4", "s5", "s6", "s7", "s8", "s9", "s10", "s11", "s12", "s13", "s14", "s15", "s16", "s17", "s18", "s19", "s20", "s21", "s22", "s23", "s24", "s25", "s26", "s27", "s28", "s29", "s30", "s31", "s34", "s35", "s36", "s37", "s38", "s39", "s40", "s41", "s42", "s43", "s44", "s45", "s46", "s47", "s48", "s49", "s50", "s51", "s52", "s53", "s54", "s55", "s56", "s57", "s58", "s59", "s60", "s61", "s62", "s63", "s64", "s65", "s66", "s67", "s68", "s69", "s70", "s71", "s72", "s73", "s74", "s75", "s76", "s77", "s78", "s79", "s80", "s81", "s82", "s83", "s84", "s85", "s86", "s87", "s88", "s89", "s90", "s91", "s92", "s93", "s94", "s95", "s96", "s97", "s98", "s99", "vcc", "scc", "memory"
#define LDS_PAR 162816
#define Q1_CYC 420
#define PERIOD 408

constexpr size_t MB = 1u << 20;
constexpr size_t OFF_COMM = 0;
constexpr size_t OFF_FLAG = 10 * MB;
constexpr size_t FLAG_BYTES = 2 * 16 * 32 * 128;
constexpr size_t OFF_PROBE = OFF_FLAG + 131072;
constexpr size_t OFF_SCR = OFF_FLAG + 196608;
constexpr size_t OFF_X1 = 11 * MB, OFF_X2 = 13 * MB, OFF_PQ = 15 * MB;
constexpr size_t OFF_STAT = 17 * MB;
constexpr size_t OFF_FST = 53 * MB;
constexpr size_t OFF_KVP = 19 * MB - 131072;
constexpr size_t OFF_MF = 18 * MB;
constexpr size_t OFF_Z = 18 * MB + 524288;
constexpr size_t OFF_KV = 20 * MB;
constexpr size_t OFF_O = 62 * MB;

DEV f4 mfma32(bf8 a, bf8 b, f4 c) { return __builtin_amdgcn_mfma_f32_16x16x32_bf16(a, b, c, 0, 0, 0); }
DEV bf4 cvt4(f4 x) { return bf4{(__bf16)x[0], (__bf16)x[1], (__bf16)x[2], (__bf16)x[3]}; }
DEV bf8 cat(bf4 a, bf4 b) { return __builtin_shufflevector(a, b, 0, 1, 2, 3, 4, 5, 6, 7); }
DEV bf4 tr_read(const char* lds_base, unsigned off) { return __builtin_amdgcn_ds_read_tr16_b64_v4bf16((LAS bf4*)(lds_base + off)); }
DEV float xsum16(float x) { auto p = __builtin_amdgcn_permlane16_swap(__float_as_uint(x), __float_as_uint(x), false, false); return __uint_as_float(p[0]) + __uint_as_float(p[1]); }
DEV float xsum32(float x) { auto p = __builtin_amdgcn_permlane32_swap(__float_as_uint(x), __float_as_uint(x), false, false); return __uint_as_float(p[0]) + __uint_as_float(p[1]); }
DEV float xmax16(float x) { auto p = __builtin_amdgcn_permlane16_swap(__float_as_uint(x), __float_as_uint(x), false, false); return fmaxf(__uint_as_float(p[0]), __uint_as_float(p[1])); }
DEV float xmax32(float x) { auto p = __builtin_amdgcn_permlane32_swap(__float_as_uint(x), __float_as_uint(x), false, false); return fmaxf(__uint_as_float(p[0]), __uint_as_float(p[1])); }
DEV float phi(float t) { return (t > 0.f ? t + 1.f : __expf(t)) + 1e-6f; }
#define STAMP(i)

DEV unsigned probe_one(char* p) {
  u64 t0, t1;
  asm volatile("s_memtime %0\n\ts_waitcnt lgkmcnt(0)" : "=s"(t0)::"memory");
  if ((threadIdx.x & 63) == 0) __hip_atomic_store((GAS unsigned*)p, ~0u, RLX_AGENT);
  asm volatile("s_waitcnt vmcnt(0)\n\ts_memtime %0\n\ts_waitcnt lgkmcnt(0)" : "=s"(t1)::"memory");
  return (unsigned)(t1 - t0);
}
DEV void probe_block(char* ws, int blk) {
  char* p = ws + OFF_COMM + (size_t)blk * 2 * MB + 1920;
  unsigned n0 = ~0u, n1 = ~0u;
  for (int r = 0; r < 3; ++r) { n0 = min(n0, probe_one(p)); n1 = min(n1, probe_one(p + 0x2000)); }
  const unsigned nr = n0 <= n1 ? 0 : 2;
  unsigned m[4] = {~0u, ~0u, ~0u, ~0u};
  for (int r = 0; r < 4; ++r)
#pragma unroll
    for (int i = 0; i < 4; ++i) m[i] = min(m[i], probe_one(p + nr * 0x1000 + (i & 1) * 0x800 + (i >> 1) * 0x4000));
  const unsigned k = (m[0] + m[2] > m[1] + m[3] ? 1 : 0) | nr | (m[0] + m[1] > m[2] + m[3] ? 4 : 0), l = threadIdx.x & 63;
  if (l < 8) ((unsigned*)(ws + OFF_PROBE))[blk * 8 + l] = l < 4 ? k : m[0] * (l == 4) + m[1] * (l == 5) + m[2] * (l == 6) + m[3] * (l == 7);
}

DEV void glds(const char* gsrc, unsigned lds_dst) {
  unsigned keep;
  asm volatile("s_mov_b32 %0, m0\n\ts_mov_b32 m0, %2\n\ts_nop 0\n\tglobal_load_lds_dwordx4 %1, off\n\ts_mov_b32 m0, %0" : "=&s"(keep) : "v"(gsrc), "s"(lds_dst) : "memory");
}
template <int N> DEV void vm_bar() { asm volatile("s_waitcnt vmcnt(%0) lgkmcnt(0)\n\ts_barrier" ::"n"(N) : "memory"); }
DEV bf8 lds8(const char* lds, unsigned off) { return *(const LAS bf8*)(lds + off); }
DEV bf8 ones8() { bf8 o; for (int j = 0; j < 8; ++j) o[j] = (__bf16)1.f; return o; }

constexpr int K1_XS = 1040, K1_W = 64 * K1_XS, K1_ST = K1_W + 3 * 24576, K1_LDS = K1_ST + 512;
constexpr bool FIRST = true;
__global__ void __launch_bounds__(320) k1_qkv(const __bf16* x, const __bf16* W, char* ws) {
  extern __shared__ __attribute__((aligned(16))) char lds[];
  const int b = blockIdx.x, tid = threadIdx.x, lane = tid & 63, w = __builtin_amdgcn_readfirstlane(tid >> 6);
  if (w == 4) { if ((b & 7) == 0 && b < 40) probe_block(ws, b >> 3); return; }
  const int hq = (b >> 3) & 7, rg = (b & 7) * 4 + (b >> 6), n = lane & 15, g = lane >> 4;
  const char* wsrc = (const char*)W + (size_t)(w * 128 + (lane >> 3)) * 3072 + hq * 128 + (((lane & 7) ^ ((lane >> 2) & 4)) * 16);
  auto stage = [&](int c) {
#pragma unroll
    for (int i = 0; i < 6; ++i) glds(wsrc + (size_t)(2 * c + (i & 1)) * 8 * 3072 + (i >> 1) * 1024, K1_W + (c % 3) * 24576 + (i >> 1) * 8192 + ((i & 1) * 4 + w) * 1024);
  };
  stage(0);
#pragma unroll
  for (int i = 0; i < 16; ++i) glds((const char*)x + (size_t)(rg * 64 + w * 16 + i) * 1024 + lane * 16, (w * 16 + i) * K1_XS);
  stage(1);
  f4 acc[3][4], cs[3], gram = {0, 0, 0, 0}, sum = {0, 0, 0, 0};
#pragma unroll
  for (int m = 0; m < 3; ++m) { cs[m] = f4{0, 0, 0, 0};
#pragma unroll
    for (int t = 0; t < 4; ++t) acc[m][t] = f4{0, 0, 0, 0}; }
  const bf8 ones = ones8();
  const unsigned xa = n * K1_XS + g * 256, q = n >> 2;
  const unsigned wa = K1_W + (8 * g + q) * 128 + (((2 * w + ((n & 3) >> 1)) ^ ((q >> 1) << 2)) * 16) + (n & 1) * 8;
#pragma unroll
  for (int c = 0; c < 8; ++c) {
    constexpr int F = FIRST ? 2 : 0;
    if (c == 0) vm_bar<6>(); else if (c == 1 || c == 6) vm_bar<6 + F>(); else if (c < 6) vm_bar<6 + 2 * F>(); else vm_bar<0>();
    if (c < 6) stage(c + 2);
    if (FIRST && c < 5) {
      const u4 sent = {~0u, ~0u, ~0u, ~0u}; char* p = ws + OFF_COMM + (size_t)b * 40960 + c * 8192 + tid * 16;
      asm volatile("global_store_dwordx4 %0, %1, off\n\tglobal_store_dwordx4 %2, %1, off\n\ts_nop 1" ::"v"(p), "v"(sent), "v"(p + 4096) : "memory");
    }
#pragma unroll
    for (int k2 = 0; k2 < 2; ++k2) {
      bf8 xf[4], wf[3];
#pragma unroll
      for (int t = 0; t < 4; ++t) xf[t] = lds8(lds, xa + t * 16 * K1_XS + (2 * c + k2) * 16);
#pragma unroll
      for (int m = 0; m < 3; ++m) { const unsigned o = wa + (c % 3) * 24576 + m * 8192 + k2 * 4096; wf[m] = cat(tr_read(lds, o), tr_read(lds, o + 512)); }
      const bf8 xw = lds8(lds, xa + w * 16 * K1_XS + (2 * c + k2) * 16);
#pragma unroll
      for (int t = 0; t < 4; ++t) { acc[0][t] = mfma32(wf[0], xf[t], acc[0][t]); acc[1][t] = mfma32(xf[t], wf[1], acc[1][t]); acc[2][t] = mfma32(xf[t], wf[2], acc[2][t]); }
      cs[0] = mfma32(wf[0], ones, cs[0]); cs[1] = mfma32(ones, wf[1], cs[1]); cs[2] = mfma32(ones, wf[2], cs[2]);
      gram = mfma32(xw, xw, gram); sum = mfma32(ones, xw, sum);
    }
  }
  { float d = 0.f;
#pragma unroll
    for (int r = 0; r < 4; ++r) d = (4 * g + r == n) ? gram[r] : d;
    d = xsum32(xsum16(d)) * (1.f / 512);
    const float mu = sum[0] * (1.f / 512), rs = rsqrtf(fmaxf(d - mu * mu, 0.f) + 1e-5f);
    if (g == 0) { *(LAS float*)(lds + K1_ST + (w * 16 + n) * 4) = rs; *(LAS float*)(lds + K1_ST + 256 + (w * 16 + n) * 4) = -mu * rs; } }
  __syncthreads();
  if (FIRST && tid < 32) ((u4*)(ws + OFF_FLAG))[b * 32 + tid] = u4{0, 0, 0, 0};
  const int h = hq * 4 + w;
  f4 kv = {0, 0, 0, 0}, z = {0, 0, 0, 0};
#pragma unroll
  for (int tp = 0; tp < 2; ++tp) {
    bf4 pk[2], vv[2];
#pragma unroll
    for (int u = 0; u < 2; ++u) { const int t = 2 * tp + u;
      const float rs = *(const LAS float*)(lds + K1_ST + (t * 16 + n) * 4), nm = *(const LAS float*)(lds + K1_ST + 256 + (t * 16 + n) * 4);
      const f4 rs4 = *(const LAS f4*)(lds + K1_ST + (t * 16 + 4 * g) * 4), nm4 = *(const LAS f4*)(lds + K1_ST + 256 + (t * 16 + 4 * g) * 4);
      f4 p, k4;
#pragma unroll
      for (int r = 0; r < 4; ++r) { p[r] = phi((float)(__bf16)(acc[0][t][r] * rs + nm * cs[0][r])); k4[r] = phi((float)(__bf16)(acc[1][t][r] * rs4[r] + nm4[r] * cs[1][0])); }
      *(bf4*)(ws + OFF_PQ + (size_t)((rg * 4 + t) * 16 + (h >> 1)) * 1024 + lane * 16 + (h & 1) * 8) = cvt4(p);
      pk[u] = cvt4(k4); vv[u] = cvt4(acc[2][t] * rs4 + nm4 * cs[2][0]); }
    kv = mfma32(cat(pk[0], pk[1]), cat(vv[0], vv[1]), kv);
    z = mfma32(cat(pk[0], pk[1]), ones, z);
  }
  float* o = (float*)(ws + OFF_KVP) + (h * 32 + rg) * 272;
  *(f4*)(o + lane * 4) = kv;
  if (n == 0) *(f4*)(o + 256 + 4 * g) = z;
}
DEV void st8(char* p, bf4 v) { __hip_atomic_store((GAS u64*)p, __builtin_bit_cast(u64, v), RLX_AGENT); }
DEV void fold(const __bf16* wo, char* ws, char* lds, int h, int ch, unsigned* flag) {
  const int tid = threadIdx.x, lane = tid & 63, w = tid >> 6, n = lane & 15, g = lane >> 4;
  const __bf16* wr = wo + (size_t)(h * 16 + (tid >> 4)) * 512 + ch * 256 + (tid & 15) * 8;
  const u4 wv0 = *(const u4*)wr, wv1 = *(const u4*)(wr + 128);
  const float* P = (const float*)(ws + OFF_KVP) + (h * 32 + w * 8) * 272;
  f4 s = {0, 0, 0, 0}, sz = {0, 0, 0, 0};
#pragma unroll
  for (int p = 0; p < 8; ++p) { s += *(const f4*)(P + p * 272 + lane * 4); sz += *(const f4*)(P + p * 272 + 256 + (lane & 3) * 4); }
  *(LAS u4*)(lds + 4352 + (tid >> 4) * 528 + (tid & 15) * 16) = wv0; *(LAS u4*)(lds + 4352 + 256 + (tid >> 4) * 528 + (tid & 15) * 16) = wv1;
  *(LAS f4*)(lds + w * 1088 + lane * 16) = s; if (lane < 4) *(LAS f4*)(lds + w * 1088 + 1024 + lane * 16) = sz;
  __syncthreads();
  f4 a = {0, 0, 0, 0};
#pragma unroll
  for (int k = 0; k < 4; ++k)
#pragma unroll
    for (int r = 0; r < 4; ++r) a[r] += *(const LAS float*)(lds + k * 1088 + (((n >> 2) * 16 + 4 * g + r) * 4 + (n & 3)) * 4);
  const bf8 af = cat(cvt4(a), cvt4(f4{0, 0, 0, 0}));
#pragma unroll
  for (int t = 0; t < 4; ++t) {
    const bf4 wf = tr_read(lds, 4352 + (4 * g + (n >> 2)) * 528 + (w * 64 + (t >> 1) * 32 + (n & 3) * 8 + (t & 1) * 4) * 2);
    st8(ws + OFF_MF + (size_t)(((h >> 1) * 32 + ch * 16 + w * 4 + t) * 64 + lane) * 16 + (h & 1) * 8, cvt4(mfma32(af, cat(wf, wf), f4{0, 0, 0, 0})));
  }
  if (ch == 0 && w == 0) {
    f4 z = {0, 0, 0, 0};
#pragma unroll
    for (int k = 0; k < 4; ++k) z += *(const LAS f4*)(lds + k * 1088 + 1024 + g * 16);
    st8(ws + OFF_Z + (size_t)((h >> 1) * 64 + lane) * 16 + (h & 1) * 8, cvt4((n & 1) == (h & 1) ? z : f4{0, 0, 0, 0}));
  }
  asm volatile("s_waitcnt vmcnt(0)" ::: "memory");
  __syncthreads();
  if (tid == 0) __hip_atomic_store((GAS unsigned*)flag, 1u, RLX_AGENT);
}

struct KB { const __bf16 *x0, *wqkv, *wo, *mem0, *Wq, *Wk, *Wv, *Wo; char* ws; __bf16* out; int q1, per; };
DEV unsigned* flagp(char* ws, int layer, int kind, int h) { return (unsigned*)(ws + OFF_FLAG + ((layer * 16 + kind) * 32 + h) * 128); }
DEV u64 comm(const char* ws, int blk) {
  const unsigned k = *(const unsigned*)(ws + OFF_PROBE + blk * 32) & 7;
  return (u64)(ws + OFF_COMM + (size_t)blk * 2 * MB) + (k & 1) * 0x800 + (k >> 1) * 0x2000;
}
constexpr int KB_LDS = 160 * 1024;
__global__ void __launch_bounds__(256, 1) kb_block(KB a) {
  extern __shared__ __attribute__((aligned(16))) char lds[];
  const int b = blockIdx.x;
  const unsigned w = __builtin_amdgcn_readfirstlane(threadIdx.x >> 6);
  const int x = b & 7, t = b >> 3;
  char* ws = a.ws; u64* P = (u64*)(lds + LDS_PAR);
  STAMP(0);
  if (x >= 4) {
    const int h = (x - 4) * 8 + (t & 7);
    if (t < 8) {
      if (threadIdx.x == 0) { P[0] = (u64)(a.Wk + h * 128); P[1] = (u64)(a.Wv + h * 128); P[2] = (u64)(ws + OFF_X1); P[3] = (u64)(ws + OFF_STAT);
        P[4] = (u64)(ws + OFF_KV + (size_t)h * 64 * 16384); P[5] = (u64)flagp(ws, 0, 0, h); P[6] = (u64)flagp(ws, 0, 3, 0); P[7] = (u64)(ws + OFF_FST + h * 16384);
        P[8] = (u64)(ws + OFF_X2); P[9] = (u64)(a.wqkv + h * 16); P[10] = (u64)(a.wo + h * 16 * 512); P[11] = (u64)(ws + OFF_PQ + (h >> 1) * 1024 + (h & 1) * 8);
        P[12] = (u64)(ws + OFF_MF + (h >> 1) * 32768 + (h & 1) * 8); P[13] = (u64)(ws + OFF_Z + (h >> 1) * 1024 + (h & 1) * 8); P[14] = (u64)flagp(ws, 0, 6, 0); P[16] = (u64)flagp(ws, 1, 4, h); P[17] = h; }
      __syncthreads();
      asm volatile(KV_ASM ::[w] "s"(w) : POOL_CLOBBERS);
    } else if (t < 24) {
      fold(a.wo, ws, lds, h, t >> 4, flagp(ws, 0, 4 + (t >> 4), h));
      STAMP(1);
      if (threadIdx.x == 0) { P[0] = (u64)(a.Wq + h * 128); P[1] = (u64)(a.Wk + h * 128); P[2] = (u64)(a.Wv + h * 128); P[3] = (u64)(ws + OFF_X1); P[4] = (u64)(ws + OFF_FST + h * 16384);
        P[5] = (u64)(ws + OFF_KV + (size_t)h * 64 * 16384); P[6] = (u64)flagp(ws, 0, 0, h); P[7] = (u64)flagp(ws, 0, 1 + (t >> 4), h); P[8] = (u64)(ws + OFF_O + h * 8192);
        P[9] = (u64)(comm(ws, 0) ^ 0x800) + 1024; P[10] = comm(ws, 1) ^ 0x800; P[11] = comm(ws, 2) ^ 0x800; P[12] = (u64)a.mem0; P[13] = t >> 4;
        P[16] = comm(ws, 3) ^ 0x800; P[17] = comm(ws, 4) ^ 0x800; P[14] = (u64)(ws + OFF_STAT); P[18] = (u64)flagp(ws, 0, 3, 0); }
      __syncthreads();
      asm volatile(QA_ASM ::[w] "s"(w) : POOL_CLOBBERS);
    } else {
      const int cgp = (x & 1) * 8 + t - 24, rg = (x - 4) >> 1;
      if (threadIdx.x == 0) { P[0] = (u64)(a.Wo + cgp * 32); P[1] = (u64)(ws + OFF_O); P[2] = (u64)flagp(ws, 0, 1, 0);
        P[3] = (u64)(ws + OFF_X1 + cgp * 64); P[4] = (u64)(ws + OFF_X2 + cgp * 64); P[5] = (u64)rg | ((u64)cgp << 32);
        P[6] = (u64)a.x0; P[7] = (u64)(ws + OFF_PQ); P[8] = (u64)(ws + OFF_MF); P[9] = (u64)(ws + OFF_Z);
        P[10] = (u64)flagp(ws, 0, 4, 0); P[11] = (u64)flagp(ws, 0, 3, 0); P[12] = (u64)(ws + OFF_STAT);
        P[13] = (u64)(ws + OFF_X2); P[14] = (u64)(a.out + cgp * 32); P[16] = (u64)flagp(ws, 0, 6, (rg * 16 + cgp) * 2); }
      __syncthreads();
      asm volatile(O_ASM ::[w] "s"(w) : POOL_CLOBBERS);
    }
    return;
  }
  if (threadIdx.x < 2) {
    const int role = x * 32 + t, h = role >> 2, layer = threadIdx.x;
    P += layer * 16;
    P[0] = (u64)(a.Wq + h * 128); P[15] = (u64)(a.Wk + h * 128); P[1] = (u64)(a.Wv + h * 128); P[2] = (u64)(a.Wo + (size_t)h * 128 * 512 + (role & 3) * 128);
    P[3] = (u64)(ws + OFF_KV + (size_t)h * 64 * 16384); P[4] = comm(ws, 0); P[5] = (u64)a.mem0;
    P[6] = (u64)(ws + OFF_SCR + role * 512); P[7] = (u64)(ws + 124 * MB + (layer * 128 + role) * 128);
    int* Q = (int*)(P + 8); Q[0] = h; Q[1] = role & 3; Q[2] = (role & 1) ? -1 : role >> 1; Q[3] = a.q1; Q[4] = a.per; Q[5] = 63;
    P[11] = comm(ws, 1 + 2 * layer); P[12] = comm(ws, 2 + 2 * layer);
    P[13] = (u64)flagp(ws, layer, 0, h); P[14] = layer * 63;
  }
  __syncthreads();
  asm volatile(CHAIN_ASM ::[w] "s"(w) : CHAIN_CLOBBERS);
}

extern "C" void kernel_launch(void* const* d_in, const int* in_sizes, int n_in, void* d_out, int out_size, void* d_ws, size_t ws_size, hipStream_t stream) {
  char* ws = (char*)(((size_t)d_ws + 2 * MB - 1) & ~(2 * MB - 1));
  (void)hipFuncSetAttribute((const void*)k1_qkv, hipFuncAttributeMaxDynamicSharedMemorySize, K1_LDS);
  (void)hipFuncSetAttribute((const void*)kb_block, hipFuncAttributeMaxDynamicSharedMemorySize, KB_LDS);
  KB a; a.x0 = (const __bf16*)d_in[0]; a.wqkv = (const __bf16*)d_in[1]; a.wo = (const __bf16*)d_in[2]; a.mem0 = (const __bf16*)d_in[4];
  a.Wq = (const __bf16*)d_in[5]; a.Wk = (const __bf16*)d_in[6]; a.Wv = (const __bf16*)d_in[7]; a.Wo = (const __bf16*)d_in[8];
  a.ws = ws; a.out = (__bf16*)d_out; a.q1 = Q1_CYC; a.per = PERIOD;
  k1_qkv<<<256, 320, K1_LDS, stream>>>(a.x0, a.wqkv, ws);
  kb_block<<<256, 256, KB_LDS, stream>>>(a);
}
```
